```python
import torch

def _softmax(x, axis=-1):
    return torch.softmax(x, dim=axis)
def _gelu(x):
    return torch.nn.functional.gelu(x, approximate="tanh")
def _silu(x):
    return torch.nn.functional.silu(x)
def _sigmoid(x):
    return torch.sigmoid(x)
def _layernorm(x, axis=-1, eps=1e-5):
    m = x.mean(dim=axis, keepdim=True)
    v = x.var(dim=axis, keepdim=True, unbiased=False)
    return (x - m) / torch.sqrt(v + eps)
def _scatter_add(tgt, idx, src, axis=0):
    return tgt.clone().index_add_(axis, idx, src)
def _segment_sum(src, seg, n):
    out = torch.zeros((n,) + src.shape[1:], dtype=src.dtype)
    return out.index_add_(0, seg, src)
def _pad(x, pw):
    flat = tuple(v for pair in reversed(pw) for v in pair)
    return torch.nn.functional.pad(x, flat)

kernel_name = 'sequential_preLN__sliding_block__mixer_token_mlp__prompt'

def setup_inputs():

    _w = (lambda a: (lambda t: t.to(torch.bfloat16) if t.is_floating_point() else t)(torch.as_tensor(a)))
    def _A_setup():
        B, S, D = 1, 2048, 512
        return {
            "x": _w((lambda *s: torch.randn(*s, dtype=torch.bfloat16) * 0.05)(B, S, D)),
            "wqkv": _w((lambda *s: torch.randn(*s, dtype=torch.bfloat16) * 0.05)(D, 3 * D)),
            "wo": _w((lambda *s: torch.randn(*s, dtype=torch.bfloat16) * 0.05)(D, D)),
        }
    _A_in = _A_setup()

    def _B_setup():
        B, S, D, F = 1, 2048, 512, 1376
        return {
            "x": _w((lambda *s: torch.randn(*s, dtype=torch.bfloat16) * 0.05)(B, S, D)),
            "w1": _w((lambda *s: torch.randn(*s, dtype=torch.bfloat16) * 0.8)(S, F) * (S ** -0.5)),
            "b1": _w((lambda *s: torch.randn(*s, dtype=torch.bfloat16) * 0.8)(F) * 0.02),
            "w2": _w((lambda *s: torch.randn(*s, dtype=torch.bfloat16) * 0.8)(F, S) * (F ** -0.5)),
            "b2": _w((lambda *s: torch.randn(*s, dtype=torch.bfloat16) * 0.8)(S) * 0.02),
        }
    _B_in = _B_setup()

    return {
        "A_x": _A_in["x"],
        "A_wqkv": _A_in["wqkv"],
        "A_wo": _A_in["wo"],
        "B_x": _B_in["x"],
        "B_w1": _B_in["w1"],
        "B_b1": _B_in["b1"],
        "B_w2": _B_in["w2"],
        "B_b2": _B_in["b2"],
    }

def reference(A_x, A_wqkv, A_wo, B_x, B_w1, B_b1, B_w2, B_b2):
    def _A_ref(x, wqkv, wo):
        B, S, D = x.shape
        H = 32; Dh = D // H; W = 256
        qkv = (lambda x, *d: x.permute(*d))((x @ wqkv).reshape(B, S, 3, H, Dh), 2, 0, 3, 1, 4)
        q, k, v = qkv[0], qkv[1], qkv[2]
        scores = torch.einsum("bhqd,bhkd->bhqk", q, k) * (Dh ** -0.5)
        idx = torch.arange(S)
        mask = (idx[:, None] - idx[None, :] >= 0) & (idx[:, None] - idx[None, :] < W)
        scores = torch.where(mask[None, None], scores, -1e9)
        p = _softmax(scores, axis=-1)
        o = torch.einsum("bhqk,bhkd->bhqd", p, v)
        return (lambda x, *d: x.permute(*d))(o, 0, 2, 1, 3).reshape(B, S, D) @ wo

    def _B_ref(x, w1, b1, w2, b2):
        B, S, D = x.shape
        h = _layernorm(x, axis=-1)
        u = torch.einsum("bsd,sf->bdf", h, w1) + b1
        u = _gelu(u)
        y = torch.einsum("bdf,fs->bds", u, w2) + b2
        return (lambda x, *d: x.permute(*d))(y, 0, 2, 1)

    x = A_x
    h = x + _A_ref(_layernorm(x, axis=-1), A_wqkv, A_wo)
    h2 = h + _B_ref(_layernorm(h, axis=-1), B_w1, B_b1, B_w2, B_b2)
    return h2
```

```cpp
#include <hip/hip_runtime.h>
#include <hip/hip_bf16.h>

#define LAS __attribute__((address_space(3)))
#define DI __device__ __forceinline__
typedef unsigned u32x4 __attribute__((ext_vector_type(4)));
typedef unsigned u32x2 __attribute__((ext_vector_type(2)));
typedef float f32x4 __attribute__((ext_vector_type(4)));
typedef float f32x2 __attribute__((ext_vector_type(2)));
typedef float f32x16 __attribute__((ext_vector_type(16)));
typedef short s16x4 __attribute__((ext_vector_type(4)));
typedef short s16x8 __attribute__((ext_vector_type(8)));
typedef __bf16 bf16x8 __attribute__((ext_vector_type(8)));
typedef __bf16 bf16x2 __attribute__((ext_vector_type(2)));

constexpr int S = 2048, D = 512, F = 1376;
constexpr size_t MB = 1 << 20;
constexpr size_t QF_OFF = 1 * MB, KF_OFF = 3 * MB, VF_OFF = 5 * MB, OF_OFF = 7 * MB, HN_OFF = 9 * MB, UF_OFF = 11 * MB, H_OFF = 13 * MB, ST_OFF = 15 * MB;

DI f32x16 mfma32(u32x4 a, u32x4 b, f32x16 c) { return __builtin_amdgcn_mfma_f32_32x32x16_bf16(__builtin_bit_cast(bf16x8, a), __builtin_bit_cast(bf16x8, b), c, 0, 0, 0); }
DI f32x4 mfma16(u32x4 a, u32x4 b, f32x4 c) { return __builtin_amdgcn_mfma_f32_16x16x32_bf16(__builtin_bit_cast(bf16x8, a), __builtin_bit_cast(bf16x8, b), c, 0, 0, 0); }
DI unsigned cvtpk(float lo, float hi) { f32x2 v = {lo, hi}; return __builtin_bit_cast(unsigned, __builtin_convertvector(v, bf16x2)); }
DI float dot2(unsigned a, unsigned b, float c) { return __builtin_amdgcn_fdot2_f32_bf16(__builtin_bit_cast(bf16x2, a), __builtin_bit_cast(bf16x2, b), c, false); }
DI u32x2 trread(const char* lds) { return __builtin_bit_cast(u32x2, __builtin_amdgcn_ds_read_tr16_b64_v4i16((LAS s16x4*)lds)); }
DI u32x4 cat(u32x2 a, u32x2 b) { return u32x4{a.x, a.y, b.x, b.y}; }
DI float bflo(unsigned u) { return __builtin_bit_cast(float, u << 16); }
DI float bfhi(unsigned u) { return __builtin_bit_cast(float, u & 0xffff0000u); }
DI float swap32_add(float v) { float a = v, b = v; asm volatile("s_nop 1\n\tv_permlane32_swap_b32 %0, %1" : "+v"(a), "+v"(b)); return a + b; }
DI float swap32_max(float v) { float a = v, b = v; asm volatile("s_nop 1\n\tv_permlane32_swap_b32 %0, %1" : "+v"(a), "+v"(b)); return fmaxf(a, b); }
DI __amdgpu_buffer_rsrc_t mkrs(const void* p, unsigned bytes) { return __builtin_amdgcn_make_buffer_rsrc((void*)p, 0, bytes, 0x00020000); }
DI f32x16 zero16() { f32x16 z; for (int i = 0; i < 16; ++i) z[i] = 0.f; return z; }

__global__ __launch_bounds__(256) void k_qkv(const char* x, const char* wqkv, char* ws) {
  extern __shared__ __attribute__((aligned(16))) char sm[];
  const int b = blockIdx.x, xcd = b & 7, r = b >> 3;
  const int rt = (xcd >> 1) * 4 + (r & 3), ct = (xcd & 1) * 8 + (r >> 2);
  const int w = __builtin_amdgcn_readfirstlane(threadIdx.x >> 6), lane = threadIdx.x & 63;
  auto rw = mkrs(wqkv, D * 1536 * 2);
  for (int g = 0; g < 8; ++g) {
    const int rg = g * 4 + w;
#pragma unroll
    for (int i = 0; i < 3; ++i) {
      const int n = i * 64 + lane, row = n / 12, c16 = n % 12;
      __builtin_amdgcn_raw_ptr_buffer_load_lds(rw, (LAS void*)(sm + rg * 3072 + i * 1024), 16, row * 3072 + c16 * 16 + ct * 192, rg * 16 * 3072, 0, 0);
    }
  }
  auto rx = mkrs(x, S * D * 2);
  const int row0 = rt * 128 + w * 32;
  u32x4 L[16][2];
#pragma unroll
  for (int kk = 0; kk < 16; ++kk)
#pragma unroll
    for (int q = 0; q < 2; ++q) L[kk][q] = __builtin_amdgcn_raw_buffer_load_b128(rx, (row0 + q * 16 + (lane & 15)) * 1024 + (lane >> 4) * 16, kk * 64, 0);
  asm volatile("s_waitcnt vmcnt(0)" ::: "memory");
  __syncthreads();
  f32x16 acc[3]; for (int i = 0; i < 3; ++i) acc[i] = zero16();
  float sum = 0.f, sq = 0.f, cs[3] = {0.f, 0.f, 0.f};
  const int h = lane >> 5, blk = (lane >> 4) & 1, q4 = (lane & 15) >> 2, p4 = lane & 3;
  const char* wb = sm + (16 * h + q4) * 192 + blk * 32 + p4 * 8;
  const int T0 = ct * 3;
#pragma unroll
  for (int kk = 0; kk < 16; ++kk) {
    u32x4 Fr[2];
#pragma unroll
    for (int e = 0; e < 4; ++e) { auto s = __builtin_amdgcn_permlane16_swap(L[kk][0][e], L[kk][1][e], false, false); Fr[0][e] = s[0]; Fr[1][e] = s[1]; }
#pragma unroll
    for (int f = 0; f < 2; ++f) {
#pragma unroll
      for (int e = 0; e < 4; ++e) { sum = dot2(Fr[f][e], 0x3f803f80u, sum); sq = dot2(Fr[f][e], Fr[f][e], sq); }
#pragma unroll
      for (int i = 0; i < 3; ++i) {
        const char* a = wb + (32 * kk + 8 * f) * 192 + i * 64;
        u32x4 wf = cat(trread(a), trread(a + 4 * 192));
        if ((kk & 3) == w) { for (int e = 0; e < 4; ++e) cs[i] = dot2(wf[e], 0x3f803f80u, cs[i]); }
        if (T0 + i < 32) acc[i] = mfma32(wf, Fr[f], acc[i]); else acc[i] = mfma32(Fr[f], wf, acc[i]);
      }
    }
  }
  sum = swap32_add(sum); sq = swap32_add(sq);
  const float mu = sum * (1.f / D);
  const float var = fmaxf(sq * (1.f / D) - mu * mu, 0.f);
  const float rs = rsqrtf(var + 1e-5f);
  float* csl = (float*)(sm + 98304);
  float* stl = (float*)(sm + 98304 + 2048);
#pragma unroll
  for (int i = 0; i < 3; ++i) { float c = swap32_add(cs[i]); if (h == 0) csl[w * 96 + i * 32 + lane] = c; }
  if (h == 0) { stl[w * 64 + lane] = rs; stl[w * 64 + 32 + lane] = rs * mu; }
  __syncthreads();
  const int tb = rt * 4 + w;
#pragma unroll
  for (int i = 0; i < 3; ++i) {
    const int T = T0 + i, ty = T >> 4, hd = (T & 15) * 2;
    float o[16];
    if (ty < 2) {
      const float sc = ty == 0 ? 0.25f * 1.4426950408889634f : 1.f;
#pragma unroll
      for (int rg = 0; rg < 16; ++rg) {
        const int c = i * 32 + (rg & 3) + 8 * (rg >> 2) + 4 * h;
        const float cc = csl[c] + csl[96 + c] + csl[192 + c] + csl[288 + c];
        o[rg] = (acc[i][rg] - mu * cc) * (rs * sc);
      }
      char* dst = ws + (ty == 0 ? QF_OFF : KF_OFF);
#pragma unroll
      for (int hh = 0; hh < 2; ++hh) {
        u32x4 v = {cvtpk(o[8 * hh], o[8 * hh + 1]), cvtpk(o[8 * hh + 2], o[8 * hh + 3]), cvtpk(o[8 * hh + 4], o[8 * hh + 5]), cvtpk(o[8 * hh + 6], o[8 * hh + 7])};
        *(u32x4*)(dst + ((size_t)(hd + hh) * 64 + tb) * 1024 + lane * 16) = v;
      }
    } else {
      const int c = i * 32 + (lane & 31);
      const float cc = csl[c] + csl[96 + c] + csl[192 + c] + csl[288 + c];
#pragma unroll
      for (int rg = 0; rg < 16; ++rg) {
        const int t = (rg & 3) + 8 * (rg >> 2) + 4 * h;
        o[rg] = acc[i][rg] * stl[w * 64 + t] - stl[w * 64 + 32 + t] * cc;
      }
#pragma unroll
      for (int s = 0; s < 2; ++s) {
        u32x4 v = {cvtpk(o[8 * s], o[8 * s + 1]), cvtpk(o[8 * s + 2], o[8 * s + 3]), cvtpk(o[8 * s + 4], o[8 * s + 5]), cvtpk(o[8 * s + 6], o[8 * s + 7])};
        *(u32x4*)(ws + VF_OFF + ((size_t)(hd + blk) * 64 + tb) * 1024 + s * 512 + h * 256 + (lane & 15) * 16) = v;
      }
    }
  }
}

__global__ __launch_bounds__(256) void k_attn(char* ws) {
  __shared__ __attribute__((aligned(16))) char sm[32768];
  const int b = blockIdx.x, xcd = b & 7, r = b >> 3;
  const int head = xcd * 4 + (r & 3), qr = r >> 2;
  const int w = __builtin_amdgcn_readfirstlane(threadIdx.x >> 6), lane = threadIdx.x & 63;
  const int h = lane >> 5;
  for (int kl = w; kl < 16; kl += 4) {
    const int kb = qr * 8 - 8 + kl;
    if (kb >= 0) {
      *(u32x4*)(sm + kl * 1024 + lane * 16) = *(const u32x4*)(ws + KF_OFF + ((size_t)head * 64 + kb) * 1024 + lane * 16);
      *(u32x4*)(sm + 16384 + kl * 1024 + lane * 16) = *(const u32x4*)(ws + VF_OFF + ((size_t)head * 64 + kb) * 1024 + lane * 16);
    }
  }
  __syncthreads();
  const unsigned ones = ((lane & 31) == 16 || (lane & 31) == 20) ? 0x3f803f80u : 0u;
  const bool vl = (lane & 31) < 16;
  for (int qq = 0; qq < 2; ++qq) {
    const int ql = 2 * w + qq, qb = qr * 8 + ql;
    const u32x4 qf = *(const u32x4*)(ws + QF_OFF + ((size_t)head * 64 + qb) * 1024 + lane * 16);
    const int t0 = qb >= 8 ? 0 : 8 - qb;
    float m = 0.f;
    f32x16 o;
    for (int pass = 0; pass < 2; ++pass) {
      if (pass == 1) {
        m = -3.0e38f;
        for (int t = t0; t <= 8; ++t) {
          const u32x4 kf = *(const u32x4*)(sm + (ql + t) * 1024 + lane * 16);
          f32x16 s = mfma32(kf, qf, zero16());
#pragma unroll
          for (int rg = 0; rg < 16; ++rg) {
            const int kl = (rg & 3) + 8 * (rg >> 2) + 4 * h, q = lane & 31;
            const bool ok = t == 0 ? kl > q : (t == 8 ? kl <= q : true);
            m = fmaxf(m, ok ? s[rg] : -3.0e38f);
          }
        }
        m = swap32_max(m);
      }
      o = zero16();
      for (int t = t0; t <= 8; ++t) {
        const u32x4 kf = *(const u32x4*)(sm + (ql + t) * 1024 + lane * 16);
        f32x16 s = mfma32(kf, qf, zero16());
        float p[16];
#pragma unroll
        for (int rg = 0; rg < 16; ++rg) {
          const int kl = (rg & 3) + 8 * (rg >> 2) + 4 * h, q = lane & 31;
          const bool ok = t == 0 ? kl > q : (t == 8 ? kl <= q : true);
          p[rg] = ok ? __builtin_amdgcn_exp2f(s[rg] - m) : 0.f;
        }
#pragma unroll
        for (int st = 0; st < 2; ++st) {
          u32x4 pf = {cvtpk(p[8 * st], p[8 * st + 1]), cvtpk(p[8 * st + 2], p[8 * st + 3]), cvtpk(p[8 * st + 4], p[8 * st + 5]), cvtpk(p[8 * st + 6], p[8 * st + 7])};
          u32x4 vf = {ones, ones, ones, ones};
          if (vl) vf = *(const u32x4*)(sm + 16384 + (ql + t) * 1024 + st * 512 + h * 256 + (lane & 15) * 16);
          o = mfma32(vf, pf, o);
        }
      }
      const float l = o[8];
      bool ok = l >= 0x1p-60f && l <= 0x1p60f;
      if (pass == 1 || __all(ok)) break;
    }
    const float il = 1.f / o[8];
    u32x4 v = {cvtpk(o[0] * il, o[1] * il), cvtpk(o[2] * il, o[3] * il), cvtpk(o[4] * il, o[5] * il), cvtpk(o[6] * il, o[7] * il)};
    *(u32x4*)(ws + OF_OFF + ((size_t)qb * 32 + head) * 1024 + lane * 16) = v;
  }
}

__global__ __launch_bounds__(256) void k_proj(const char* x, const char* wo, char* ws) {
  extern __shared__ __attribute__((aligned(16))) char sm[];
  const int b = blockIdx.x, xcd = b & 7, r = b >> 3;
  const int tt = (xcd >> 1) * 8 + (r & 7), cc = (xcd & 1) * 4 + (r >> 3);
  const int w = __builtin_amdgcn_readfirstlane(threadIdx.x >> 6), lane = threadIdx.x & 63;
  const int h = lane >> 5, blk = (lane >> 4) & 1, q4 = (lane & 15) >> 2, p4 = lane & 3;
  auto rw = mkrs(wo, D * D * 2);
  {
    const int rr = lane >> 3, c16 = lane & 7;
    const int src = rr * 1024 + ((c16 ^ (((rr >> 1) & 1) << 2)) * 16) + cc * 128;
    for (int i = w; i < 64; i += 4) __builtin_amdgcn_raw_ptr_buffer_load_lds(rw, (LAS void*)(sm + i * 1024), 16, src, i * 8 * 1024, 0, 0);
  }
  const int t2 = w & 1, kh = w >> 1;
  const int tb = tt * 2 + t2;
  u32x4 of[16];
#pragma unroll
  for (int j = 0; j < 16; ++j) of[j] = *(const u32x4*)(ws + OF_OFF + ((size_t)tb * 32 + kh * 16 + j) * 1024 + lane * 16);
  const int tok = tb * 32 + (lane & 31);
  u32x2 xr[4];
#pragma unroll
  for (int g = 0; g < 4; ++g) xr[g] = *(const u32x2*)(x + (size_t)tok * 1024 + (cc * 64 + kh * 32 + 8 * g + 4 * h) * 2);
  asm volatile("s_waitcnt vmcnt(0)" ::: "memory");
  __syncthreads();
  f32x16 acc[2] = {zero16(), zero16()};
#pragma unroll
  for (int j = 0; j < 16; ++j)
#pragma unroll
    for (int i = 0; i < 2; ++i) {
      const int k0 = (kh * 16 + j) * 16 + 4 * h + q4;
      const int sw = ((k0 >> 1) & 1);
      const char* a = sm + k0 * 128 + ((i ^ sw) * 64) + blk * 32 + p4 * 8;
      u32x4 wf = cat(trread(a), trread(a + 8 * 128));
      acc[i] = mfma32(wf, of[j], acc[i]);
    }
  f32x4* ex = (f32x4*)(sm + 65536);
  {
    const f32x16 snd = kh == 0 ? acc[1] : acc[0];
#pragma unroll
    for (int g = 0; g < 4; ++g) ex[(w * 4 + g) * 64 + lane] = f32x4{snd[4 * g], snd[4 * g + 1], snd[4 * g + 2], snd[4 * g + 3]};
  }
  __syncthreads();
  const f32x16 own = kh == 0 ? acc[0] : acc[1];
  float sum = 0.f, sq = 0.f;
  unsigned hp[8];
#pragma unroll
  for (int g = 0; g < 4; ++g) {
    const f32x4 o = ex[((w ^ 2) * 4 + g) * 64 + lane];
    const float v0 = own[4 * g] + o[0] + bflo(xr[g].x), v1 = own[4 * g + 1] + o[1] + bfhi(xr[g].x), v2 = own[4 * g + 2] + o[2] + bflo(xr[g].y), v3 = own[4 * g + 3] + o[3] + bfhi(xr[g].y);
    hp[2 * g] = cvtpk(v0, v1); hp[2 * g + 1] = cvtpk(v2, v3);
    sum = dot2(hp[2 * g], 0x3f803f80u, sum); sum = dot2(hp[2 * g + 1], 0x3f803f80u, sum);
    sq = dot2(hp[2 * g], hp[2 * g], sq); sq = dot2(hp[2 * g + 1], hp[2 * g + 1], sq);
  }
  sum = swap32_add(sum); sq = swap32_add(sq);
  *(u32x4*)(ws + H_OFF + ((size_t)b * 4 + w) * 2048 + lane * 16) = u32x4{hp[0], hp[1], hp[2], hp[3]};
  *(u32x4*)(ws + H_OFF + ((size_t)b * 4 + w) * 2048 + 1024 + lane * 16) = u32x4{hp[4], hp[5], hp[6], hp[7]};
  if (h == 0) *(f32x2*)(ws + ST_OFF + ((size_t)tok * 16 + cc * 2 + kh) * 8) = f32x2{sum, sq};
}

__global__ __launch_bounds__(256) void k_norm(char* ws) {
  __shared__ __attribute__((aligned(16))) char sm[4 * 2048];
  const int b = blockIdx.x, xcd = b & 7, r = b >> 3;
  const int tt = (xcd >> 1) * 8 + (r & 7), cc = (xcd & 1) * 4 + (r >> 3);
  const int w = __builtin_amdgcn_readfirstlane(threadIdx.x >> 6), lane = threadIdx.x & 63;
  const int h = lane >> 5;
  const int t2 = w & 1, kh = w >> 1, tb = tt * 2 + t2, tok = tb * 32 + (lane & 31);
  const u32x4 h0 = *(const u32x4*)(ws + H_OFF + ((size_t)b * 4 + w) * 2048 + lane * 16), h1 = *(const u32x4*)(ws + H_OFF + ((size_t)b * 4 + w) * 2048 + 1024 + lane * 16);
  const unsigned hp[8] = {h0.x, h0.y, h0.z, h0.w, h1.x, h1.y, h1.z, h1.w};
  float sum = 0.f, sq = 0.f;
  for (int j = 0; j < 16; ++j) { const f32x2 s = *(const f32x2*)(ws + ST_OFF + ((size_t)tok * 16 + j) * 8); sum += s.x; sq += s.y; }
  const float mu = sum * (1.f / D), var = fmaxf(sq * (1.f / D) - mu * mu, 0.f);
  const float r1 = rsqrtf(var + 1e-5f);
  const float v2 = var * r1 * r1;
  const float rs = r1 * rsqrtf(v2 + 1e-5f);
  char* im = sm + w * 2048;
#pragma unroll
  for (int g = 0; g < 4; ++g) {
    const unsigned a = cvtpk((bflo(hp[2 * g]) - mu) * rs, (bfhi(hp[2 * g]) - mu) * rs), c = cvtpk((bflo(hp[2 * g + 1]) - mu) * rs, (bfhi(hp[2 * g + 1]) - mu) * rs);
    *(u32x2*)(im + (g >> 1) * 1024 + (lane & 31) * 32 + ((g & 1) * 8 + 4 * h) * 2) = u32x2{a, c};
  }
  __syncthreads();
  const int g4 = lane >> 4, q4 = (lane & 15) >> 2, p4 = lane & 3;
#pragma unroll
  for (int cb = 0; cb < 2; ++cb) {
    const char* a = im + cb * 1024 + (8 * g4 + q4) * 32 + p4 * 8;
    u32x4 v = cat(trread(a), trread(a + 4 * 32));
    *(u32x4*)(ws + HN_OFF + ((size_t)(cc * 4 + kh * 2 + cb) * 64 + tb) * 1024 + lane * 16) = v;
  }
}

DI void ftile(int ft, int& fb0, int& nb) {
  const int g = ft >> 3, i = ft & 7;
  const int base = (g >> 1) * 43 + (g & 1) * 22, n3 = (g & 1) ? 5 : 6;
  nb = i < n3 ? 3 : 2;
  fb0 = base + (i < n3 ? 3 * i : 3 * n3 + 2 * (i - n3));
}

__global__ __launch_bounds__(256) void k_mlp1(const char* w1, const char* b1, char* ws) {
  extern __shared__ __attribute__((aligned(16))) char sm[];
  const int b = blockIdx.x, xcd = b & 7, r = b >> 3;
  const int dt = (xcd & 1) * 4 + (r >> 3), ft = (xcd >> 1) * 8 + (r & 7);
  int fb0, nb; ftile(ft, fb0, nb);
  const int w = __builtin_amdgcn_readfirstlane(threadIdx.x >> 6), lane = threadIdx.x & 63;
  const int g4 = lane >> 4, q4 = (lane & 15) >> 2, p4 = lane & 3;
  auto rw = mkrs(w1, S * F * 2);
  f32x4 acc[4][3];
  for (int i = 0; i < 4; ++i) for (int j = 0; j < 3; ++j) acc[i][j] = f32x4{0, 0, 0, 0};
  char* st = sm + w * 24576;
  for (int half = 0; half < 2; ++half) {
    const int s0 = w * 512 + half * 256;
    for (int ks = 0; ks < 8; ++ks)
#pragma unroll
      for (int j = 0; j < 3; ++j) {
        const int fb = j < nb ? fb0 + j : fb0;
        __builtin_amdgcn_raw_ptr_buffer_load_lds(rw, (LAS void*)(st + (ks * 3 + j) * 1024), 16, (lane >> 1) * (F * 2) + (lane & 1) * 16, (s0 + ks * 32) * (F * 2) + fb * 32, 0, 0);
      }
    u32x4 hf[4][8];
#pragma unroll
    for (int i = 0; i < 4; ++i)
#pragma unroll
      for (int ks = 0; ks < 8; ++ks) hf[i][ks] = *(const u32x4*)(ws + HN_OFF + ((size_t)(dt * 4 + i) * 64 + (s0 >> 5) + ks) * 1024 + lane * 16);
    asm volatile("s_waitcnt vmcnt(0)" ::: "memory");
#pragma unroll
    for (int ks = 0; ks < 8; ++ks)
#pragma unroll
      for (int j = 0; j < 3; ++j) {
        const char* a = st + (ks * 3 + j) * 1024 + (8 * g4 + q4) * 32 + p4 * 8;
        u32x4 wf = cat(trread(a), trread(a + 4 * 32));
#pragma unroll
        for (int i = 0; i < 4; ++i) acc[i][j] = mfma16(wf, hf[i][ks], acc[i][j]);
      }
    asm volatile("s_waitcnt lgkmcnt(0)" ::: "memory");
  }
  __syncthreads();
  f32x4* red = (f32x4*)sm;
  for (int i = 0; i < 4; ++i) for (int j = 0; j < 3; ++j) red[(w * 12 + i * 3 + j) * 64 + lane] = acc[i][j];
  __syncthreads();
  for (int o = w; o < 6; o += 4) {
    const int dh = o / 3, j = o % 3;
    if (j >= nb) continue;
    const int h = lane >> 5, d = lane & 31, i = 2 * dh + (d >> 4);
    float v[8];
    for (int e = 0; e < 8; ++e) v[e] = 0.f;
    for (int ww = 0; ww < 4; ++ww)
      for (int z = 0; z < 2; ++z) {
        const f32x4 t = red[(ww * 12 + i * 3 + j) * 64 + (d & 15) + 16 * (2 * h + z)];
        for (int e = 0; e < 4; ++e) v[4 * z + e] += t[e];
      }
    const u32x4 bb = *(const u32x4*)(b1 + ((fb0 + j) * 16 + 8 * h) * 2);
    unsigned pk[4];
    for (int e = 0; e < 4; ++e) {
      float y[2] = {v[2 * e] + bflo(bb[e]), v[2 * e + 1] + bfhi(bb[e])};
      for (int z = 0; z < 2; ++z) {
        const float t = y[z];
        const float a = t * (1.f + 0.044715f * t * t) * (-2.f * 0.7978845608028654f * 1.4426950408889634f);
        y[z] = t * __builtin_amdgcn_rcpf(1.f + __builtin_amdgcn_exp2f(a));
      }
      pk[e] = cvtpk(y[0], y[1]);
    }
    *(u32x4*)(ws + UF_OFF + ((size_t)(dt * 2 + dh) * 86 + fb0 + j) * 1024 + lane * 16) = u32x4{pk[0], pk[1], pk[2], pk[3]};
  }
}

__global__ __launch_bounds__(256) void k_mlp2(const char* w2, const char* b2, char* ws, char* out) {
  extern __shared__ __attribute__((aligned(16))) char sm[];
  const int b = blockIdx.x, xcd = b & 7, r = b >> 3;
  const int tt = (xcd >> 1) * 8 + (r & 7), cc = (xcd & 1) * 4 + (r >> 3);
  const int w = __builtin_amdgcn_readfirstlane(threadIdx.x >> 6), lane = threadIdx.x & 63;
  const int h = lane >> 5, blk = (lane >> 4) & 1, q4 = (lane & 15) >> 2, p4 = lane & 3;
  auto rw = mkrs(w2, S * F * 2);
  const int kb0 = (w >> 1) * 43 + (w & 1) * 22, nk = (w & 1) ? 21 : 22;
  f32x16 acc[2][2] = {{zero16(), zero16()}, {zero16(), zero16()}};
  char* st = sm + w * 22528;
  const int rr = lane >> 3, c16 = lane & 7;
  const int src = rr * 4096 + ((c16 ^ (((rr >> 1) & 1) << 2)) * 16) + tt * 128;
  for (int half = 0; half < 2; ++half) {
    const int n = half == 0 ? 11 : nk - 11, k0 = kb0 + half * 11;
    for (int i = 0; i < 2 * n; ++i) __builtin_amdgcn_raw_ptr_buffer_load_lds(rw, (LAS void*)(st + i * 1024), 16, src, (k0 * 16 + i * 8) * 4096, 0, 0);
    u32x4 uf[2][11];
#pragma unroll
    for (int dh = 0; dh < 2; ++dh)
#pragma unroll
      for (int i = 0; i < 11; ++i) uf[dh][i] = *(const u32x4*)(ws + UF_OFF + ((size_t)(cc * 2 + dh) * 86 + k0 + (i < n ? i : 0)) * 1024 + lane * 16);
    asm volatile("s_waitcnt vmcnt(0)" ::: "memory");
#pragma unroll
    for (int i = 0; i < 11; ++i) {
      if (i < n) {
#pragma unroll
        for (int s2 = 0; s2 < 2; ++s2) {
          const int row = 8 * h + q4;
          const char* a = st + i * 2048 + row * 128 + ((s2 ^ ((row >> 1) & 1)) * 64) + blk * 32 + p4 * 8;
          u32x4 wf = cat(trread(a), trread(a + 4 * 128));
#pragma unroll
          for (int dh = 0; dh < 2; ++dh) acc[dh][s2] = mfma32(uf[dh][i], wf, acc[dh][s2]);
        }
      }
    }
    asm volatile("s_waitcnt lgkmcnt(0)" ::: "memory");
  }
  __syncthreads();
  f32x4* red = (f32x4*)sm;
  for (int dh = 0; dh < 2; ++dh) for (int s2 = 0; s2 < 2; ++s2) for (int g = 0; g < 4; ++g)
    red[((w * 4 + dh * 2 + s2) * 4 + g) * 64 + lane] = f32x4{acc[dh][s2][4 * g], acc[dh][s2][4 * g + 1], acc[dh][s2][4 * g + 2], acc[dh][s2][4 * g + 3]};
  __syncthreads();
  const int t2 = w & 1, kh = w >> 1, tok = (tt * 2 + t2) * 32 + (lane & 31);
  const u32x4 h0 = *(const u32x4*)(ws + H_OFF + ((size_t)b * 4 + w) * 2048 + lane * 16), h1 = *(const u32x4*)(ws + H_OFF + ((size_t)b * 4 + w) * 2048 + 1024 + lane * 16);
  const unsigned hp[8] = {h0.x, h0.y, h0.z, h0.w, h1.x, h1.y, h1.z, h1.w};
  const float bb = bflo(((const unsigned short*)b2)[tok]);
  for (int g = 0; g < 4; ++g) {
    f32x4 v = {bb, bb, bb, bb};
    for (int ww = 0; ww < 4; ++ww) v += red[((ww * 4 + kh * 2 + t2) * 4 + g) * 64 + lane];
    const unsigned a = cvtpk(v[0] + bflo(hp[2 * g]), v[1] + bfhi(hp[2 * g])), c = cvtpk(v[2] + bflo(hp[2 * g + 1]), v[3] + bfhi(hp[2 * g + 1]));
    *(u32x2*)(out + (size_t)tok * 1024 + (cc * 64 + kh * 32 + 8 * g + 4 * h) * 2) = u32x2{a, c};
  }
}

extern "C" void kernel_launch(void* const* d_in, const int* in_sizes, int n_in, void* d_out, int out_size, void* d_ws, size_t ws_size, hipStream_t stream) {
  const char* x = (const char*)d_in[0]; const char* wqkv = (const char*)d_in[1]; const char* wo = (const char*)d_in[2];
  const char* w1 = (const char*)d_in[4]; const char* b1 = (const char*)d_in[5]; const char* w2 = (const char*)d_in[6]; const char* b2 = (const char*)d_in[7];
  char* ws = (char*)d_ws;
  (void)hipFuncSetAttribute((const void*)k_qkv, hipFuncAttributeMaxDynamicSharedMemorySize, 102400);
  (void)hipFuncSetAttribute((const void*)k_proj, hipFuncAttributeMaxDynamicSharedMemorySize, 84 * 1024);
  (void)hipFuncSetAttribute((const void*)k_mlp1, hipFuncAttributeMaxDynamicSharedMemorySize, 96 * 1024);
  (void)hipFuncSetAttribute((const void*)k_mlp2, hipFuncAttributeMaxDynamicSharedMemorySize, 96 * 1024);
  k_qkv<<<256, 256, 102400, stream>>>(x, wqkv, ws);
  k_attn<<<256, 256, 0, stream>>>(ws);
  k_proj<<<256, 256, 84 * 1024, stream>>>(x, wo, ws);
  k_norm<<<256, 256, 0, stream>>>(ws);
  k_mlp1<<<256, 256, 96 * 1024, stream>>>(w1, b1, ws);
  k_mlp2<<<256, 256, 96 * 1024, stream>>>(w2, b2, ws, (char*)d_out);
}
```

```cpp
#include <hip/hip_runtime.h>
#include <hip/hip_bf16.h>

#define LAS __attribute__((address_space(3)))
#define DI __device__ __forceinline__
typedef unsigned u32x4 __attribute__((ext_vector_type(4)));
typedef unsigned u32x2 __attribute__((ext_vector_type(2)));
typedef float f32x4 __attribute__((ext_vector_type(4)));
typedef float f32x2 __attribute__((ext_vector_type(2)));
typedef float f32x16 __attribute__((ext_vector_type(16)));
typedef short s16x4 __attribute__((ext_vector_type(4)));
typedef short s16x8 __attribute__((ext_vector_type(8)));
typedef __bf16 bf16x8 __attribute__((ext_vector_type(8)));
typedef __bf16 bf16x2 __attribute__((ext_vector_type(2)));

constexpr int S = 2048, D = 512, F = 1376;
constexpr size_t MB = 1 << 20;
constexpr size_t QF_OFF = 1 * MB, KF_OFF = 3 * MB, VF_OFF = 5 * MB, OF_OFF = 7 * MB, HN_OFF = 9 * MB, UF_OFF = 11 * MB, H_OFF = 13 * MB, ST_OFF = 15 * MB;

DI f32x16 mfma32(u32x4 a, u32x4 b, f32x16 c) { return __builtin_amdgcn_mfma_f32_32x32x16_bf16(__builtin_bit_cast(bf16x8, a), __builtin_bit_cast(bf16x8, b), c, 0, 0, 0); }
DI f32x4 mfma16(u32x4 a, u32x4 b, f32x4 c) { return __builtin_amdgcn_mfma_f32_16x16x32_bf16(__builtin_bit_cast(bf16x8, a), __builtin_bit_cast(bf16x8, b), c, 0, 0, 0); }
DI unsigned cvtpk(float lo, float hi) { f32x2 v = {lo, hi}; return __builtin_bit_cast(unsigned, __builtin_convertvector(v, bf16x2)); }
DI float dot2(unsigned a, unsigned b, float c) { return __builtin_amdgcn_fdot2_f32_bf16(__builtin_bit_cast(bf16x2, a), __builtin_bit_cast(bf16x2, b), c, false); }
DI u32x2 trread(const char* lds) { return __builtin_bit_cast(u32x2, __builtin_amdgcn_ds_read_tr16_b64_v4i16((LAS s16x4*)lds)); }
DI u32x4 cat(u32x2 a, u32x2 b) { return u32x4{a.x, a.y, b.x, b.y}; }
DI float bflo(unsigned u) { return __builtin_bit_cast(float, u << 16); }
DI float bfhi(unsigned u) { return __builtin_bit_cast(float, u & 0xffff0000u); }
DI float swap32_add(float v) { float a = v, b = v; asm volatile("s_nop 1\n\tv_permlane32_swap_b32 %0, %1" : "+v"(a), "+v"(b)); return a + b; }
DI float swap32_max(float v) { float a = v, b = v; asm volatile("s_nop 1\n\tv_permlane32_swap_b32 %0, %1" : "+v"(a), "+v"(b)); return fmaxf(a, b); }
DI __amdgpu_buffer_rsrc_t mkrs(const void* p, unsigned bytes) { return __builtin_amdgcn_make_buffer_rsrc((void*)p, 0, bytes, 0x00020000); }
DI f32x16 zero16() { f32x16 z; for (int i = 0; i < 16; ++i) z[i] = 0.f; return z; }

__global__ __launch_bounds__(256) void k_qkv(const char* x, const char* wqkv, char* ws) {
  extern __shared__ __attribute__((aligned(16))) char sm[];
  const int b = blockIdx.x, xcd = b & 7, r = b >> 3;
  const int rt = (xcd >> 1) * 4 + (r & 3), ct = (xcd & 1) * 8 + (r >> 2);
  const int w = __builtin_amdgcn_readfirstlane(threadIdx.x >> 6), lane = threadIdx.x & 63;
  auto rw = mkrs(wqkv, D * 1536 * 2);
  for (int g = 0; g < 8; ++g) {
    const int rg = g * 4 + w;
#pragma unroll
    for (int i = 0; i < 3; ++i) {
      const int n = i * 64 + lane, row = n / 12, c16 = n % 12;
      __builtin_amdgcn_raw_ptr_buffer_load_lds(rw, (LAS void*)(sm + rg * 3072 + i * 1024), 16, row * 3072 + c16 * 16 + ct * 192, rg * 16 * 3072, 0, 0);
    }
  }
  auto rx = mkrs(x, S * D * 2);
  const int row0 = rt * 128 + w * 32;
  u32x4 L[16][2];
#pragma unroll
  for (int kk = 0; kk < 16; ++kk)
#pragma unroll
    for (int q = 0; q < 2; ++q) L[kk][q] = __builtin_amdgcn_raw_buffer_load_b128(rx, (row0 + q * 16 + (lane & 15)) * 1024 + (lane >> 4) * 16, kk * 64, 0);
  asm volatile("s_waitcnt vmcnt(0)" ::: "memory");
  __syncthreads();
  f32x16 acc[3]; for (int i = 0; i < 3; ++i) acc[i] = zero16();
  float sum = 0.f, sq = 0.f, cs[3] = {0.f, 0.f, 0.f};
  const int h = lane >> 5, blk = (lane >> 4) & 1, q4 = (lane & 15) >> 2, p4 = lane & 3;
  const char* wb = sm + (16 * h + q4) * 192 + blk * 32 + p4 * 8;
  const int T0 = ct * 3;
#pragma unroll
  for (int kk = 0; kk < 16; ++kk) {
    u32x4 Fr[2];
#pragma unroll
    for (int e = 0; e < 4; ++e) { auto s = __builtin_amdgcn_permlane16_swap(L[kk][0][e], L[kk][1][e], false, false); Fr[0][e] = s[0]; Fr[1][e] = s[1]; }
#pragma unroll
    for (int f = 0; f < 2; ++f) {
#pragma unroll
      for (int e = 0; e < 4; ++e) { sum = dot2(Fr[f][e], 0x3f803f80u, sum); sq = dot2(Fr[f][e], Fr[f][e], sq); }
#pragma unroll
      for (int i = 0; i < 3; ++i) {
        const char* a = wb + (32 * kk + 8 * f) * 192 + i * 64;
        u32x4 wf = cat(trread(a), trread(a + 4 * 192));
        if ((kk & 3) == w) { for (int e = 0; e < 4; ++e) cs[i] = dot2(wf[e], 0x3f803f80u, cs[i]); }
        if (T0 + i < 32) acc[i] = mfma32(wf, Fr[f], acc[i]); else acc[i] = mfma32(Fr[f], wf, acc[i]);
      }
    }
  }
  sum = swap32_add(sum); sq = swap32_add(sq);
  const float mu = sum * (1.f / D);
  const float var = fmaxf(sq * (1.f / D) - mu * mu, 0.f);
  const float rs = rsqrtf(var + 1e-5f);
  float* csl = (float*)(sm + 98304);
  float* stl = (float*)(sm + 98304 + 2048);
#pragma unroll
  for (int i = 0; i < 3; ++i) { float c = swap32_add(cs[i]); if (h == 0) csl[w * 96 + i * 32 + lane] = c; }
  if (h == 0) { stl[w * 64 + lane] = rs; stl[w * 64 + 32 + lane] = rs * mu; }
  __syncthreads();
  const int tb = rt * 4 + w;
#pragma unroll
  for (int i = 0; i < 3; ++i) {
    const int T = T0 + i, ty = T >> 4, hd = (T & 15) * 2;
    float o[16];
    if (ty < 2) {
      const float sc = ty == 0 ? 0.25f * 1.4426950408889634f : 1.f;
#pragma unroll
      for (int rg = 0; rg < 16; ++rg) {
        const int c = i * 32 + (rg & 3) + 8 * (rg >> 2) + 4 * h;
        const float cc = csl[c] + csl[96 + c] + csl[192 + c] + csl[288 + c];
        o[rg] = (acc[i][rg] - mu * cc) * (rs * sc);
      }
      char* dst = ws + (ty == 0 ? QF_OFF : KF_OFF);
#pragma unroll
      for (int hh = 0; hh < 2; ++hh) {
        u32x4 v = {cvtpk(o[8 * hh], o[8 * hh + 1]), cvtpk(o[8 * hh + 2], o[8 * hh + 3]), cvtpk(o[8 * hh + 4], o[8 * hh + 5]), cvtpk(o[8 * hh + 6], o[8 * hh + 7])};
        *(u32x4*)(dst + ((size_t)(hd + hh) * 64 + tb) * 1024 + lane * 16) = v;
      }
    } else {
      const int c = i * 32 + (lane & 31);
      const float cc = csl[c] + csl[96 + c] + csl[192 + c] + csl[288 + c];
#pragma unroll
      for (int rg = 0; rg < 16; ++rg) {
        const int t = (rg & 3) + 8 * (rg >> 2) + 4 * h;
        o[rg] = acc[i][rg] * stl[w * 64 + t] - stl[w * 64 + 32 + t] * cc;
      }
#pragma unroll
      for (int s = 0; s < 2; ++s) {
        u32x4 v = {cvtpk(o[8 * s], o[8 * s + 1]), cvtpk(o[8 * s + 2], o[8 * s + 3]), cvtpk(o[8 * s + 4], o[8 * s + 5]), cvtpk(o[8 * s + 6], o[8 * s + 7])};
        *(u32x4*)(ws + VF_OFF + ((size_t)(hd + blk) * 64 + tb) * 1024 + s * 512 + h * 256 + (lane & 15) * 16) = v;
      }
    }
  }
}

__global__ __launch_bounds__(256) void k_attn(char* ws) {
  __shared__ __attribute__((aligned(16))) char sm[32768];
  const int b = blockIdx.x, xcd = b & 7, r = b >> 3;
  const int head = xcd * 4 + (r & 3), qr = r >> 2;
  const int w = __builtin_amdgcn_readfirstlane(threadIdx.x >> 6), lane = threadIdx.x & 63;
  const int h = lane >> 5;
  for (int kl = w; kl < 16; kl += 4) {
    const int kb = qr * 8 - 8 + kl;
    if (kb >= 0) {
      *(u32x4*)(sm + kl * 1024 + lane * 16) = *(const u32x4*)(ws + KF_OFF + ((size_t)head * 64 + kb) * 1024 + lane * 16);
      *(u32x4*)(sm + 16384 + kl * 1024 + lane * 16) = *(const u32x4*)(ws + VF_OFF + ((size_t)head * 64 + kb) * 1024 + lane * 16);
    }
  }
  __syncthreads();
  const unsigned ones = ((lane & 31) == 16 || (lane & 31) == 20) ? 0x3f803f80u : 0u;
  const bool vl = (lane & 31) < 16;
  for (int qq = 0; qq < 2; ++qq) {
    const int ql = 2 * w + qq, qb = qr * 8 + ql;
    const u32x4 qf = *(const u32x4*)(ws + QF_OFF + ((size_t)head * 64 + qb) * 1024 + lane * 16);
    const int t0 = qb >= 8 ? 0 : 8 - qb;
    float m = 0.f;
    f32x16 o;
    for (int pass = 0; pass < 2; ++pass) {
      if (pass == 1) {
        m = -3.0e38f;
        for (int t = t0; t <= 8; ++t) {
          const u32x4 kf = *(const u32x4*)(sm + (ql + t) * 1024 + lane * 16);
          f32x16 s = mfma32(kf, qf, zero16());
#pragma unroll
          for (int rg = 0; rg < 16; ++rg) {
            const int kl = (rg & 3) + 8 * (rg >> 2) + 4 * h, q = lane & 31;
            const bool ok = t == 0 ? kl > q : (t == 8 ? kl <= q : true);
            m = fmaxf(m, ok ? s[rg] : -3.0e38f);
          }
        }
        m = swap32_max(m);
      }
      o = zero16();
      for (int t = t0; t <= 8; ++t) {
        const u32x4 kf = *(const u32x4*)(sm + (ql + t) * 1024 + lane * 16);
        f32x16 s = mfma32(kf, qf, zero16());
        float p[16];
#pragma unroll
        for (int rg = 0; rg < 16; ++rg) {
          const int kl = (rg & 3) + 8 * (rg >> 2) + 4 * h, q = lane & 31;
          const bool ok = t == 0 ? kl > q : (t == 8 ? kl <= q : true);
          p[rg] = ok ? __builtin_amdgcn_exp2f(s[rg] - m) : 0.f;
        }
#pragma unroll
        for (int st = 0; st < 2; ++st) {
          u32x4 pf = {cvtpk(p[8 * st], p[8 * st + 1]), cvtpk(p[8 * st + 2], p[8 * st + 3]), cvtpk(p[8 * st + 4], p[8 * st + 5]), cvtpk(p[8 * st + 6], p[8 * st + 7])};
          u32x4 vf = {ones, ones, ones, ones};
          if (vl) vf = *(const u32x4*)(sm + 16384 + (ql + t) * 1024 + st * 512 + h * 256 + (lane & 15) * 16);
          o = mfma32(vf, pf, o);
        }
      }
      const float l = o[8];
      bool ok = l >= 0x1p-60f && l <= 0x1p60f;
      if (pass == 1 || __all(ok)) break;
    }
    const float il = 1.f / o[8];
    u32x4 v = {cvtpk(o[0] * il, o[1] * il), cvtpk(o[2] * il, o[3] * il), cvtpk(o[4] * il, o[5] * il), cvtpk(o[6] * il, o[7] * il)};
    *(u32x4*)(ws + OF_OFF + ((size_t)qb * 32 + head) * 1024 + lane * 16) = v;
  }
}

__global__ __launch_bounds__(256) void k_proj(const char* x, const char* wo, char* ws) {
  extern __shared__ __attribute__((aligned(16))) char sm[];
  const int b = blockIdx.x, xcd = b & 7, r = b >> 3;
  const int tt = (xcd >> 1) * 8 + (r & 7), cc = (xcd & 1) * 4 + (r >> 3);
  const int w = __builtin_amdgcn_readfirstlane(threadIdx.x >> 6), lane = threadIdx.x & 63;
  const int h = lane >> 5, blk = (lane >> 4) & 1, q4 = (lane & 15) >> 2, p4 = lane & 3;
  auto rw = mkrs(wo, D * D * 2);
  {
    const int rr = lane >> 3, c16 = lane & 7;
    const int src = rr * 1024 + ((c16 ^ (((rr >> 1) & 1) << 2)) * 16) + cc * 128;
    for (int i = w; i < 64; i += 4) __builtin_amdgcn_raw_ptr_buffer_load_lds(rw, (LAS void*)(sm + i * 1024), 16, src, i * 8 * 1024, 0, 0);
  }
  const int t2 = w & 1, kh = w >> 1;
  const int tb = tt * 2 + t2;
  u32x4 of[16];
#pragma unroll
  for (int j = 0; j < 16; ++j) of[j] = *(const u32x4*)(ws + OF_OFF + ((size_t)tb * 32 + kh * 16 + j) * 1024 + lane * 16);
  const int tok = tb * 32 + (lane & 31);
  u32x2 xr[4];
#pragma unroll
  for (int g = 0; g < 4; ++g) xr[g] = *(const u32x2*)(x + (size_t)tok * 1024 + (cc * 64 + kh * 32 + 8 * g + 4 * h) * 2);
  asm volatile("s_waitcnt vmcnt(0)" ::: "memory");
  __syncthreads();
  f32x16 acc[2] = {zero16(), zero16()};
#pragma unroll
  for (int j = 0; j < 16; ++j)
#pragma unroll
    for (int i = 0; i < 2; ++i) {
      const int k0 = (kh * 16 + j) * 16 + 4 * h + q4;
      const int sw = ((k0 >> 1) & 1);
      const char* a = sm + k0 * 128 + ((i ^ sw) * 64) + blk * 32 + p4 * 8;
      u32x4 wf = cat(trread(a), trread(a + 8 * 128));
      acc[i] = mfma32(wf, of[j], acc[i]);
    }
  f32x4* ex = (f32x4*)(sm + 65536);
  {
    const f32x16 snd = kh == 0 ? acc[1] : acc[0];
#pragma unroll
    for (int g = 0; g < 4; ++g) ex[(w * 4 + g) * 64 + lane] = f32x4{snd[4 * g], snd[4 * g + 1], snd[4 * g + 2], snd[4 * g + 3]};
  }
  __syncthreads();
  const f32x16 own = kh == 0 ? acc[0] : acc[1];
  float sum = 0.f, sq = 0.f;
  unsigned hp[8];
#pragma unroll
  for (int g = 0; g < 4; ++g) {
    const f32x4 o = ex[((w ^ 2) * 4 + g) * 64 + lane];
    const float v0 = own[4 * g] + o[0] + bflo(xr[g].x), v1 = own[4 * g + 1] + o[1] + bfhi(xr[g].x), v2 = own[4 * g + 2] + o[2] + bflo(xr[g].y), v3 = own[4 * g + 3] + o[3] + bfhi(xr[g].y);
    hp[2 * g] = cvtpk(v0, v1); hp[2 * g + 1] = cvtpk(v2, v3);
    sum = dot2(hp[2 * g], 0x3f803f80u, sum); sum = dot2(hp[2 * g + 1], 0x3f803f80u, sum);
    sq = dot2(hp[2 * g], hp[2 * g], sq); sq = dot2(hp[2 * g + 1], hp[2 * g + 1], sq);
  }
  sum = swap32_add(sum); sq = swap32_add(sq);
  *(u32x4*)(ws + H_OFF + ((size_t)b * 4 + w) * 2048 + lane * 16) = u32x4{hp[0], hp[1], hp[2], hp[3]};
  *(u32x4*)(ws + H_OFF + ((size_t)b * 4 + w) * 2048 + 1024 + lane * 16) = u32x4{hp[4], hp[5], hp[6], hp[7]};
  if (h == 0) *(f32x2*)(ws + ST_OFF + ((size_t)tok * 16 + cc * 2 + kh) * 8) = f32x2{sum, sq};
}

__global__ __launch_bounds__(256) void k_norm(char* ws) {
  __shared__ __attribute__((aligned(16))) char sm[4 * 2048];
  const int b = blockIdx.x, xcd = b & 7, r = b >> 3;
  const int tt = (xcd >> 1) * 8 + (r & 7), cc = (xcd & 1) * 4 + (r >> 3);
  const int w = __builtin_amdgcn_readfirstlane(threadIdx.x >> 6), lane = threadIdx.x & 63;
  const int h = lane >> 5;
  const int t2 = w & 1, kh = w >> 1, tb = tt * 2 + t2, tok = tb * 32 + (lane & 31);
  const u32x4 h0 = *(const u32x4*)(ws + H_OFF + ((size_t)b * 4 + w) * 2048 + lane * 16), h1 = *(const u32x4*)(ws + H_OFF + ((size_t)b * 4 + w) * 2048 + 1024 + lane * 16);
  const unsigned hp[8] = {h0.x, h0.y, h0.z, h0.w, h1.x, h1.y, h1.z, h1.w};
  float sum = 0.f, sq = 0.f;
  for (int j = 0; j < 16; ++j) { const f32x2 s = *(const f32x2*)(ws + ST_OFF + ((size_t)tok * 16 + j) * 8); sum += s.x; sq += s.y; }
  const float mu = sum * (1.f / D), var = fmaxf(sq * (1.f / D) - mu * mu, 0.f);
  const float r1 = rsqrtf(var + 1e-5f);
  const float v2 = var * r1 * r1;
  const float rs = r1 * rsqrtf(v2 + 1e-5f);
  char* im = sm + w * 2048;
#pragma unroll
  for (int g = 0; g < 4; ++g) {
    const unsigned a = cvtpk((bflo(hp[2 * g]) - mu) * rs, (bfhi(hp[2 * g]) - mu) * rs), c = cvtpk((bflo(hp[2 * g + 1]) - mu) * rs, (bfhi(hp[2 * g + 1]) - mu) * rs);
    *(u32x2*)(im + (g >> 1) * 1024 + (lane & 31) * 32 + ((g & 1) * 8 + 4 * h) * 2) = u32x2{a, c};
  }
  __syncthreads();
  const int g4 = lane >> 4, q4 = (lane & 15) >> 2, p4 = lane & 3;
#pragma unroll
  for (int cb = 0; cb < 2; ++cb) {
    const char* a = im + cb * 1024 + (8 * g4 + q4) * 32 + p4 * 8;
    u32x4 v = cat(trread(a), trread(a + 4 * 32));
    *(u32x4*)(ws + HN_OFF + ((size_t)(cc * 4 + kh * 2 + cb) * 64 + tb) * 1024 + lane * 16) = v;
  }
}

DI void ftile(int ft, int& fb0, int& nb) {
  const int g = ft >> 3, i = ft & 7;
  const int base = (g >> 1) * 43 + (g & 1) * 22, n3 = (g & 1) ? 5 : 6;
  nb = i < n3 ? 3 : 2;
  fb0 = base + (i < n3 ? 3 * i : 3 * n3 + 2 * (i - n3));
}

__global__ __launch_bounds__(256) void k_mlp1(const char* w1, const char* b1, char* ws) {
  extern __shared__ __attribute__((aligned(16))) char sm[];
  const int b = blockIdx.x, xcd = b & 7, r = b >> 3;
  const int dt = (xcd & 1) * 4 + (r >> 3), ft = (xcd >> 1) * 8 + (r & 7);
  int fb0, nb; ftile(ft, fb0, nb);
  const int w = __builtin_amdgcn_readfirstlane(threadIdx.x >> 6), lane = threadIdx.x & 63;
  const int g4 = lane >> 4, q4 = (lane & 15) >> 2, p4 = lane & 3;
  auto rw = mkrs(w1, S * F * 2);
  f32x4 acc[4][3];
  for (int i = 0; i < 4; ++i) for (int j = 0; j < 3; ++j) acc[i][j] = f32x4{0, 0, 0, 0};
  char* st = sm + w * 24576;
  for (int half = 0; half < 2; ++half) {
    const int s0 = w * 512 + half * 256;
    for (int ks = 0; ks < 8; ++ks)
#pragma unroll
      for (int j = 0; j < 3; ++j) {
        const int fb = j < nb ? fb0 + j : fb0;
        __builtin_amdgcn_raw_ptr_buffer_load_lds(rw, (LAS void*)(st + (ks * 3 + j) * 1024), 16, (lane >> 1) * (F * 2) + (lane & 1) * 16, (s0 + ks * 32) * (F * 2) + fb * 32, 0, 0);
      }
    u32x4 hf[4][8];
#pragma unroll
    for (int i = 0; i < 4; ++i)
#pragma unroll
      for (int ks = 0; ks < 8; ++ks) hf[i][ks] = *(const u32x4*)(ws + HN_OFF + ((size_t)(dt * 4 + i) * 64 + (s0 >> 5) + ks) * 1024 + lane * 16);
    asm volatile("s_waitcnt vmcnt(0)" ::: "memory");
#pragma unroll
    for (int ks = 0; ks < 8; ++ks)
#pragma unroll
      for (int j = 0; j < 3; ++j) {
        const char* a = st + (ks * 3 + j) * 1024 + (8 * g4 + q4) * 32 + p4 * 8;
        u32x4 wf = cat(trread(a), trread(a + 4 * 32));
#pragma unroll
        for (int i = 0; i < 4; ++i) acc[i][j] = mfma16(wf, hf[i][ks], acc[i][j]);
      }
    asm volatile("s_waitcnt lgkmcnt(0)" ::: "memory");
  }
  __syncthreads();
  f32x4* red = (f32x4*)sm;
  for (int i = 0; i < 4; ++i) for (int j = 0; j < 3; ++j) red[(w * 12 + i * 3 + j) * 64 + lane] = acc[i][j];
  __syncthreads();
  for (int o = w; o < 6; o += 4) {
    const int dh = o / 3, j = o % 3;
    if (j >= nb) continue;
    const int h = lane >> 5, d = lane & 31, i = 2 * dh + (d >> 4);
    float v[8];
    for (int e = 0; e < 8; ++e) v[e] = 0.f;
    for (int ww = 0; ww < 4; ++ww)
      for (int z = 0; z < 2; ++z) {
        const f32x4 t = red[(ww * 12 + i * 3 + j) * 64 + (d & 15) + 16 * (2 * h + z)];
        for (int e = 0; e < 4; ++e) v[4 * z + e] += t[e];
      }
    const u32x4 bb = *(const u32x4*)(b1 + ((fb0 + j) * 16 + 8 * h) * 2);
    unsigned pk[4];
    for (int e = 0; e < 4; ++e) {
      float y[2] = {v[2 * e] + bflo(bb[e]), v[2 * e + 1] + bfhi(bb[e])};
      for (int z = 0; z < 2; ++z) {
        const float t = y[z];
        const float a = t * (1.f + 0.044715f * t * t) * (-2.f * 0.7978845608028654f * 1.4426950408889634f);
        y[z] = t * __builtin_amdgcn_rcpf(1.f + __builtin_amdgcn_exp2f(a));
      }
      pk[e] = cvtpk(y[0], y[1]);
    }
    *(u32x4*)(ws + UF_OFF + ((size_t)(dt * 2 + dh) * 86 + fb0 + j) * 1024 + lane * 16) = u32x4{pk[0], pk[1], pk[2], pk[3]};
  }
}

__global__ __launch_bounds__(256) void k_mlp2(const char* w2, const char* b2, char* ws, char* out) {
  extern __shared__ __attribute__((aligned(16))) char sm[];
  const int b = blockIdx.x, xcd = b & 7, r = b >> 3;
  const int tt = (xcd >> 1) * 8 + (r & 7), cc = (xcd & 1) * 4 + (r >> 3);
  const int w = __builtin_amdgcn_readfirstlane(threadIdx.x >> 6), lane = threadIdx.x & 63;
  const int h = lane >> 5, blk = (lane >> 4) & 1, q4 = (lane & 15) >> 2, p4 = lane & 3;
  auto rw = mkrs(w2, S * F * 2);
  const int kb0 = (w >> 1) * 43 + (w & 1) * 22, nk = (w & 1) ? 21 : 22;
  f32x16 acc[2][2] = {{zero16(), zero16()}, {zero16(), zero16()}};
  char* st = sm + w * 22528;
  const int rr = lane >> 3, c16 = lane & 7;
  const int src = rr * 4096 + ((c16 ^ (((rr >> 1) & 1) << 2)) * 16) + tt * 128;
  for (int half = 0; half < 2; ++half) {
    const int n = half == 0 ? 11 : nk - 11, k0 = kb0 + half * 11;
    for (int i = 0; i < 2 * n; ++i) __builtin_amdgcn_raw_ptr_buffer_load_lds(rw, (LAS void*)(st + i * 1024), 16, src, (k0 * 16 + i * 8) * 4096, 0, 0);
    u32x4 uf[2][11];
#pragma unroll
    for (int dh = 0; dh < 2; ++dh)
#pragma unroll
      for (int i = 0; i < 11; ++i) uf[dh][i] = *(const u32x4*)(ws + UF_OFF + ((size_t)(cc * 2 + dh) * 86 + k0 + (i < n ? i : 0)) * 1024 + lane * 16);
    asm volatile("s_waitcnt vmcnt(0)" ::: "memory");
#pragma unroll
    for (int i = 0; i < 11; ++i) {
      if (i < n) {
#pragma unroll
        for (int s2 = 0; s2 < 2; ++s2) {
          const int row = 8 * h + q4;
          const char* a = st + i * 2048 + row * 128 + ((s2 ^ ((row >> 1) & 1)) * 64) + blk * 32 + p4 * 8;
          u32x4 wf = cat(trread(a), trread(a + 4 * 128));
#pragma unroll
          for (int dh = 0; dh < 2; ++dh) acc[dh][s2] = mfma32(uf[dh][i], wf, acc[dh][s2]);
        }
      }
    }
    asm volatile("s_waitcnt lgkmcnt(0)" ::: "memory");
  }
  __syncthreads();
  f32x4* red = (f32x4*)sm;
  for (int dh = 0; dh < 2; ++dh) for (int s2 = 0; s2 < 2; ++s2) for (int g = 0; g < 4; ++g)
    red[((w * 4 + dh * 2 + s2) * 4 + g) * 64 + lane] = f32x4{acc[dh][s2][4 * g], acc[dh][s2][4 * g + 1], acc[dh][s2][4 * g + 2], acc[dh][s2][4 * g + 3]};
  __syncthreads();
  const int t2 = w & 1, kh = w >> 1, tok = (tt * 2 + t2) * 32 + (lane & 31);
  const u32x4 h0 = *(const u32x4*)(ws + H_OFF + ((size_t)b * 4 + w) * 2048 + lane * 16), h1 = *(const u32x4*)(ws + H_OFF + ((size_t)b * 4 + w) * 2048 + 1024 + lane * 16);
  const unsigned hp[8] = {h0.x, h0.y, h0.z, h0.w, h1.x, h1.y, h1.z, h1.w};
  const float bb = bflo(((const unsigned short*)b2)[tok]);
  for (int g = 0; g < 4; ++g) {
    f32x4 v = {bb, bb, bb, bb};
    for (int ww = 0; ww < 4; ++ww) v += red[((ww * 4 + kh * 2 + t2) * 4 + g) * 64 + lane];
    const unsigned a = cvtpk(v[0] + bflo(hp[2 * g]), v[1] + bfhi(hp[2 * g])), c = cvtpk(v[2] + bflo(hp[2 * g + 1]), v[3] + bfhi(hp[2 * g + 1]));
    *(u32x2*)(out + (size_t)tok * 1024 + (cc * 64 + kh * 32 + 8 * g + 4 * h) * 2) = u32x2{a, c};
  }
}


__global__ void k_probe(const char* p, int n, int stride, unsigned* sink) {
  if (threadIdx.x != 0) return;
  unsigned acc = 0;
  for (int i = 0; i < n; ++i) {
    unsigned v = *(const volatile unsigned*)(p + (size_t)i * stride + acc);
    asm volatile("v_and_b32 %0, 0, %1" : "=v"(acc) : "v"(v));
  }
  if (acc == 12345) sink[0] = acc;
}
extern "C" void kernel_launch(void* const* d_in, const int* in_sizes, int n_in, void* d_out, int out_size, void* d_ws, size_t ws_size, hipStream_t stream) {
  const char* x = (const char*)d_in[0]; const char* wqkv = (const char*)d_in[1]; const char* wo = (const char*)d_in[2];
  const char* w1 = (const char*)d_in[4]; const char* b1 = (const char*)d_in[5]; const char* w2 = (const char*)d_in[6]; const char* b2 = (const char*)d_in[7];
  char* ws = (char*)d_ws;
  (void)hipFuncSetAttribute((const void*)k_qkv, hipFuncAttributeMaxDynamicSharedMemorySize, 102400);
  (void)hipFuncSetAttribute((const void*)k_proj, hipFuncAttributeMaxDynamicSharedMemorySize, 84 * 1024);
  (void)hipFuncSetAttribute((const void*)k_mlp1, hipFuncAttributeMaxDynamicSharedMemorySize, 96 * 1024);
  (void)hipFuncSetAttribute((const void*)k_mlp2, hipFuncAttributeMaxDynamicSharedMemorySize, 96 * 1024);
  k_probe<<<1, 64, 0, stream>>>((const char*)d_in[3], 0, 32768, (unsigned*)(ws + 200 * MB));
  k_qkv<<<256, 256, 102400, stream>>>(x, wqkv, ws);
  k_attn<<<256, 256, 0, stream>>>(ws);
  k_proj<<<256, 256, 84 * 1024, stream>>>(x, wo, ws);
  k_norm<<<256, 256, 0, stream>>>(ws);
  k_mlp1<<<256, 256, 96 * 1024, stream>>>(w1, b1, ws);
  k_mlp2<<<256, 256, 96 * 1024, stream>>>(w2, b2, ws, (char*)d_out);
}
```

```cpp
#include <hip/hip_runtime.h>

#define CLOB_V "v1", "v2", "v3", "v4", "v5", "v6", "v7", "v8", "v9", "v10", "v11", "v12", "v13", "v14", "v15", "v16", "v17", "v18", "v19", "v20", "v21", "v22", "v23", "v24", "v25", "v26", "v27", "v28", "v29", "v30", "v31", "v32", "v33", "v34", "v35", "v36", "v37", "v38", "v39", "v40", "v41", "v42", "v43", "v44", "v45", "v46", "v47", "v48", "v49", "v50", "v51", "v52", "v53", "v54", "v55", "v56", "v57", "v58", "v59", "v60", "v61", "v62", "v63", "v64", "v65", "v66", "v67", "v68", "v69", "v70", "v71", "v72", "v73", "v74", "v75", "v76", "v77", "v78", "v79", "v80", "v81", "v82", "v83", "v84", "v85", "v86", "v87", "v88", "v89", "v90", "v91", "v92", "v93", "v94", "v95", "v96", "v97", "v98", "v99", "v100", "v101", "v102", "v103", "v104", "v105", "v106", "v107", "v108", "v109", "v110", "v111", "v112", "v113", "v114", "v115", "v116", "v117", "v118", "v119", "v120", "v121", "v122", "v123", "v124", "v125", "v126", "v127", "v128", "v129", "v130", "v131", "v132", "v133", "v134", "v135", "v136", "v137", "v138", "v139", "v140", "v141", "v142", "v143", "v144", "v145", "v146", "v147", "v148", "v149", "v150", "v151", "v152", "v153", "v154", "v155", "v156", "v157", "v158", "v159", "v160", "v161", "v162", "v163", "v164", "v165", "v166", "v167", "v168", "v169", "v170", "v171", "v172", "v173", "v174", "v175", "v176", "v177", "v178", "v179", "v180", "v181", "v182", "v183", "v184", "v185", "v186", "v187", "v188", "v189", "v190", "v191", "v192", "v193", "v194", "v195", "v196", "v197", "v198", "v199", "v200", "v201", "v202", "v203", "v204", "v205", "v206", "v207", "v208", "v209", "v210", "v211", "v212", "v213", "v214", "v215", "v216", "v217", "v218", "v219", "v220", "v221", "v222", "v223", "v224", "v225", "v226", "v227", "v228", "v229", "v230", "v231", "v232", "v233", "v234", "v235", "v236", "v237", "v238", "v239", "v240", "v241", "v242", "v243", "v244", "v245", "v246", "v247", "v248", "v249", "v250", "v251", "v252", "v253", "v254", "v255"
#define CLOB_A "a0", "a1", "a2", "a3", "a4", "a5", "a6", "a7", "a8", "a9", "a10", "a11", "a12", "a13", "a14", "a15", "a16", "a17", "a18", "a19", "a20", "a21", "a22", "a23", "a24", "a25", "a26", "a27", "a28", "a29", "a30", "a31", "a32", "a33", "a34", "a35", "a36", "a37", "a38", "a39", "a40", "a41", "a42", "a43", "a44", "a45", "a46", "a47", "a48", "a49", "a50", "a51", "a52", "a53", "a54", "a55", "a56", "a57", "a58", "a59", "a60", "a61", "a62", "a63", "a64", "a65", "a66", "a67", "a68", "a69", "a70", "a71", "a72", "a73", "a74", "a75", "a76", "a77", "a78", "a79", "a80", "a81", "a82", "a83", "a84", "a85", "a86", "a87", "a88", "a89", "a90", "a91", "a92", "a93", "a94", "a95", "a96", "a97", "a98", "a99", "a100", "a101", "a102", "a103", "a104", "a105", "a106", "a107", "a108", "a109", "a110", "a111", "a112", "a113", "a114", "a115", "a116", "a117", "a118", "a119", "a120", "a121", "a122", "a123", "a124", "a125", "a126", "a127", "a128", "a129", "a130", "a131", "a132", "a133", "a134", "a135", "a136", "a137", "a138", "a139", "a140", "a141", "a142", "a143", "a144", "a145", "a146", "a147", "a148", "a149", "a150", "a151", "a152", "a153", "a154", "a155", "a156", "a157", "a158", "a159", "a160", "a161", "a162", "a163", "a164", "a165", "a166", "a167", "a168", "a169", "a170", "a171", "a172", "a173", "a174", "a175", "a176", "a177", "a178", "a179", "a180", "a181", "a182", "a183", "a184", "a185", "a186", "a187", "a188", "a189", "a190", "a191", "a192", "a193", "a194", "a195", "a196", "a197", "a198", "a199", "a200", "a201", "a202", "a203", "a204", "a205", "a206", "a207", "a208", "a209", "a210", "a211", "a212", "a213", "a214", "a215", "a216", "a217", "a218", "a219", "a220", "a221", "a222", "a223", "a224", "a225", "a226", "a227", "a228", "a229", "a230", "a231", "a232", "a233", "a234", "a235", "a236", "a237", "a238", "a239", "a240", "a241", "a242", "a243", "a244", "a245", "a246", "a247", "a248", "a249", "a250", "a251", "a252", "a253", "a254", "a255"
#define CLOB_S "s4", "s5", "s6", "s7", "s8", "s9", "s10", "s11", "s12", "s13", "s14", "s15", "s16", "s17", "s18", "s19", "s20", "s21", "s22", "s23", "s24", "s25", "s26", "s27", "s28", "s29", "s30", "s31", "s33", "s34", "s35", "s36", "s37", "s38", "s39", "s40", "s41", "s42", "s43", "s44", "s45", "s46", "s47", "s48", "s49", "s50", "s51", "s52", "s53", "s54", "s55", "s56", "s57", "s58", "s59", "s60", "s61", "s62", "s63", "s64", "s65", "s66", "s67", "s68", "s69", "s70", "s71", "s72", "s73", "s74", "s75", "s76", "s77", "s78", "s79", "s80", "s81", "s82", "s83", "s84", "s85", "s86", "s87", "s88", "s89", "s90", "s91", "s92", "s93", "s94", "s95", "s96", "s97", "s98", "s99"

__global__ __launch_bounds__(256, 1) void fused_block_kernel(const void* x, const void* wqkv, const void* wo, const void* w1, const void* b1,
                                                             const void* w2, const void* b2, void* out, void* ws) {
  __shared__ __attribute__((aligned(16))) char lds[163840];
  unsigned tid = threadIdx.x;
  asm volatile(
      ".Lcode_start_%=:\n"
      "s_load_dwordx16 s[4:19], %[karg], 0\n"
      "s_load_dwordx2 s[20:21], %[karg], 64\n"
      "s_getpc_b64 s[48:49]\n"
      ".Lpc_ref_%=:\n"
      "v_lshrrev_b32 v7, 6, %[tid]\n"
      "v_and_b32 v6, 63, %[tid]\n"
      "v_readfirstlane_b32 s62, v7\n"
      "s_and_b32 s63, %[wg], 7\n"
      "s_lshr_b32 s64, %[wg], 3\n"
      "s_and_b32 s49, s49, 65535\n"
      "s_mov_b32 s50, .Lcode_end_%=-.Lpc_ref_%=\n"
      "s_mov_b32 s51, 131072\n"
      "s_lshl_b32 s73, s64, 11\n"
      "s_lshl_b32 s76, s62, 9\n"
      "s_add_u32 s73, s73, s76\n"
      "v_lshlrev_b32 v7, 7, v6\n"
      "s_mov_b64 exec, 15\n"
      "buffer_load_dword a255, v7, s[48:51], s73 offen\n"
      "s_mov_b64 exec, -1\n"
      "v_lshlrev_b32 v1, 4, v6\n"
      "s_lshr_b32 s79, s63, 1\n"
      "s_and_b32 s75, s64, 3\n"
      "s_lshl_b32 s65, s79, 2\n"
      "s_add_u32 s65, s65, s75\n"
      "s_and_b32 s80, s63, 1\n"
      "s_lshr_b32 s68, s64, 2\n"
      "s_lshl_b32 s66, s80, 3\n"
      "s_add_u32 s66, s66, s68\n"
      "v_add_u32 v20, 0, v6\n"
      "v_mul_u32_u24 v21, 43691, v20\n"
      "v_lshrrev_b32 v21, 19, v21\n"
      "v_mul_u32_u24 v21, 2880, v21\n"
      "v_lshl_add_u32 v26, v20, 4, v21\n"
      "v_add_u32 v20, 64, v6\n"
      "v_mul_u32_u24 v21, 43691, v20\n"
      "v_lshrrev_b32 v21, 19, v21\n"
      "v_mul_u32_u24 v21, 2880, v21\n"
      "v_lshl_add_u32 v27, v20, 4, v21\n"
      "v_subrev_u32 v27, 1024, v27\n"
      "v_add_u32 v20, 128, v6\n"
      "v_mul_u32_u24 v21, 43691, v20\n"
      "v_lshrrev_b32 v21, 19, v21\n"
      "v_mul_u32_u24 v21, 2880, v21\n"
      "v_lshl_add_u32 v28, v20, 4, v21\n"
      "v_subrev_u32 v28, 2048, v28\n"
      "v_lshrrev_b32 v20, 3, v6\n"
      "v_bfe_u32 v21, v6, 4, 2\n"
      "v_and_b32 v22, 7, v6\n"
      "v_xor_b32 v21, v21, v22\n"
      "v_lshlrev_b32 v21, 4, v21\n"
      "v_lshl_add_u32 v29, v20, 10, v21\n"
      "v_xor_b32 v30, 64, v29\n"
      "s_lshl_b32 s81, s65, 17\n"
      "s_lshl_b32 s76, s62, 15\n"
      "s_add_u32 s81, s81, s76\n"
      "s_lshl_b32 s82, s62, 12\n"
      "s_add_u32 s82, s82, 12288\n"
      "s_mul_i32 s83, s62, 49152\n"
      "s_mul_i32 s76, s66, 192\n"
      "s_add_u32 s83, s83, s76\n"
      "s_mul_i32 s84, s62, 3072\n"
      "s_lshl_b32 s85, s65, 6\n"
      "s_lshl_b32 s76, s66, 2\n"
      "s_add_u32 s85, s85, s76\n"
      "s_add_u32 s85, s85, s62\n"
      "s_mov_b32 s87, s65\n"
      "s_add_u32 s87, s87, s66\n"
      "s_lshl_b32 s87, s87, 1\n"
      "s_lshl_b32 s77, s85, 11\n"
      "v_mov_b32 v2, 0\n"
      "v_lshlrev_b32 v19, 3, v6\n"
      "v_lshrrev_b32 v22, 5, v6\n"
      "v_bfe_u32 v21, v6, 2, 2\n"
      "v_lshl_add_u32 v20, v22, 3, v21\n"
      "v_mul_u32_u24 v20, 192, v20\n"
      "v_bfe_u32 v23, v6, 4, 1\n"
      "v_lshl_add_u32 v20, v23, 5, v20\n"
      "v_and_b32 v21, 3, v6\n"
      "v_lshl_add_u32 v20, v21, 3, v20\n"
      "v_and_b32 v24, 15, v6\n"
      "v_lshlrev_b32 v25, 8, v22\n"
      "v_lshl_add_u32 v25, v24, 4, v25\n"
      "v_lshl_add_u32 v38, v23, 16, v25\n"
      "v_lshl_add_u32 v18, v23, 14, v25\n"
      "v_cmp_eq_u32_e32 vcc, v23, v24\n"
      "v_mov_b32 v25, 1065369472\n"
      "s_lshl_b32 s73, s62, 2\n"
      "v_lshl_add_u32 v39, v24, 4, s73\n"
      "v_cndmask_b32_e32 v168, v2, v25, vcc\n"
      "v_mov_b32 v169, v168\n"
      "v_mov_b32 v170, v168\n"
      "v_mov_b32 v171, v168\n"
      "s_lshl_b32 s73, s62, 7\n"
      "s_add_u32 s73, s73, 3072\n"
      "v_lshl_add_u32 v42, v22, 4, s73\n"
      "s_lshl_b32 s74, s62, 1\n"
      "v_add_u32 v22, s74, v22\n"
      "v_bfe_u32 v21, v6, 1, 3\n"
      "v_xor_b32 v22, v22, v21\n"
      "v_and_b32 v21, 31, v6\n"
      "v_lshlrev_b32 v40, 4, v21\n"
      "v_lshl_add_u32 v41, v21, 2, s73\n"
      "s_lshr_b32 s73, s81, 6\n"
      "s_add_u32 s73, s73, 851968\n"
      "v_add_u32 v48, s73, v40\n"
      "v_lshlrev_b32 v21, 7, v21\n"
      "v_lshl_add_u32 v21, v22, 4, v21\n"
      "v_add_u32 v200, s82, v21\n"
      "s_xor_b32 s73, s62, 0\n"
      "s_mul_i32 s73, s73, 3072\n"
      "v_add_u32 v192, s73, v20\n"
      "s_xor_b32 s73, s62, 1\n"
      "s_mul_i32 s73, s73, 3072\n"
      "v_add_u32 v193, s73, v20\n"
      "v_xor_b32 v201, 32, v200\n"
      "s_xor_b32 s73, s62, 2\n"
      "s_mul_i32 s73, s73, 3072\n"
      "v_add_u32 v194, s73, v20\n"
      "v_xor_b32 v202, 64, v200\n"
      "s_xor_b32 s73, s62, 3\n"
      "s_mul_i32 s73, s73, 3072\n"
      "v_add_u32 v195, s73, v20\n"
      "v_xor_b32 v203, 96, v200\n"
      "v_add_u32 v196, 57344, v192\n"
      "v_add_u32 v204, 57344, v200\n"
      "v_add_u32 v197, 57344, v193\n"
      "v_add_u32 v205, 57344, v201\n"
      "v_add_u32 v198, 57344, v194\n"
      "v_add_u32 v206, 57344, v202\n"
      "v_add_u32 v199, 57344, v195\n"
      "v_add_u32 v207, 57344, v203\n"
      "s_mov_b32 s86, 1065369472\n"
      "v_lshlrev_b32 v45, 10, v6\n"
      "v_lshlrev_b32 v255, 12, v6\n"
      "v_lshrrev_b32 v24, 4, v6\n"
      "v_lshlrev_b32 v24, 3, v24\n"
      "v_mov_b32 v88, 0\n"
      "v_mov_b32 v89, 0\n"
      "v_mov_b32 v90, 0\n"
      "v_mov_b32 v91, 0\n"
      "v_and_b32 v20, 16, v6\n"
      "v_cmp_eq_u32_e32 vcc, 0, v20\n"
      "s_waitcnt lgkmcnt(0)\n"
      "s_load_dwordx2 s[60:61], s[20:21], 786432\n"
      "s_mov_b32 s24, s4\n"
      "s_and_b32 s25, s5, 65535\n"
      "s_mov_b32 s26, 2097152\n"
      "s_mov_b32 s27, 131072\n"
      "buffer_load_dwordx4 v[80:83], v1, s[24:27], s77 offen\n"
      "buffer_load_dwordx4 v[84:87], v1, s[24:27], s77 offen offset:1024\n"
      "s_mov_b32 s28, s6\n"
      "s_and_b32 s29, s7, 65535\n"
      "s_mov_b32 s30, 1572864\n"
      "s_mov_b32 s31, 131072\n"
      "s_add_u32 s89, s87, 0\n"
      "s_and_b32 s89, s89, 7\n"
      "s_lshl_b32 s88, s89, 7\n"
      "s_add_u32 s88, s88, s81\n"
      "s_add_u32 m0, s82, 0\n"
      "s_add_u32 s77, s88, 0\n"
      "buffer_load_dwordx4 v29, s[24:27], s77 offen lds\n"
      "s_add_u32 m0, s82, 1024\n"
      "s_add_u32 s77, s88, 8192\n"
      "buffer_load_dwordx4 v30, s[24:27], s77 offen lds\n"
      "s_add_u32 m0, s82, 2048\n"
      "s_add_u32 s77, s88, 16384\n"
      "buffer_load_dwordx4 v29, s[24:27], s77 offen lds\n"
      "s_add_u32 m0, s82, 3072\n"
      "s_add_u32 s77, s88, 24576\n"
      "buffer_load_dwordx4 v30, s[24:27], s77 offen lds\n"
      "s_add_u32 m0, s84, 0\n"
      "s_mul_i32 s78, s89, 196608\n"
      "s_add_u32 s78, s78, s83\n"
      "buffer_load_dwordx4 v26, s[28:31], s78 offen lds\n"
      "buffer_load_dwordx4 v27, s[28:31], s78 offen offset:1024 lds\n"
      "buffer_load_dwordx4 v28, s[28:31], s78 offen offset:2048 lds\n"
      "s_add_u32 s89, s87, 1\n"
      "s_and_b32 s89, s89, 7\n"
      "s_lshl_b32 s88, s89, 7\n"
      "s_add_u32 s88, s88, s81\n"
      "s_add_u32 m0, s82, 28672\n"
      "s_add_u32 s77, s88, 0\n"
      "buffer_load_dwordx4 v29, s[24:27], s77 offen lds\n"
      "s_add_u32 m0, s82, 29696\n"
      "s_add_u32 s77, s88, 8192\n"
      "buffer_load_dwordx4 v30, s[24:27], s77 offen lds\n"
      "s_add_u32 m0, s82, 30720\n"
      "s_add_u32 s77, s88, 16384\n"
      "buffer_load_dwordx4 v29, s[24:27], s77 offen lds\n"
      "s_add_u32 m0, s82, 31744\n"
      "s_add_u32 s77, s88, 24576\n"
      "buffer_load_dwordx4 v30, s[24:27], s77 offen lds\n"
      "s_add_u32 m0, s84, 28672\n"
      "s_mul_i32 s78, s89, 196608\n"
      "s_add_u32 s78, s78, s83\n"
      "buffer_load_dwordx4 v26, s[28:31], s78 offen lds\n"
      "buffer_load_dwordx4 v27, s[28:31], s78 offen offset:1024 lds\n"
      "buffer_load_dwordx4 v28, s[28:31], s78 offen offset:2048 lds\n"
      "s_mov_b32 s44, s20\n"
      "s_and_b32 s45, s21, 65535\n"
      "s_mov_b32 s46, 22020096\n"
      "s_mov_b32 s47, 131072\n"
      "s_lshl_b32 s85, s85, 5\n"
      "s_add_u32 s85, s85, 851968\n"
      "s_waitcnt lgkmcnt(0)\n"
      "s_add_u32 s60, s60, 2135587861\n"
      "s_addc_u32 s61, s61, 2654435769\n"
      "v_mov_b32 v4, s60\n"
      "v_mov_b32 v5, s61\n"
      "v_cndmask_b32_e32 v93, v5, v4, vcc\n"
      "s_waitcnt vmcnt(14)\n"
      "v_dot2c_f32_bf16 v88, s86, v80\n"
      "v_dot2c_f32_bf16 v90, v80, v80\n"
      "v_dot2c_f32_bf16 v89, s86, v84\n"
      "v_dot2c_f32_bf16 v91, v84, v84\n"
      "v_dot2c_f32_bf16 v88, s86, v81\n"
      "v_dot2c_f32_bf16 v90, v81, v81\n"
      "v_dot2c_f32_bf16 v89, s86, v85\n"
      "v_dot2c_f32_bf16 v91, v85, v85\n"
      "v_dot2c_f32_bf16 v88, s86, v82\n"
      "v_dot2c_f32_bf16 v90, v82, v82\n"
      "v_dot2c_f32_bf16 v89, s86, v86\n"
      "v_dot2c_f32_bf16 v91, v86, v86\n"
      "v_dot2c_f32_bf16 v88, s86, v83\n"
      "v_dot2c_f32_bf16 v90, v83, v83\n"
      "v_dot2c_f32_bf16 v89, s86, v87\n"
      "v_dot2c_f32_bf16 v91, v87, v87\n"
      "s_nop 1\n"
      "v_permlane32_swap_b32 v88, v89\n"
      "v_permlane32_swap_b32 v90, v91\n"
      "v_add_f32 v88, v88, v89\n"
      "v_add_f32 v90, v90, v91\n"
      "s_nop 1\n"
      "v_permlane16_swap_b32 v88, v90\n"
      "v_add_f32 v92, v88, v90\n"
      "s_nop 1\n"
      "v_add_f32_dpp v92, v92, v92 row_ror:8 row_mask:0xf bank_mask:0xf\n"
      "s_nop 1\n"
      "v_add_f32_dpp v92, v92, v92 row_ror:4 row_mask:0xf bank_mask:0xf\n"
      "s_nop 1\n"
      "v_add_f32_dpp v92, v92, v92 row_ror:2 row_mask:0xf bank_mask:0xf\n"
      "s_nop 1\n"
      "v_add_f32_dpp v92, v92, v92 row_ror:1 row_mask:0xf bank_mask:0xf\n"
      "s_mov_b32 exec_lo, 0x10001\n"
      "s_mov_b32 exec_hi, 0x10001\n"
      "buffer_store_dwordx2 v[92:93], v24, s[44:47], s85 offen sc1\n"
      "s_mov_b64 exec, -1\n"
      "s_cmp_gt_u32 s66, 10\n"
      "s_cbranch_scc1 .Lp1VVV_3_%=\n"
      "s_cmp_eq_u32 s66, 10\n"
      "s_cbranch_scc1 .Lp1QQV_2_%=\n"
      ".Lp1QQQ_1_%=:\n"
      "s_waitcnt vmcnt(8)\n"
      "s_barrier\n"
      "ds_read_b128 v[244:247], v200\n"
      "ds_read_b64_tr_b16 v[56:57], v192\n"
      "ds_read_b64_tr_b16 v[58:59], v192 offset:768\n"
      "ds_read_b64_tr_b16 v[60:61], v192 offset:64\n"
      "ds_read_b64_tr_b16 v[62:63], v192 offset:832\n"
      "ds_read_b64_tr_b16 v[64:65], v192 offset:128\n"
      "ds_read_b64_tr_b16 v[66:67], v192 offset:896\n"
      "ds_read_b128 v[248:251], v201\n"
      "ds_read_b64_tr_b16 v[68:69], v193\n"
      "ds_read_b64_tr_b16 v[70:71], v193 offset:768\n"
      "s_waitcnt lgkmcnt(7)\n"
      "v_mfma_f32_32x32x16_bf16 v[80:95], v[56:59], v[244:247], 0\n"
      "s_add_u32 s89, s87, 2\n"
      "s_and_b32 s89, s89, 7\n"
      "s_lshl_b32 s88, s89, 7\n"
      "s_add_u32 s88, s88, s81\n"
      "s_add_u32 m0, s82, 57344\n"
      "s_add_u32 s77, s88, 0\n"
      "buffer_load_dwordx4 v29, s[24:27], s77 offen lds\n"
      "s_mov_b32 s36, s10\n"
      "s_and_b32 s37, s11, 65535\n"
      "v_mfma_f32_16x16x32_bf16 v[172:175], v[168:171], v[56:59], 0\n"
      "ds_read_b64_tr_b16 v[72:73], v193 offset:64\n"
      "ds_read_b64_tr_b16 v[74:75], v193 offset:832\n"
      "s_waitcnt lgkmcnt(7)\n"
      "v_mfma_f32_32x32x16_bf16 v[96:111], v[60:63], v[244:247], 0\n"
      "s_add_u32 m0, s82, 58368\n"
      "s_add_u32 s77, s88, 8192\n"
      "buffer_load_dwordx4 v30, s[24:27], s77 offen lds\n"
      "s_mov_b32 s38, 5636096\n"
      "s_mov_b32 s39, 131072\n"
      "v_mfma_f32_16x16x32_bf16 v[176:179], v[168:171], v[60:63], 0\n"
      "ds_read_b64_tr_b16 v[76:77], v193 offset:128\n"
      "ds_read_b64_tr_b16 v[78:79], v193 offset:896\n"
      "s_waitcnt lgkmcnt(7)\n"
      "v_mfma_f32_32x32x16_bf16 v[112:127], v[64:67], v[244:247], 0\n"
      "s_and_b32 s75, s64, 7\n"
      "s_lshl_b32 s69, s79, 3\n"
      "v_mfma_f32_16x16x32_bf16 v[180:183], v[168:171], v[64:67], 0\n"
      "ds_read_b128 v[244:247], v202\n"
      "ds_read_b64_tr_b16 v[56:57], v194\n"
      "ds_read_b64_tr_b16 v[58:59], v194 offset:768\n"
      "s_waitcnt lgkmcnt(7)\n"
      "v_mfma_f32_32x32x16_bf16 v[80:95], v[68:71], v[248:251], v[80:95]\n"
      "s_add_u32 m0, s82, 59392\n"
      "s_add_u32 s77, s88, 16384\n"
      "buffer_load_dwordx4 v29, s[24:27], s77 offen lds\n"
      "s_add_u32 s69, s69, s75\n"
      "s_lshr_b32 s74, s64, 3\n"
      "ds_read_b64_tr_b16 v[60:61], v194 offset:64\n"
      "ds_read_b64_tr_b16 v[62:63], v194 offset:832\n"
      "s_waitcnt lgkmcnt(7)\n"
      "v_mfma_f32_32x32x16_bf16 v[96:111], v[72:75], v[248:251], v[96:111]\n"
      "s_add_u32 m0, s82, 60416\n"
      "s_add_u32 s77, s88, 24576\n"
      "buffer_load_dwordx4 v30, s[24:27], s77 offen lds\n"
      "s_lshl_b32 s70, s80, 2\n"
      "s_add_u32 s70, s70, s74\n"
      "ds_read_b64_tr_b16 v[64:65], v194 offset:128\n"
      "ds_read_b64_tr_b16 v[66:67], v194 offset:896\n"
      "s_waitcnt lgkmcnt(7)\n"
      "v_mfma_f32_32x32x16_bf16 v[112:127], v[76:79], v[248:251], v[112:127]\n"
      "s_cmp_eq_u32 s79, 3\n"
      "s_cselect_b32 s74, 4, 6\n"
      "s_min_u32 s73, s75, s74\n"
      "ds_read_b128 v[248:251], v203\n"
      "ds_read_b64_tr_b16 v[68:69], v195\n"
      "ds_read_b64_tr_b16 v[70:71], v195 offset:768\n"
      "s_waitcnt lgkmcnt(7)\n"
      "v_mfma_f32_32x32x16_bf16 v[80:95], v[56:59], v[244:247], v[80:95]\n"
      "s_add_u32 m0, s84, 57344\n"
      "s_mul_i32 s78, s89, 196608\n"
      "s_add_u32 s78, s78, s83\n"
      "buffer_load_dwordx4 v26, s[28:31], s78 offen lds\n"
      "s_mul_i32 s71, s79, 22\n"
      "s_lshl_b32 s76, s75, 1\n"
      "ds_read_b64_tr_b16 v[72:73], v195 offset:64\n"
      "ds_read_b64_tr_b16 v[74:75], v195 offset:832\n"
      "s_waitcnt lgkmcnt(7)\n"
      "v_mfma_f32_32x32x16_bf16 v[96:111], v[60:63], v[244:247], v[96:111]\n"
      "buffer_load_dwordx4 v27, s[28:31], s78 offen offset:1024 lds\n"
      "s_add_u32 s71, s71, s76\n"
      "s_add_u32 s71, s71, s73\n"
      "ds_read_b64_tr_b16 v[76:77], v195 offset:128\n"
      "ds_read_b64_tr_b16 v[78:79], v195 offset:896\n"
      "s_waitcnt lgkmcnt(7)\n"
      "v_mfma_f32_32x32x16_bf16 v[112:127], v[64:67], v[244:247], v[112:127]\n"
      "s_cmp_lt_u32 s75, s74\n"
      "s_cselect_b32 s72, 0, 1073741824\n"
      "s_mul_i32 s90, %[wg], 22016\n"
      "s_waitcnt lgkmcnt(4)\n"
      "v_mfma_f32_32x32x16_bf16 v[80:95], v[68:71], v[248:251], v[80:95]\n"
      "buffer_load_dwordx4 v28, s[28:31], s78 offen offset:2048 lds\n"
      "s_mul_i32 s76, s62, 704\n"
      "s_add_u32 s90, s90, s76\n"
      "s_waitcnt vmcnt(8) lgkmcnt(0)\n"
      "s_barrier\n"
      "ds_read_b128 v[244:247], v200 offset:28672\n"
      "ds_read_b64_tr_b16 v[56:57], v192 offset:28672\n"
      "ds_read_b64_tr_b16 v[58:59], v192 offset:29440\n"
      "ds_read_b64_tr_b16 v[60:61], v192 offset:28736\n"
      "ds_read_b64_tr_b16 v[62:63], v192 offset:29504\n"
      "ds_read_b64_tr_b16 v[64:65], v192 offset:28800\n"
      "ds_read_b64_tr_b16 v[66:67], v192 offset:29568\n"
      "v_mfma_f32_32x32x16_bf16 v[96:111], v[72:75], v[248:251], v[96:111]\n"
      "s_mov_b32 s32, s8\n"
      "s_and_b32 s33, s9, 65535\n"
      "v_mfma_f32_32x32x16_bf16 v[112:127], v[76:79], v[248:251], v[112:127]\n"
      "s_mov_b32 s34, 524288\n"
      "s_mov_b32 s35, 131072\n"
      "ds_read_b128 v[248:251], v201 offset:28672\n"
      "ds_read_b64_tr_b16 v[68:69], v193 offset:28672\n"
      "ds_read_b64_tr_b16 v[70:71], v193 offset:29440\n"
      "s_waitcnt lgkmcnt(7)\n"
      "v_mfma_f32_32x32x16_bf16 v[80:95], v[56:59], v[244:247], v[80:95]\n"
      "s_add_u32 s89, s87, 3\n"
      "s_and_b32 s89, s89, 7\n"
      "s_lshl_b32 s88, s89, 7\n"
      "s_add_u32 s88, s88, s81\n"
      "s_add_u32 m0, s82, 86016\n"
      "s_add_u32 s77, s88, 0\n"
      "buffer_load_dwordx4 v29, s[24:27], s77 offen lds\n"
      "s_and_b32 s73, s69, 7\n"
      "s_lshl_b32 s95, s73, 16\n"
      "v_mfma_f32_16x16x32_bf16 v[172:175], v[168:171], v[56:59], v[172:175]\n"
      "ds_read_b64_tr_b16 v[72:73], v193 offset:28736\n"
      "ds_read_b64_tr_b16 v[74:75], v193 offset:29504\n"
      "s_waitcnt lgkmcnt(7)\n"
      "v_mfma_f32_32x32x16_bf16 v[96:111], v[60:63], v[244:247], v[96:111]\n"
      "s_add_u32 m0, s82, 87040\n"
      "s_add_u32 s77, s88, 8192\n"
      "buffer_load_dwordx4 v30, s[24:27], s77 offen lds\n"
      "s_lshl_b32 s76, s62, 14\n"
      "s_add_u32 s95, s95, s76\n"
      "v_mfma_f32_16x16x32_bf16 v[176:179], v[168:171], v[60:63], v[176:179]\n"
      "ds_read_b64_tr_b16 v[76:77], v193 offset:28800\n"
      "ds_read_b64_tr_b16 v[78:79], v193 offset:29568\n"
      "s_waitcnt lgkmcnt(7)\n"
      "v_mfma_f32_32x32x16_bf16 v[112:127], v[64:67], v[244:247], v[112:127]\n"
      "s_lshl_b32 s76, s70, 7\n"
      "s_add_u32 s95, s95, s76\n"
      "v_mfma_f32_16x16x32_bf16 v[180:183], v[168:171], v[64:67], v[180:183]\n"
      "ds_read_b128 v[244:247], v202 offset:28672\n"
      "ds_read_b64_tr_b16 v[56:57], v194 offset:28672\n"
      "ds_read_b64_tr_b16 v[58:59], v194 offset:29440\n"
      "s_waitcnt lgkmcnt(7)\n"
      "v_mfma_f32_32x32x16_bf16 v[80:95], v[68:71], v[248:251], v[80:95]\n"
      "s_add_u32 m0, s82, 88064\n"
      "s_add_u32 s77, s88, 16384\n"
      "buffer_load_dwordx4 v29, s[24:27], s77 offen lds\n"
      "s_mul_i32 s91, s62, 12288\n"
      "s_add_u32 s91, s91, 114688\n"
      "ds_read_b64_tr_b16 v[60:61], v194 offset:28736\n"
      "ds_read_b64_tr_b16 v[62:63], v194 offset:29504\n"
      "s_waitcnt lgkmcnt(7)\n"
      "v_mfma_f32_32x32x16_bf16 v[96:111], v[72:75], v[248:251], v[96:111]\n"
      "s_add_u32 m0, s82, 89088\n"
      "s_add_u32 s77, s88, 24576\n"
      "buffer_load_dwordx4 v30, s[24:27], s77 offen lds\n"
      "s_lshl_b32 s92, s62, 14\n"
      "s_mov_b32 s40, s14\n"
      "ds_read_b64_tr_b16 v[64:65], v194 offset:28800\n"
      "ds_read_b64_tr_b16 v[66:67], v194 offset:29568\n"
      "s_waitcnt lgkmcnt(7)\n"
      "v_mfma_f32_32x32x16_bf16 v[112:127], v[76:79], v[248:251], v[112:127]\n"
      "s_and_b32 s41, s15, 65535\n"
      "s_mov_b32 s42, 5636096\n"
      "ds_read_b128 v[248:251], v203 offset:28672\n"
      "ds_read_b64_tr_b16 v[68:69], v195 offset:28672\n"
      "ds_read_b64_tr_b16 v[70:71], v195 offset:29440\n"
      "s_waitcnt lgkmcnt(7)\n"
      "v_mfma_f32_32x32x16_bf16 v[80:95], v[56:59], v[244:247], v[80:95]\n"
      "s_add_u32 m0, s84, 86016\n"
      "s_mul_i32 s78, s89, 196608\n"
      "s_add_u32 s78, s78, s83\n"
      "buffer_load_dwordx4 v26, s[28:31], s78 offen lds\n"
      "s_mov_b32 s43, 131072\n"
      "s_mul_i32 s93, s62, 1441792\n"
      "ds_read_b64_tr_b16 v[72:73], v195 offset:28736\n"
      "ds_read_b64_tr_b16 v[74:75], v195 offset:29504\n"
      "s_waitcnt lgkmcnt(7)\n"
      "v_mfma_f32_32x32x16_bf16 v[96:111], v[60:63], v[244:247], v[96:111]\n"
      "buffer_load_dwordx4 v27, s[28:31], s78 offen offset:1024 lds\n"
      "s_lshl_b32 s76, s69, 7\n"
      "s_add_u32 s93, s93, s76\n"
      "ds_read_b64_tr_b16 v[76:77], v195 offset:28800\n"
      "ds_read_b64_tr_b16 v[78:79], v195 offset:29568\n"
      "s_waitcnt lgkmcnt(7)\n"
      "v_mfma_f32_32x32x16_bf16 v[112:127], v[64:67], v[244:247], v[112:127]\n"
      "s_mov_b32 s52, s12\n"
      "s_and_b32 s53, s13, 65535\n"
      "s_waitcnt lgkmcnt(4)\n"
      "v_mfma_f32_32x32x16_bf16 v[80:95], v[68:71], v[248:251], v[80:95]\n"
      "buffer_load_dwordx4 v28, s[28:31], s78 offen offset:2048 lds\n"
      "s_mov_b32 s54, 2752\n"
      "s_mov_b32 s55, 131072\n"
      "s_waitcnt vmcnt(7) lgkmcnt(0)\n"
      "s_barrier\n"
      "ds_read_b128 v[244:247], v204\n"
      "ds_read_b64_tr_b16 v[56:57], v196\n"
      "ds_read_b64_tr_b16 v[58:59], v196 offset:768\n"
      "ds_read_b64_tr_b16 v[60:61], v196 offset:64\n"
      "ds_read_b64_tr_b16 v[62:63], v196 offset:832\n"
      "ds_read_b64_tr_b16 v[64:65], v196 offset:128\n"
      "ds_read_b64_tr_b16 v[66:67], v196 offset:896\n"
      "v_mfma_f32_32x32x16_bf16 v[96:111], v[72:75], v[248:251], v[96:111]\n"
      "s_mov_b32 s56, s16\n"
      "s_and_b32 s57, s17, 65535\n"
      "v_mfma_f32_32x32x16_bf16 v[112:127], v[76:79], v[248:251], v[112:127]\n"
      "s_mov_b32 s58, 4096\n"
      "s_mov_b32 s59, 131072\n"
      "ds_read_b128 v[248:251], v205\n"
      "ds_read_b64_tr_b16 v[68:69], v197\n"
      "ds_read_b64_tr_b16 v[70:71], v197 offset:768\n"
      "s_waitcnt lgkmcnt(7)\n"
      "v_mfma_f32_32x32x16_bf16 v[80:95], v[56:59], v[244:247], v[80:95]\n"
      "s_add_u32 s89, s87, 4\n"
      "s_and_b32 s89, s89, 7\n"
      "s_lshl_b32 s88, s89, 7\n"
      "s_add_u32 s88, s88, s81\n"
      "s_add_u32 m0, s82, 0\n"
      "s_add_u32 s77, s88, 0\n"
      "buffer_load_dwordx4 v29, s[24:27], s77 offen lds\n"
      "s_mov_b32 s48, s18\n"
      "s_and_b32 s49, s19, 65535\n"
      "v_mfma_f32_16x16x32_bf16 v[172:175], v[168:171], v[56:59], v[172:175]\n"
      "ds_read_b64_tr_b16 v[72:73], v197 offset:64\n"
      "ds_read_b64_tr_b16 v[74:75], v197 offset:832\n"
      "s_waitcnt lgkmcnt(7)\n"
      "v_mfma_f32_32x32x16_bf16 v[96:111], v[60:63], v[244:247], v[96:111]\n"
      "s_add_u32 m0, s82, 1024\n"
      "s_add_u32 s77, s88, 8192\n"
      "buffer_load_dwordx4 v30, s[24:27], s77 offen lds\n"
      "s_mov_b32 s50, 2097152\n"
      "s_mov_b32 s51, 131072\n"
      "v_mfma_f32_16x16x32_bf16 v[176:179], v[168:171], v[60:63], v[176:179]\n"
      "ds_read_b64_tr_b16 v[76:77], v197 offset:128\n"
      "ds_read_b64_tr_b16 v[78:79], v197 offset:896\n"
      "s_waitcnt lgkmcnt(7)\n"
      "v_mfma_f32_32x32x16_bf16 v[112:127], v[64:67], v[244:247], v[112:127]\n"
      "s_lshl_b32 s13, s65, 13\n"
      "s_lshl_b32 s76, s66, 9\n"
      "v_mfma_f32_16x16x32_bf16 v[180:183], v[168:171], v[64:67], v[180:183]\n"
      "ds_read_b128 v[244:247], v206\n"
      "ds_read_b64_tr_b16 v[56:57], v198\n"
      "ds_read_b64_tr_b16 v[58:59], v198 offset:768\n"
      "s_waitcnt lgkmcnt(7)\n"
      "v_mfma_f32_32x32x16_bf16 v[80:95], v[68:71], v[248:251], v[80:95]\n"
      "s_add_u32 m0, s82, 2048\n"
      "s_add_u32 s77, s88, 16384\n"
      "buffer_load_dwordx4 v29, s[24:27], s77 offen lds\n"
      "s_add_u32 s13, s13, s76\n"
      "s_lshl_b32 s76, s62, 7\n"
      "ds_read_b64_tr_b16 v[60:61], v198 offset:64\n"
      "ds_read_b64_tr_b16 v[62:63], v198 offset:832\n"
      "s_waitcnt lgkmcnt(7)\n"
      "v_mfma_f32_32x32x16_bf16 v[96:111], v[72:75], v[248:251], v[96:111]\n"
      "s_add_u32 m0, s82, 3072\n"
      "s_add_u32 s77, s88, 24576\n"
      "buffer_load_dwordx4 v30, s[24:27], s77 offen lds\n"
      "s_add_u32 s13, s13, s76\n"
      "s_lshl_b32 s73, s65, 12\n"
      "ds_read_b64_tr_b16 v[64:65], v198 offset:128\n"
      "ds_read_b64_tr_b16 v[66:67], v198 offset:896\n"
      "s_waitcnt lgkmcnt(7)\n"
      "v_mfma_f32_32x32x16_bf16 v[112:127], v[76:79], v[248:251], v[112:127]\n"
      "s_lshl_b32 s76, s62, 10\n"
      "s_add_u32 s73, s73, s76\n"
      "ds_read_b128 v[248:251], v207\n"
      "ds_read_b64_tr_b16 v[68:69], v199\n"
      "ds_read_b64_tr_b16 v[70:71], v199 offset:768\n"
      "s_waitcnt lgkmcnt(7)\n"
      "v_mfma_f32_32x32x16_bf16 v[80:95], v[56:59], v[244:247], v[80:95]\n"
      "s_add_u32 m0, s84, 0\n"
      "s_mul_i32 s78, s89, 196608\n"
      "s_add_u32 s78, s78, s83\n"
      "buffer_load_dwordx4 v26, s[28:31], s78 offen lds\n"
      "s_mul_i32 s76, s66, 393216\n"
      "s_add_u32 s73, s73, s76\n"
      "ds_read_b64_tr_b16 v[72:73], v199 offset:64\n"
      "ds_read_b64_tr_b16 v[74:75], v199 offset:832\n"
      "s_waitcnt lgkmcnt(7)\n"
      "v_mfma_f32_32x32x16_bf16 v[96:111], v[60:63], v[244:247], v[96:111]\n"
      "buffer_load_dwordx4 v27, s[28:31], s78 offen offset:1024 lds\n"
      "s_add_u32 s73, s73, 1048576\n"
      "s_mul_i32 s74, s66, 3\n"
      "ds_read_b64_tr_b16 v[76:77], v199 offset:128\n"
      "ds_read_b64_tr_b16 v[78:79], v199 offset:896\n"
      "s_waitcnt lgkmcnt(7)\n"
      "v_mfma_f32_32x32x16_bf16 v[112:127], v[64:67], v[244:247], v[112:127]\n"
      "s_add_u32 s4, s73, 0\n"
      "s_add_u32 s5, s4, 65536\n"
      "s_waitcnt lgkmcnt(4)\n"
      "v_mfma_f32_32x32x16_bf16 v[80:95], v[68:71], v[248:251], v[80:95]\n"
      "buffer_load_dwordx4 v28, s[28:31], s78 offen offset:2048 lds\n"
      "s_cmp_lt_u32 s74, 16\n"
      "s_cselect_b32 s10, 1052289595, 1065353216\n"
      "s_add_u32 s6, s73, 131072\n"
      "s_waitcnt vmcnt(7) lgkmcnt(0)\n"
      "s_barrier\n"
      "ds_read_b128 v[244:247], v204 offset:28672\n"
      "ds_read_b64_tr_b16 v[56:57], v196 offset:28672\n"
      "ds_read_b64_tr_b16 v[58:59], v196 offset:29440\n"
      "ds_read_b64_tr_b16 v[60:61], v196 offset:28736\n"
      "ds_read_b64_tr_b16 v[62:63], v196 offset:29504\n"
      "ds_read_b64_tr_b16 v[64:65], v196 offset:28800\n"
      "ds_read_b64_tr_b16 v[66:67], v196 offset:29568\n"
      "v_mfma_f32_32x32x16_bf16 v[96:111], v[72:75], v[248:251], v[96:111]\n"
      "s_add_u32 s7, s6, 65536\n"
      "s_cmp_lt_u32 s74, 15\n"
      "s_cselect_b32 s11, 1052289595, 1065353216\n"
      "v_mfma_f32_32x32x16_bf16 v[112:127], v[76:79], v[248:251], v[112:127]\n"
      "s_add_u32 s8, s73, 262144\n"
      "s_add_u32 s9, s8, 65536\n"
      "ds_read_b128 v[248:251], v205 offset:28672\n"
      "ds_read_b64_tr_b16 v[68:69], v197 offset:28672\n"
      "ds_read_b64_tr_b16 v[70:71], v197 offset:29440\n"
      "s_waitcnt lgkmcnt(7)\n"
      "v_mfma_f32_32x32x16_bf16 v[80:95], v[56:59], v[244:247], v[80:95]\n"
      "s_add_u32 s89, s87, 5\n"
      "s_and_b32 s89, s89, 7\n"
      "s_lshl_b32 s88, s89, 7\n"
      "s_add_u32 s88, s88, s81\n"
      "s_add_u32 m0, s82, 28672\n"
      "s_add_u32 s77, s88, 0\n"
      "buffer_load_dwordx4 v29, s[24:27], s77 offen lds\n"
      "s_cmp_lt_u32 s74, 14\n"
      "s_cselect_b32 s12, 1052289595, 1065353216\n"
      "s_lshl_b32 s14, %[wg], 8\n"
      "v_mfma_f32_16x16x32_bf16 v[172:175], v[168:171], v[56:59], v[172:175]\n"
      "ds_read_b64_tr_b16 v[72:73], v197 offset:28736\n"
      "ds_read_b64_tr_b16 v[74:75], v197 offset:29504\n"
      "s_waitcnt lgkmcnt(7)\n"
      "v_mfma_f32_32x32x16_bf16 v[96:111], v[60:63], v[244:247], v[96:111]\n"
      "s_add_u32 m0, s82, 29696\n"
      "s_add_u32 s77, s88, 8192\n"
      "buffer_load_dwordx4 v30, s[24:27], s77 offen lds\n"
      "s_mul_i32 s76, s62, 1441792\n"
      "s_add_u32 s14, s14, s76\n"
      "v_mfma_f32_16x16x32_bf16 v[176:179], v[168:171], v[60:63], v[176:179]\n"
      "ds_read_b64_tr_b16 v[76:77], v197 offset:28800\n"
      "ds_read_b64_tr_b16 v[78:79], v197 offset:29568\n"
      "s_waitcnt lgkmcnt(7)\n"
      "v_mfma_f32_32x32x16_bf16 v[112:127], v[64:67], v[244:247], v[112:127]\n"
      "s_add_u32 s14, s14, 14680064\n"
      "s_lshl_b32 s15, %[wg], 7\n"
      "v_mfma_f32_16x16x32_bf16 v[180:183], v[168:171], v[64:67], v[180:183]\n"
      "ds_read_b128 v[244:247], v206 offset:28672\n"
      "ds_read_b64_tr_b16 v[56:57], v198 offset:28672\n"
      "ds_read_b64_tr_b16 v[58:59], v198 offset:29440\n"
      "s_waitcnt lgkmcnt(7)\n"
      "v_mfma_f32_32x32x16_bf16 v[80:95], v[68:71], v[248:251], v[80:95]\n"
      "s_add_u32 m0, s82, 30720\n"
      "s_add_u32 s77, s88, 16384\n"
      "buffer_load_dwordx4 v29, s[24:27], s77 offen lds\n"
      "s_lshl_b32 s76, s62, 15\n"
      "s_add_u32 s15, s15, s76\n"
      "ds_read_b64_tr_b16 v[60:61], v198 offset:28736\n"
      "ds_read_b64_tr_b16 v[62:63], v198 offset:29504\n"
      "s_waitcnt lgkmcnt(7)\n"
      "v_mfma_f32_32x32x16_bf16 v[96:111], v[72:75], v[248:251], v[96:111]\n"
      "s_add_u32 m0, s82, 31744\n"
      "s_add_u32 s77, s88, 24576\n"
      "buffer_load_dwordx4 v30, s[24:27], s77 offen lds\n"
      "s_and_b32 s74, s64, 3\n"
      "s_lshl_b32 s67, s63, 2\n"
      "ds_read_b64_tr_b16 v[64:65], v198 offset:28800\n"
      "ds_read_b64_tr_b16 v[66:67], v198 offset:29568\n"
      "s_waitcnt lgkmcnt(7)\n"
      "v_mfma_f32_32x32x16_bf16 v[112:127], v[76:79], v[248:251], v[112:127]\n"
      "s_add_u32 s67, s67, s74\n"
      "s_min_u32 s22, s68, 1\n"
      "ds_read_b128 v[248:251], v207 offset:28672\n"
      "ds_read_b64_tr_b16 v[68:69], v199 offset:28672\n"
      "ds_read_b64_tr_b16 v[70:71], v199 offset:29440\n"
      "s_waitcnt lgkmcnt(7)\n"
      "v_mfma_f32_32x32x16_bf16 v[80:95], v[56:59], v[244:247], v[80:95]\n"
      "s_add_u32 m0, s84, 28672\n"
      "s_mul_i32 s78, s89, 196608\n"
      "s_add_u32 s78, s78, s83\n"
      "buffer_load_dwordx4 v26, s[28:31], s78 offen lds\n"
      "s_lshl_b32 s97, s68, 3\n"
      "s_lshl_b32 s76, s62, 1\n"
      "ds_read_b64_tr_b16 v[72:73], v199 offset:28736\n"
      "ds_read_b64_tr_b16 v[74:75], v199 offset:29504\n"
      "s_waitcnt lgkmcnt(7)\n"
      "v_mfma_f32_32x32x16_bf16 v[96:111], v[60:63], v[244:247], v[96:111]\n"
      "buffer_load_dwordx4 v27, s[28:31], s78 offen offset:1024 lds\n"
      "s_add_u32 s97, s97, s76\n"
      "s_lshl_b32 s23, s22, 3\n"
      "ds_read_b64_tr_b16 v[76:77], v199 offset:28800\n"
      "ds_read_b64_tr_b16 v[78:79], v199 offset:29568\n"
      "s_waitcnt lgkmcnt(7)\n"
      "v_mfma_f32_32x32x16_bf16 v[112:127], v[64:67], v[244:247], v[112:127]\n"
      "s_sub_u32 s23, s97, s23\n"
      "s_lshl_b32 s16, s67, 16\n"
      "s_waitcnt lgkmcnt(4)\n"
      "v_mfma_f32_32x32x16_bf16 v[80:95], v[68:71], v[248:251], v[80:95]\n"
      "buffer_load_dwordx4 v28, s[28:31], s78 offen offset:2048 lds\n"
      "s_lshl_b32 s76, s23, 10\n"
      "s_add_u32 s16, s16, s76\n"
      "s_waitcnt vmcnt(7) lgkmcnt(0)\n"
      "s_barrier\n"
      "ds_read_b128 v[244:247], v200\n"
      "ds_read_b64_tr_b16 v[56:57], v192\n"
      "ds_read_b64_tr_b16 v[58:59], v192 offset:768\n"
      "ds_read_b64_tr_b16 v[60:61], v192 offset:64\n"
      "ds_read_b64_tr_b16 v[62:63], v192 offset:832\n"
      "ds_read_b64_tr_b16 v[64:65], v192 offset:128\n"
      "ds_read_b64_tr_b16 v[66:67], v192 offset:896\n"
      "v_mfma_f32_32x32x16_bf16 v[96:111], v[72:75], v[248:251], v[96:111]\n"
      "s_add_u32 s16, s16, 3145728\n"
      "s_add_u32 s17, s16, 2097152\n"
      "v_mfma_f32_32x32x16_bf16 v[112:127], v[76:79], v[248:251], v[112:127]\n"
      "s_lshl_b32 s18, s67, 16\n"
      "s_lshl_b32 s76, s97, 10\n"
      "ds_read_b128 v[248:251], v201\n"
      "ds_read_b64_tr_b16 v[68:69], v193\n"
      "ds_read_b64_tr_b16 v[70:71], v193 offset:768\n"
      "s_waitcnt lgkmcnt(7)\n"
      "v_mfma_f32_32x32x16_bf16 v[80:95], v[56:59], v[244:247], v[80:95]\n"
      "s_add_u32 s89, s87, 6\n"
      "s_and_b32 s89, s89, 7\n"
      "s_lshl_b32 s88, s89, 7\n"
      "s_add_u32 s88, s88, s81\n"
      "s_add_u32 m0, s82, 57344\n"
      "s_add_u32 s77, s88, 0\n"
      "buffer_load_dwordx4 v29, s[24:27], s77 offen lds\n"
      "s_add_u32 s18, s18, s76\n"
      "s_add_u32 s18, s18, 1048576\n"
      "v_mfma_f32_16x16x32_bf16 v[172:175], v[168:171], v[56:59], v[172:175]\n"
      "ds_read_b64_tr_b16 v[72:73], v193 offset:64\n"
      "ds_read_b64_tr_b16 v[74:75], v193 offset:832\n"
      "s_waitcnt lgkmcnt(7)\n"
      "v_mfma_f32_32x32x16_bf16 v[96:111], v[60:63], v[244:247], v[96:111]\n"
      "s_add_u32 m0, s82, 58368\n"
      "s_add_u32 s77, s88, 8192\n"
      "buffer_load_dwordx4 v30, s[24:27], s77 offen lds\n"
      "s_lshl_b32 s19, s62, 11\n"
      "s_add_u32 s19, s19, 65536\n"
      "v_mfma_f32_16x16x32_bf16 v[176:179], v[168:171], v[60:63], v[176:179]\n"
      "ds_read_b64_tr_b16 v[76:77], v193 offset:128\n"
      "ds_read_b64_tr_b16 v[78:79], v193 offset:896\n"
      "s_waitcnt lgkmcnt(7)\n"
      "v_mfma_f32_32x32x16_bf16 v[112:127], v[64:67], v[244:247], v[112:127]\n"
      "s_lshl_b32 s94, s69, 10\n"
      "s_lshl_b32 s76, s70, 7\n"
      "v_mfma_f32_16x16x32_bf16 v[180:183], v[168:171], v[64:67], v[180:183]\n"
      "ds_read_b128 v[244:247], v202\n"
      "ds_read_b64_tr_b16 v[56:57], v194\n"
      "ds_read_b64_tr_b16 v[58:59], v194 offset:768\n"
      "s_waitcnt lgkmcnt(7)\n"
      "v_mfma_f32_32x32x16_bf16 v[80:95], v[68:71], v[248:251], v[80:95]\n"
      "s_add_u32 m0, s82, 59392\n"
      "s_add_u32 s77, s88, 16384\n"
      "buffer_load_dwordx4 v29, s[24:27], s77 offen lds\n"
      "s_add_u32 s94, s94, s76\n"
      "s_lshl_b32 s76, s62, 5\n"
      "ds_read_b64_tr_b16 v[60:61], v194 offset:64\n"
      "ds_read_b64_tr_b16 v[62:63], v194 offset:832\n"
      "s_waitcnt lgkmcnt(7)\n"
      "v_mfma_f32_32x32x16_bf16 v[96:111], v[72:75], v[248:251], v[96:111]\n"
      "s_add_u32 m0, s82, 60416\n"
      "s_add_u32 s77, s88, 24576\n"
      "buffer_load_dwordx4 v30, s[24:27], s77 offen lds\n"
      "s_add_u32 s94, s94, s76\n"
      "ds_read_b64_tr_b16 v[64:65], v194 offset:128\n"
      "ds_read_b64_tr_b16 v[66:67], v194 offset:896\n"
      "s_waitcnt lgkmcnt(7)\n"
      "v_mfma_f32_32x32x16_bf16 v[112:127], v[76:79], v[248:251], v[112:127]\n"
      "ds_read_b128 v[248:251], v203\n"
      "ds_read_b64_tr_b16 v[68:69], v195\n"
      "ds_read_b64_tr_b16 v[70:71], v195 offset:768\n"
      "s_waitcnt lgkmcnt(7)\n"
      "v_mfma_f32_32x32x16_bf16 v[80:95], v[56:59], v[244:247], v[80:95]\n"
      "s_add_u32 m0, s84, 57344\n"
      "s_mul_i32 s78, s89, 196608\n"
      "s_add_u32 s78, s78, s83\n"
      "buffer_load_dwordx4 v26, s[28:31], s78 offen lds\n"
      "ds_read_b64_tr_b16 v[72:73], v195 offset:64\n"
      "ds_read_b64_tr_b16 v[74:75], v195 offset:832\n"
      "s_waitcnt lgkmcnt(7)\n"
      "v_mfma_f32_32x32x16_bf16 v[96:111], v[60:63], v[244:247], v[96:111]\n"
      "buffer_load_dwordx4 v27, s[28:31], s78 offen offset:1024 lds\n"
      "ds_read_b64_tr_b16 v[76:77], v195 offset:128\n"
      "ds_read_b64_tr_b16 v[78:79], v195 offset:896\n"
      "s_waitcnt lgkmcnt(7)\n"
      "v_mfma_f32_32x32x16_bf16 v[112:127], v[64:67], v[244:247], v[112:127]\n"
      "s_waitcnt lgkmcnt(4)\n"
      "v_mfma_f32_32x32x16_bf16 v[80:95], v[68:71], v[248:251], v[80:95]\n"
      "buffer_load_dwordx4 v28, s[28:31], s78 offen offset:2048 lds\n"
      "buffer_load_dwordx4 v[164:167], v48, s[44:47], 0 offen sc1\n"
      "s_waitcnt vmcnt(8) lgkmcnt(0)\n"
      "s_barrier\n"
      "ds_read_b128 v[244:247], v200 offset:28672\n"
      "ds_read_b64_tr_b16 v[56:57], v192 offset:28672\n"
      "ds_read_b64_tr_b16 v[58:59], v192 offset:29440\n"
      "ds_read_b64_tr_b16 v[60:61], v192 offset:28736\n"
      "ds_read_b64_tr_b16 v[62:63], v192 offset:29504\n"
      "ds_read_b64_tr_b16 v[64:65], v192 offset:28800\n"
      "ds_read_b64_tr_b16 v[66:67], v192 offset:29568\n"
      "v_mfma_f32_32x32x16_bf16 v[96:111], v[72:75], v[248:251], v[96:111]\n"
      "v_mfma_f32_32x32x16_bf16 v[112:127], v[76:79], v[248:251], v[112:127]\n"
      "ds_read_b128 v[248:251], v201 offset:28672\n"
      "ds_read_b64_tr_b16 v[68:69], v193 offset:28672\n"
      "ds_read_b64_tr_b16 v[70:71], v193 offset:29440\n"
      "s_waitcnt lgkmcnt(7)\n"
      "v_mfma_f32_32x32x16_bf16 v[80:95], v[56:59], v[244:247], v[80:95]\n"
      "s_add_u32 s89, s87, 7\n"
      "s_and_b32 s89, s89, 7\n"
      "s_lshl_b32 s88, s89, 7\n"
      "s_add_u32 s88, s88, s81\n"
      "s_add_u32 m0, s82, 86016\n"
      "s_add_u32 s77, s88, 0\n"
      "buffer_load_dwordx4 v29, s[24:27], s77 offen lds\n"
      "v_mfma_f32_16x16x32_bf16 v[172:175], v[168:171], v[56:59], v[172:175]\n"
      "ds_read_b64_tr_b16 v[72:73], v193 offset:28736\n"
      "ds_read_b64_tr_b16 v[74:75], v193 offset:29504\n"
      "s_waitcnt lgkmcnt(7)\n"
      "v_mfma_f32_32x32x16_bf16 v[96:111], v[60:63], v[244:247], v[96:111]\n"
      "s_add_u32 m0, s82, 87040\n"
      "s_add_u32 s77, s88, 8192\n"
      "buffer_load_dwordx4 v30, s[24:27], s77 offen lds\n"
      "v_mfma_f32_16x16x32_bf16 v[176:179], v[168:171], v[60:63], v[176:179]\n"
      "ds_read_b64_tr_b16 v[76:77], v193 offset:28800\n"
      "ds_read_b64_tr_b16 v[78:79], v193 offset:29568\n"
      "s_waitcnt lgkmcnt(7)\n"
      "v_mfma_f32_32x32x16_bf16 v[112:127], v[64:67], v[244:247], v[112:127]\n"
      "v_mfma_f32_16x16x32_bf16 v[180:183], v[168:171], v[64:67], v[180:183]\n"
      "ds_read_b128 v[244:247], v202 offset:28672\n"
      "ds_read_b64_tr_b16 v[56:57], v194 offset:28672\n"
      "ds_read_b64_tr_b16 v[58:59], v194 offset:29440\n"
      "s_waitcnt lgkmcnt(7)\n"
      "v_mfma_f32_32x32x16_bf16 v[80:95], v[68:71], v[248:251], v[80:95]\n"
      "s_add_u32 m0, s82, 88064\n"
      "s_add_u32 s77, s88, 16384\n"
      "buffer_load_dwordx4 v29, s[24:27], s77 offen lds\n"
      "ds_read_b64_tr_b16 v[60:61], v194 offset:28736\n"
      "ds_read_b64_tr_b16 v[62:63], v194 offset:29504\n"
      "s_waitcnt lgkmcnt(7)\n"
      "v_mfma_f32_32x32x16_bf16 v[96:111], v[72:75], v[248:251], v[96:111]\n"
      "s_add_u32 m0, s82, 89088\n"
      "s_add_u32 s77, s88, 24576\n"
      "buffer_load_dwordx4 v30, s[24:27], s77 offen lds\n"
      "ds_read_b64_tr_b16 v[64:65], v194 offset:28800\n"
      "ds_read_b64_tr_b16 v[66:67], v194 offset:29568\n"
      "s_waitcnt lgkmcnt(7)\n"
      "v_mfma_f32_32x32x16_bf16 v[112:127], v[76:79], v[248:251], v[112:127]\n"
      "ds_read_b128 v[248:251], v203 offset:28672\n"
      "ds_read_b64_tr_b16 v[68:69], v195 offset:28672\n"
      "ds_read_b64_tr_b16 v[70:71], v195 offset:29440\n"
      "s_waitcnt lgkmcnt(7)\n"
      "v_mfma_f32_32x32x16_bf16 v[80:95], v[56:59], v[244:247], v[80:95]\n"
      "s_add_u32 m0, s84, 86016\n"
      "s_mul_i32 s78, s89, 196608\n"
      "s_add_u32 s78, s78, s83\n"
      "buffer_load_dwordx4 v26, s[28:31], s78 offen lds\n"
      "ds_read_b64_tr_b16 v[72:73], v195 offset:28736\n"
      "ds_read_b64_tr_b16 v[74:75], v195 offset:29504\n"
      "s_waitcnt lgkmcnt(7)\n"
      "v_mfma_f32_32x32x16_bf16 v[96:111], v[60:63], v[244:247], v[96:111]\n"
      "buffer_load_dwordx4 v27, s[28:31], s78 offen offset:1024 lds\n"
      "ds_read_b64_tr_b16 v[76:77], v195 offset:28800\n"
      "ds_read_b64_tr_b16 v[78:79], v195 offset:29568\n"
      "s_waitcnt lgkmcnt(7)\n"
      "v_mfma_f32_32x32x16_bf16 v[112:127], v[64:67], v[244:247], v[112:127]\n"
      "s_waitcnt lgkmcnt(4)\n"
      "v_mfma_f32_32x32x16_bf16 v[80:95], v[68:71], v[248:251], v[80:95]\n"
      "buffer_load_dwordx4 v28, s[28:31], s78 offen offset:2048 lds\n"
      "s_waitcnt vmcnt(8) lgkmcnt(0)\n"
      "s_barrier\n"
      "ds_read_b128 v[244:247], v204\n"
      "ds_read_b64_tr_b16 v[56:57], v196\n"
      "ds_read_b64_tr_b16 v[58:59], v196 offset:768\n"
      "ds_read_b64_tr_b16 v[60:61], v196 offset:64\n"
      "ds_read_b64_tr_b16 v[62:63], v196 offset:832\n"
      "ds_read_b64_tr_b16 v[64:65], v196 offset:128\n"
      "ds_read_b64_tr_b16 v[66:67], v196 offset:896\n"
      "v_mfma_f32_32x32x16_bf16 v[96:111], v[72:75], v[248:251], v[96:111]\n"
      "v_mfma_f32_32x32x16_bf16 v[112:127], v[76:79], v[248:251], v[112:127]\n"
      "ds_read_b128 v[248:251], v205\n"
      "ds_read_b64_tr_b16 v[68:69], v197\n"
      "ds_read_b64_tr_b16 v[70:71], v197 offset:768\n"
      "s_waitcnt lgkmcnt(7)\n"
      "v_mfma_f32_32x32x16_bf16 v[80:95], v[56:59], v[244:247], v[80:95]\n"
      "s_mov_b64 exec, 65535\n"
      "buffer_load_dword a255, v45, s[32:35], s95 offen\n"
      "s_mov_b64 exec, -1\n"
      "v_mfma_f32_16x16x32_bf16 v[172:175], v[168:171], v[56:59], v[172:175]\n"
      "ds_read_b64_tr_b16 v[72:73], v197 offset:64\n"
      "ds_read_b64_tr_b16 v[74:75], v197 offset:832\n"
      "s_waitcnt lgkmcnt(7)\n"
      "v_mfma_f32_32x32x16_bf16 v[96:111], v[60:63], v[244:247], v[96:111]\n"
      "s_add_u32 m0, s91, 0\n"
      "s_add_u32 s77, s90, 0\n"
      "s_mov_b32 exec_hi, 0xfff\n"
      "buffer_load_dwordx4 v1, s[36:39], s77 offen nt lds\n"
      "s_mov_b32 exec_hi, -1\n"
      "v_mfma_f32_16x16x32_bf16 v[176:179], v[168:171], v[60:63], v[176:179]\n"
      "ds_read_b64_tr_b16 v[76:77], v197 offset:128\n"
      "ds_read_b64_tr_b16 v[78:79], v197 offset:896\n"
      "s_waitcnt lgkmcnt(7)\n"
      "v_mfma_f32_32x32x16_bf16 v[112:127], v[64:67], v[244:247], v[112:127]\n"
      "s_add_u32 m0, s91, 704\n"
      "s_add_u32 s77, s90, 2752\n"
      "s_mov_b32 exec_hi, 0xfff\n"
      "buffer_load_dwordx4 v1, s[36:39], s77 offen nt lds\n"
      "s_mov_b32 exec_hi, -1\n"
      "v_mfma_f32_16x16x32_bf16 v[180:183], v[168:171], v[64:67], v[180:183]\n"
      "ds_read_b128 v[244:247], v206\n"
      "ds_read_b64_tr_b16 v[56:57], v198\n"
      "ds_read_b64_tr_b16 v[58:59], v198 offset:768\n"
      "s_waitcnt lgkmcnt(7)\n"
      "v_mfma_f32_32x32x16_bf16 v[80:95], v[68:71], v[248:251], v[80:95]\n"
      "s_add_u32 m0, s91, 1408\n"
      "s_add_u32 s77, s90, 5504\n"
      "s_mov_b32 exec_hi, 0xfff\n"
      "buffer_load_dwordx4 v1, s[36:39], s77 offen nt lds\n"
      "s_mov_b32 exec_hi, -1\n"
      "ds_read_b64_tr_b16 v[60:61], v198 offset:64\n"
      "ds_read_b64_tr_b16 v[62:63], v198 offset:832\n"
      "s_waitcnt lgkmcnt(7)\n"
      "v_mfma_f32_32x32x16_bf16 v[96:111], v[72:75], v[248:251], v[96:111]\n"
      "s_add_u32 m0, s91, 2112\n"
      "s_add_u32 s77, s90, 8256\n"
      "s_mov_b32 exec_hi, 0xfff\n"
      "buffer_load_dwordx4 v1, s[36:39], s77 offen nt lds\n"
      "s_mov_b32 exec_hi, -1\n"
      "ds_read_b64_tr_b16 v[64:65], v198 offset:128\n"
      "ds_read_b64_tr_b16 v[66:67], v198 offset:896\n"
      "s_waitcnt lgkmcnt(7)\n"
      "v_mfma_f32_32x32x16_bf16 v[112:127], v[76:79], v[248:251], v[112:127]\n"
      "s_add_u32 m0, s91, 2816\n"
      "s_add_u32 s77, s90, 11008\n"
      "s_mov_b32 exec_hi, 0xfff\n"
      "buffer_load_dwordx4 v1, s[36:39], s77 offen nt lds\n"
      "s_mov_b32 exec_hi, -1\n"
      "ds_read_b128 v[248:251], v207\n"
      "ds_read_b64_tr_b16 v[68:69], v199\n"
      "ds_read_b64_tr_b16 v[70:71], v199 offset:768\n"
      "s_waitcnt lgkmcnt(7)\n"
      "v_mfma_f32_32x32x16_bf16 v[80:95], v[56:59], v[244:247], v[80:95]\n"
      "s_add_u32 m0, s91, 3520\n"
      "s_add_u32 s77, s90, 13760\n"
      "s_mov_b32 exec_hi, 0xfff\n"
      "buffer_load_dwordx4 v1, s[36:39], s77 offen nt lds\n"
      "s_mov_b32 exec_hi, -1\n"
      "s_waitcnt vmcnt(14)\n"
      "v_xor_b32 v20, s60, v165\n"
      "v_xor_b32 v21, s61, v167\n"
      "v_or_b32 v20, v20, v21\n"
      "v_cmp_ne_u32_e32 vcc, 0, v20\n"
      "s_cbranch_vccz .Lgok_6_%=\n"
      "s_mov_b32 s96, 131072\n"
      ".Lgspin_5_%=:\n"
      "buffer_load_dwordx4 v[164:167], v48, s[44:47], 0 offen sc1\n"
      "s_waitcnt vmcnt(0)\n"
      "v_xor_b32 v20, s60, v165\n"
      "v_xor_b32 v21, s61, v167\n"
      "v_or_b32 v20, v20, v21\n"
      "v_cmp_ne_u32_e32 vcc, 0, v20\n"
      "s_cbranch_vccz .Lgok_6_%=\n"
      "s_sub_u32 s96, s96, 1\n"
      "s_cbranch_scc0 .Lgspin_5_%=\n"
      ".Lgok_6_%=:\n"
      "v_mul_f32 v21, 989855744, v164\n"
      "v_mul_f32 v20, 989855744, v166\n"
      "ds_read_b64_tr_b16 v[72:73], v199 offset:64\n"
      "ds_read_b64_tr_b16 v[74:75], v199 offset:832\n"
      "s_waitcnt lgkmcnt(7)\n"
      "v_mfma_f32_32x32x16_bf16 v[96:111], v[60:63], v[244:247], v[96:111]\n"
      "s_add_u32 m0, s91, 4224\n"
      "s_add_u32 s77, s90, 16512\n"
      "s_mov_b32 exec_hi, 0xfff\n"
      "buffer_load_dwordx4 v1, s[36:39], s77 offen nt lds\n"
      "s_mov_b32 exec_hi, -1\n"
      "v_fma_f32 v20, -v21, v21, v20\n"
      "v_mul_f32 v43, -0.5, v21\n"
      "v_max_f32 v20, 0, v20\n"
      "ds_read_b64_tr_b16 v[76:77], v199 offset:128\n"
      "ds_read_b64_tr_b16 v[78:79], v199 offset:896\n"
      "s_waitcnt lgkmcnt(7)\n"
      "v_mfma_f32_32x32x16_bf16 v[112:127], v[64:67], v[244:247], v[112:127]\n"
      "s_add_u32 m0, s91, 4928\n"
      "s_add_u32 s77, s90, 19264\n"
      "s_mov_b32 exec_hi, 0xfff\n"
      "buffer_load_dwordx4 v1, s[36:39], s77 offen nt lds\n"
      "s_mov_b32 exec_hi, -1\n"
      "v_add_f32 v20, 925353388, v20\n"
      "v_rsq_f32 v44, v20\n"
      "s_waitcnt lgkmcnt(4)\n"
      "v_mfma_f32_32x32x16_bf16 v[80:95], v[68:71], v[248:251], v[80:95]\n"
      "v_mul_f32 v236, s10, v44\n"
      "s_waitcnt vmcnt(9) lgkmcnt(0)\n"
      "s_barrier\n"
      "ds_read_b128 v[244:247], v204 offset:28672\n"
      "ds_read_b64_tr_b16 v[56:57], v196 offset:28672\n"
      "ds_read_b64_tr_b16 v[58:59], v196 offset:29440\n"
      "ds_read_b64_tr_b16 v[60:61], v196 offset:28736\n"
      "ds_read_b64_tr_b16 v[62:63], v196 offset:29504\n"
      "ds_read_b64_tr_b16 v[64:65], v196 offset:28800\n"
      "ds_read_b64_tr_b16 v[66:67], v196 offset:29568\n"
      "v_mfma_f32_32x32x16_bf16 v[96:111], v[72:75], v[248:251], v[96:111]\n"
      "v_mul_f32 v238, s11, v44\n"
      "v_mfma_f32_32x32x16_bf16 v[112:127], v[76:79], v[248:251], v[112:127]\n"
      "v_mul_f32 v240, s12, v44\n"
      "ds_read_b128 v[248:251], v205 offset:28672\n"
      "ds_read_b64_tr_b16 v[68:69], v197 offset:28672\n"
      "ds_read_b64_tr_b16 v[70:71], v197 offset:29440\n"
      "s_waitcnt lgkmcnt(7)\n"
      "v_mfma_f32_32x32x16_bf16 v[80:95], v[56:59], v[244:247], v[80:95]\n"
      "v_mfma_f32_16x16x32_bf16 v[172:175], v[168:171], v[56:59], v[172:175]\n"
      "ds_read_b64_tr_b16 v[72:73], v197 offset:28736\n"
      "ds_read_b64_tr_b16 v[74:75], v197 offset:29504\n"
      "s_waitcnt lgkmcnt(7)\n"
      "v_mfma_f32_32x32x16_bf16 v[96:111], v[60:63], v[244:247], v[96:111]\n"
      "v_mfma_f32_16x16x32_bf16 v[176:179], v[168:171], v[60:63], v[176:179]\n"
      "ds_read_b64_tr_b16 v[76:77], v197 offset:28800\n"
      "ds_read_b64_tr_b16 v[78:79], v197 offset:29568\n"
      "s_waitcnt lgkmcnt(7)\n"
      "v_mfma_f32_32x32x16_bf16 v[112:127], v[64:67], v[244:247], v[112:127]\n"
      "v_mfma_f32_16x16x32_bf16 v[180:183], v[168:171], v[64:67], v[180:183]\n"
      "ds_read_b128 v[244:247], v206 offset:28672\n"
      "ds_read_b64_tr_b16 v[56:57], v198 offset:28672\n"
      "ds_read_b64_tr_b16 v[58:59], v198 offset:29440\n"
      "s_waitcnt lgkmcnt(7)\n"
      "v_mfma_f32_32x32x16_bf16 v[80:95], v[68:71], v[248:251], v[80:95]\n"
      "s_mov_b64 exec, 65535\n"
      "ds_write_b32 v39, v172\n"
      "ds_write_b32 v39, v173 offset:256\n"
      "ds_write_b32 v39, v176 offset:512\n"
      "ds_write_b32 v39, v177 offset:768\n"
      "ds_write_b32 v39, v180 offset:1024\n"
      "ds_write_b32 v39, v181 offset:1280\n"
      "s_mov_b64 exec, -1\n"
      "ds_read_b64_tr_b16 v[60:61], v198 offset:28736\n"
      "ds_read_b64_tr_b16 v[62:63], v198 offset:29504\n"
      "s_waitcnt lgkmcnt(13)\n"
      "v_mfma_f32_32x32x16_bf16 v[96:111], v[72:75], v[248:251], v[96:111]\n"
      "ds_read_b64_tr_b16 v[64:65], v198 offset:28800\n"
      "ds_read_b64_tr_b16 v[66:67], v198 offset:29568\n"
      "s_waitcnt lgkmcnt(13)\n"
      "v_mfma_f32_32x32x16_bf16 v[112:127], v[76:79], v[248:251], v[112:127]\n"
      "s_waitcnt lgkmcnt(0)\n"
      "s_barrier\n"
      "ds_read_b128 v[208:211], v40\n"
      "ds_read_b128 v[212:215], v40 offset:512\n"
      "ds_read_b128 v[216:219], v40 offset:1024\n"
      "ds_read_b128 v[248:251], v207 offset:28672\n"
      "ds_read_b64_tr_b16 v[68:69], v199 offset:28672\n"
      "ds_read_b64_tr_b16 v[70:71], v199 offset:29440\n"
      "v_mfma_f32_32x32x16_bf16 v[80:95], v[56:59], v[244:247], v[80:95]\n"
      "ds_read_b64_tr_b16 v[72:73], v199 offset:28736\n"
      "ds_read_b64_tr_b16 v[74:75], v199 offset:29504\n"
      "v_mfma_f32_32x32x16_bf16 v[96:111], v[60:63], v[244:247], v[96:111]\n"
      "ds_read_b64_tr_b16 v[76:77], v199 offset:28800\n"
      "ds_read_b64_tr_b16 v[78:79], v199 offset:29568\n"
      "v_mfma_f32_32x32x16_bf16 v[112:127], v[64:67], v[244:247], v[112:127]\n"
      "s_waitcnt lgkmcnt(9)\n"
      "v_add_f32 v208, v208, v209\n"
      "v_add_f32 v210, v210, v211\n"
      "v_add_f32 v35, v208, v210\n"
      "s_waitcnt lgkmcnt(8)\n"
      "v_add_f32 v212, v212, v213\n"
      "v_add_f32 v214, v214, v215\n"
      "v_add_f32 v36, v212, v214\n"
      "s_waitcnt lgkmcnt(7)\n"
      "v_add_f32 v216, v216, v217\n"
      "v_add_f32 v218, v218, v219\n"
      "v_add_f32 v37, v216, v218\n"
      "v_mfma_f32_32x32x2_f32 v[80:95], v35, v43, v[80:95]\n"
      "v_mfma_f32_32x32x2_f32 v[96:111], v36, v43, v[96:111]\n"
      "v_mfma_f32_32x32x2_f32 v[112:127], v37, v43, v[112:127]\n"
      "s_waitcnt lgkmcnt(4)\n"
      "v_mfma_f32_32x32x16_bf16 v[80:95], v[68:71], v[248:251], v[80:95]\n"
      "s_waitcnt lgkmcnt(2)\n"
      "v_mfma_f32_32x32x16_bf16 v[96:111], v[72:75], v[248:251], v[96:111]\n"
      "s_waitcnt lgkmcnt(0)\n"
      "v_mfma_f32_32x32x16_bf16 v[112:127], v[76:79], v[248:251], v[112:127]\n"
      "s_nop 9\n"
      "v_pk_mul_f32 v[80:81], v[80:81], v[236:237] op_sel_hi:[1,0]\n"
      "v_pk_mul_f32 v[82:83], v[82:83], v[236:237] op_sel_hi:[1,0]\n"
      "v_pk_mul_f32 v[84:85], v[84:85], v[236:237] op_sel_hi:[1,0]\n"
      "v_pk_mul_f32 v[86:87], v[86:87], v[236:237] op_sel_hi:[1,0]\n"
      "v_pk_mul_f32 v[88:89], v[88:89], v[236:237] op_sel_hi:[1,0]\n"
      "v_pk_mul_f32 v[90:91], v[90:91], v[236:237] op_sel_hi:[1,0]\n"
      "v_pk_mul_f32 v[92:93], v[92:93], v[236:237] op_sel_hi:[1,0]\n"
      "v_pk_mul_f32 v[94:95], v[94:95], v[236:237] op_sel_hi:[1,0]\n"
      "v_cvt_pk_bf16_f32 v56, v80, v81\n"
      "v_cvt_pk_bf16_f32 v57, v82, v83\n"
      "v_cvt_pk_bf16_f32 v58, v84, v85\n"
      "v_cvt_pk_bf16_f32 v59, v86, v87\n"
      "buffer_store_dwordx4 v[56:59], v1, s[44:47], s4 offen sc1\n"
      "v_cvt_pk_bf16_f32 v60, v88, v89\n"
      "v_cvt_pk_bf16_f32 v61, v90, v91\n"
      "v_cvt_pk_bf16_f32 v62, v92, v93\n"
      "v_cvt_pk_bf16_f32 v63, v94, v95\n"
      "buffer_store_dwordx4 v[60:63], v1, s[44:47], s5 offen sc1\n"
      "v_pk_mul_f32 v[96:97], v[96:97], v[238:239] op_sel_hi:[1,0]\n"
      "v_pk_mul_f32 v[98:99], v[98:99], v[238:239] op_sel_hi:[1,0]\n"
      "v_pk_mul_f32 v[100:101], v[100:101], v[238:239] op_sel_hi:[1,0]\n"
      "v_pk_mul_f32 v[102:103], v[102:103], v[238:239] op_sel_hi:[1,0]\n"
      "v_pk_mul_f32 v[104:105], v[104:105], v[238:239] op_sel_hi:[1,0]\n"
      "v_pk_mul_f32 v[106:107], v[106:107], v[238:239] op_sel_hi:[1,0]\n"
      "v_pk_mul_f32 v[108:109], v[108:109], v[238:239] op_sel_hi:[1,0]\n"
      "v_pk_mul_f32 v[110:111], v[110:111], v[238:239] op_sel_hi:[1,0]\n"
      "v_cvt_pk_bf16_f32 v64, v96, v97\n"
      "v_cvt_pk_bf16_f32 v65, v98, v99\n"
      "v_cvt_pk_bf16_f32 v66, v100, v101\n"
      "v_cvt_pk_bf16_f32 v67, v102, v103\n"
      "buffer_store_dwordx4 v[64:67], v1, s[44:47], s6 offen sc1\n"
      "v_cvt_pk_bf16_f32 v68, v104, v105\n"
      "v_cvt_pk_bf16_f32 v69, v106, v107\n"
      "v_cvt_pk_bf16_f32 v70, v108, v109\n"
      "v_cvt_pk_bf16_f32 v71, v110, v111\n"
      "buffer_store_dwordx4 v[68:71], v1, s[44:47], s7 offen sc1\n"
      "v_pk_mul_f32 v[112:113], v[112:113], v[240:241] op_sel_hi:[1,0]\n"
      "v_pk_mul_f32 v[114:115], v[114:115], v[240:241] op_sel_hi:[1,0]\n"
      "v_pk_mul_f32 v[116:117], v[116:117], v[240:241] op_sel_hi:[1,0]\n"
      "v_pk_mul_f32 v[118:119], v[118:119], v[240:241] op_sel_hi:[1,0]\n"
      "v_pk_mul_f32 v[120:121], v[120:121], v[240:241] op_sel_hi:[1,0]\n"
      "v_pk_mul_f32 v[122:123], v[122:123], v[240:241] op_sel_hi:[1,0]\n"
      "v_pk_mul_f32 v[124:125], v[124:125], v[240:241] op_sel_hi:[1,0]\n"
      "v_pk_mul_f32 v[126:127], v[126:127], v[240:241] op_sel_hi:[1,0]\n"
      "v_cvt_pk_bf16_f32 v72, v112, v113\n"
      "v_cvt_pk_bf16_f32 v73, v114, v115\n"
      "v_cvt_pk_bf16_f32 v74, v116, v117\n"
      "v_cvt_pk_bf16_f32 v75, v118, v119\n"
      "buffer_store_dwordx4 v[72:75], v1, s[44:47], s8 offen sc1\n"
      "v_cvt_pk_bf16_f32 v76, v120, v121\n"
      "v_cvt_pk_bf16_f32 v77, v122, v123\n"
      "v_cvt_pk_bf16_f32 v78, v124, v125\n"
      "v_cvt_pk_bf16_f32 v79, v126, v127\n"
      "buffer_store_dwordx4 v[76:79], v1, s[44:47], s9 offen sc1\n"
      "s_nop 1\n"
      "s_branch .Lp1end_4_%=\n"
      ".Lp1QQV_2_%=:\n"
      "s_waitcnt vmcnt(8)\n"
      "s_barrier\n"
      "ds_read_b128 v[244:247], v200\n"
      "ds_read_b64_tr_b16 v[56:57], v192\n"
      "ds_read_b64_tr_b16 v[58:59], v192 offset:768\n"
      "ds_read_b64_tr_b16 v[60:61], v192 offset:64\n"
      "ds_read_b64_tr_b16 v[62:63], v192 offset:832\n"
      "ds_read_b64_tr_b16 v[64:65], v192 offset:128\n"
      "ds_read_b64_tr_b16 v[66:67], v192 offset:896\n"
      "ds_read_b128 v[248:251], v201\n"
      "ds_read_b64_tr_b16 v[68:69], v193\n"
      "ds_read_b64_tr_b16 v[70:71], v193 offset:768\n"
      "s_waitcnt lgkmcnt(7)\n"
      "v_mfma_f32_32x32x16_bf16 v[80:95], v[56:59], v[244:247], 0\n"
      "s_add_u32 s89, s87, 2\n"
      "s_and_b32 s89, s89, 7\n"
      "s_lshl_b32 s88, s89, 7\n"
      "s_add_u32 s88, s88, s81\n"
      "s_add_u32 m0, s82, 57344\n"
      "s_add_u32 s77, s88, 0\n"
      "buffer_load_dwordx4 v29, s[24:27], s77 offen lds\n"
      "s_mov_b32 s36, s10\n"
      "s_and_b32 s37, s11, 65535\n"
      "v_mfma_f32_16x16x32_bf16 v[172:175], v[168:171], v[56:59], 0\n"
      "ds_read_b64_tr_b16 v[72:73], v193 offset:64\n"
      "ds_read_b64_tr_b16 v[74:75], v193 offset:832\n"
      "s_waitcnt lgkmcnt(7)\n"
      "v_mfma_f32_32x32x16_bf16 v[96:111], v[60:63], v[244:247], 0\n"
      "s_add_u32 m0, s82, 58368\n"
      "s_add_u32 s77, s88, 8192\n"
      "buffer_load_dwordx4 v30, s[24:27], s77 offen lds\n"
      "s_mov_b32 s38, 5636096\n"
      "s_mov_b32 s39, 131072\n"
      "v_mfma_f32_16x16x32_bf16 v[176:179], v[168:171], v[60:63], 0\n"
      "ds_read_b64_tr_b16 v[76:77], v193 offset:128\n"
      "ds_read_b64_tr_b16 v[78:79], v193 offset:896\n"
      "s_waitcnt lgkmcnt(7)\n"
      "v_mfma_f32_32x32x16_bf16 v[112:127], v[244:247], v[64:67], 0\n"
      "s_and_b32 s75, s64, 7\n"
      "s_lshl_b32 s69, s79, 3\n"
      "v_mfma_f32_16x16x32_bf16 v[180:183], v[168:171], v[64:67], 0\n"
      "ds_read_b128 v[244:247], v202\n"
      "ds_read_b64_tr_b16 v[56:57], v194\n"
      "ds_read_b64_tr_b16 v[58:59], v194 offset:768\n"
      "s_waitcnt lgkmcnt(7)\n"
      "v_mfma_f32_32x32x16_bf16 v[80:95], v[68:71], v[248:251], v[80:95]\n"
      "s_add_u32 m0, s82, 59392\n"
      "s_add_u32 s77, s88, 16384\n"
      "buffer_load_dwordx4 v29, s[24:27], s77 offen lds\n"
      "s_add_u32 s69, s69, s75\n"
      "s_lshr_b32 s74, s64, 3\n"
      "ds_read_b64_tr_b16 v[60:61], v194 offset:64\n"
      "ds_read_b64_tr_b16 v[62:63], v194 offset:832\n"
      "s_waitcnt lgkmcnt(7)\n"
      "v_mfma_f32_32x32x16_bf16 v[96:111], v[72:75], v[248:251], v[96:111]\n"
      "s_add_u32 m0, s82, 60416\n"
      "s_add_u32 s77, s88, 24576\n"
      "buffer_load_dwordx4 v30, s[24:27], s77 offen lds\n"
      "s_lshl_b32 s70, s80, 2\n"
      "s_add_u32 s70, s70, s74\n"
      "ds_read_b64_tr_b16 v[64:65], v194 offset:128\n"
      "ds_read_b64_tr_b16 v[66:67], v194 offset:896\n"
      "s_waitcnt lgkmcnt(7)\n"
      "v_mfma_f32_32x32x16_bf16 v[112:127], v[248:251], v[76:79], v[112:127]\n"
      "s_cmp_eq_u32 s79, 3\n"
      "s_cselect_b32 s74, 4, 6\n"
      "s_min_u32 s73, s75, s74\n"
      "ds_read_b128 v[248:251], v203\n"
      "ds_read_b64_tr_b16 v[68:69], v195\n"
      "ds_read_b64_tr_b16 v[70:71], v195 offset:768\n"
      "s_waitcnt lgkmcnt(7)\n"
      "v_mfma_f32_32x32x16_bf16 v[80:95], v[56:59], v[244:247], v[80:95]\n"
      "s_add_u32 m0, s84, 57344\n"
      "s_mul_i32 s78, s89, 196608\n"
      "s_add_u32 s78, s78, s83\n"
      "buffer_load_dwordx4 v26, s[28:31], s78 offen lds\n"
      "s_mul_i32 s71, s79, 22\n"
      "s_lshl_b32 s76, s75, 1\n"
      "ds_read_b64_tr_b16 v[72:73], v195 offset:64\n"
      "ds_read_b64_tr_b16 v[74:75], v195 offset:832\n"
      "s_waitcnt lgkmcnt(7)\n"
      "v_mfma_f32_32x32x16_bf16 v[96:111], v[60:63], v[244:247], v[96:111]\n"
      "buffer_load_dwordx4 v27, s[28:31], s78 offen offset:1024 lds\n"
      "s_add_u32 s71, s71, s76\n"
      "s_add_u32 s71, s71, s73\n"
      "ds_read_b64_tr_b16 v[76:77], v195 offset:128\n"
      "ds_read_b64_tr_b16 v[78:79], v195 offset:896\n"
      "s_waitcnt lgkmcnt(7)\n"
      "v_mfma_f32_32x32x16_bf16 v[112:127], v[244:247], v[64:67], v[112:127]\n"
      "s_cmp_lt_u32 s75, s74\n"
      "s_cselect_b32 s72, 0, 1073741824\n"
      "s_mul_i32 s90, %[wg], 22016\n"
      "s_waitcnt lgkmcnt(4)\n"
      "v_mfma_f32_32x32x16_bf16 v[80:95], v[68:71], v[248:251], v[80:95]\n"
      "buffer_load_dwordx4 v28, s[28:31], s78 offen offset:2048 lds\n"
      "s_mul_i32 s76, s62, 704\n"
      "s_add_u32 s90, s90, s76\n"
      "s_waitcnt vmcnt(8) lgkmcnt(0)\n"
      "s_barrier\n"
      "ds_read_b128 v[244:247], v200 offset:28672\n"
      "ds_read_b64_tr_b16 v[56:57], v192 offset:28672\n"
      "ds_read_b64_tr_b16 v[58:59], v192 offset:29440\n"
      "ds_read_b64_tr_b16 v[60:61], v192 offset:28736\n"
      "ds_read_b64_tr_b16 v[62:63], v192 offset:29504\n"
      "ds_read_b64_tr_b16 v[64:65], v192 offset:28800\n"
      "ds_read_b64_tr_b16 v[66:67], v192 offset:29568\n"
      "v_mfma_f32_32x32x16_bf16 v[96:111], v[72:75], v[248:251], v[96:111]\n"
      "s_mov_b32 s32, s8\n"
      "s_and_b32 s33, s9, 65535\n"
      "v_mfma_f32_32x32x16_bf16 v[112:127], v[248:251], v[76:79], v[112:127]\n"
      "s_mov_b32 s34, 524288\n"
      "s_mov_b32 s35, 131072\n"
      "ds_read_b128 v[248:251], v201 offset:28672\n"
      "ds_read_b64_tr_b16 v[68:69], v193 offset:28672\n"
      "ds_read_b64_tr_b16 v[70:71], v193 offset:29440\n"
      "s_waitcnt lgkmcnt(7)\n"
      "v_mfma_f32_32x32x16_bf16 v[80:95], v[56:59], v[244:247], v[80:95]\n"
      "s_add_u32 s89, s87, 3\n"
      "s_and_b32 s89, s89, 7\n"
      "s_lshl_b32 s88, s89, 7\n"
      "s_add_u32 s88, s88, s81\n"
      "s_add_u32 m0, s82, 86016\n"
      "s_add_u32 s77, s88, 0\n"
      "buffer_load_dwordx4 v29, s[24:27], s77 offen lds\n"
      "s_and_b32 s73, s69, 7\n"
      "s_lshl_b32 s95, s73, 16\n"
      "v_mfma_f32_16x16x32_bf16 v[172:175], v[168:171], v[56:59], v[172:175]\n"
      "ds_read_b64_tr_b16 v[72:73], v193 offset:28736\n"
      "ds_read_b64_tr_b16 v[74:75], v193 offset:29504\n"
      "s_waitcnt lgkmcnt(7)\n"
      "v_mfma_f32_32x32x16_bf16 v[96:111], v[60:63], v[244:247], v[96:111]\n"
      "s_add_u32 m0, s82, 87040\n"
      "s_add_u32 s77, s88, 8192\n"
      "buffer_load_dwordx4 v30, s[24:27], s77 offen lds\n"
      "s_lshl_b32 s76, s62, 14\n"
      "s_add_u32 s95, s95, s76\n"
      "v_mfma_f32_16x16x32_bf16 v[176:179], v[168:171], v[60:63], v[176:179]\n"
      "ds_read_b64_tr_b16 v[76:77], v193 offset:28800\n"
      "ds_read_b64_tr_b16 v[78:79], v193 offset:29568\n"
      "s_waitcnt lgkmcnt(7)\n"
      "v_mfma_f32_32x32x16_bf16 v[112:127], v[244:247], v[64:67], v[112:127]\n"
      "s_lshl_b32 s76, s70, 7\n"
      "s_add_u32 s95, s95, s76\n"
      "v_mfma_f32_16x16x32_bf16 v[180:183], v[168:171], v[64:67], v[180:183]\n"
      "ds_read_b128 v[244:247], v202 offset:28672\n"
      "ds_read_b64_tr_b16 v[56:57], v194 offset:28672\n"
      "ds_read_b64_tr_b16 v[58:59], v194 offset:29440\n"
      "s_waitcnt lgkmcnt(7)\n"
      "v_mfma_f32_32x32x16_bf16 v[80:95], v[68:71], v[248:251], v[80:95]\n"
      "s_add_u32 m0, s82, 88064\n"
      "s_add_u32 s77, s88, 16384\n"
      "buffer_load_dwordx4 v29, s[24:27], s77 offen lds\n"
      "s_mul_i32 s91, s62, 12288\n"
      "s_add_u32 s91, s91, 114688\n"
      "ds_read_b64_tr_b16 v[60:61], v194 offset:28736\n"
      "ds_read_b64_tr_b16 v[62:63], v194 offset:29504\n"
      "s_waitcnt lgkmcnt(7)\n"
      "v_mfma_f32_32x32x16_bf16 v[96:111], v[72:75], v[248:251], v[96:111]\n"
      "s_add_u32 m0, s82, 89088\n"
      "s_add_u32 s77, s88, 24576\n"
      "buffer_load_dwordx4 v30, s[24:27], s77 offen lds\n"
      "s_lshl_b32 s92, s62, 14\n"
      "s_mov_b32 s40, s14\n"
      "ds_read_b64_tr_b16 v[64:65], v194 offset:28800\n"
      "ds_read_b64_tr_b16 v[66:67], v194 offset:29568\n"
      "s_waitcnt lgkmcnt(7)\n"
      "v_mfma_f32_32x32x16_bf16 v[112:127], v[248:251], v[76:79], v[112:127]\n"
      "s_and_b32 s41, s15, 65535\n"
      "s_mov_b32 s42, 5636096\n"
      "ds_read_b128 v[248:251], v203 offset:28672\n"
      "ds_read_b64_tr_b16 v[68:69], v195 offset:28672\n"
      "ds_read_b64_tr_b16 v[70:71], v195 offset:29440\n"
      "s_waitcnt lgkmcnt(7)\n"
      "v_mfma_f32_32x32x16_bf16 v[80:95], v[56:59], v[244:247], v[80:95]\n"
      "s_add_u32 m0, s84, 86016\n"
      "s_mul_i32 s78, s89, 196608\n"
      "s_add_u32 s78, s78, s83\n"
      "buffer_load_dwordx4 v26, s[28:31], s78 offen lds\n"
      "s_mov_b32 s43, 131072\n"
      "s_mul_i32 s93, s62, 1441792\n"
      "ds_read_b64_tr_b16 v[72:73], v195 offset:28736\n"
      "ds_read_b64_tr_b16 v[74:75], v195 offset:29504\n"
      "s_waitcnt lgkmcnt(7)\n"
      "v_mfma_f32_32x32x16_bf16 v[96:111], v[60:63], v[244:247], v[96:111]\n"
      "buffer_load_dwordx4 v27, s[28:31], s78 offen offset:1024 lds\n"
      "s_lshl_b32 s76, s69, 7\n"
      "s_add_u32 s93, s93, s76\n"
      "ds_read_b64_tr_b16 v[76:77], v195 offset:28800\n"
      "ds_read_b64_tr_b16 v[78:79], v195 offset:29568\n"
      "s_waitcnt lgkmcnt(7)\n"
      "v_mfma_f32_32x32x16_bf16 v[112:127], v[244:247], v[64:67], v[112:127]\n"
      "s_mov_b32 s52, s12\n"
      "s_and_b32 s53, s13, 65535\n"
      "s_waitcnt lgkmcnt(4)\n"
      "v_mfma_f32_32x32x16_bf16 v[80:95], v[68:71], v[248:251], v[80:95]\n"
      "buffer_load_dwordx4 v28, s[28:31], s78 offen offset:2048 lds\n"
      "s_mov_b32 s54, 2752\n"
      "s_mov_b32 s55, 131072\n"
      "s_waitcnt vmcnt(7) lgkmcnt(0)\n"
      "s_barrier\n"
      "ds_read_b128 v[244:247], v204\n"
      "ds_read_b64_tr_b16 v[56:57], v196\n"
      "ds_read_b64_tr_b16 v[58:59], v196 offset:768\n"
      "ds_read_b64_tr_b16 v[60:61], v196 offset:64\n"
      "ds_read_b64_tr_b16 v[62:63], v196 offset:832\n"
      "ds_read_b64_tr_b16 v[64:65], v196 offset:128\n"
      "ds_read_b64_tr_b16 v[66:67], v196 offset:896\n"
      "v_mfma_f32_32x32x16_bf16 v[96:111], v[72:75], v[248:251], v[96:111]\n"
      "s_mov_b32 s56, s16\n"
      "s_and_b32 s57, s17, 65535\n"
      "v_mfma_f32_32x32x16_bf16 v[112:127], v[248:251], v[76:79], v[112:127]\n"
      "s_mov_b32 s58, 4096\n"
      "s_mov_b32 s59, 131072\n"
      "ds_read_b128 v[248:251], v205\n"
      "ds_read_b64_tr_b16 v[68:69], v197\n"
      "ds_read_b64_tr_b16 v[70:71], v197 offset:768\n"
      "s_waitcnt lgkmcnt(7)\n"
      "v_mfma_f32_32x32x16_bf16 v[80:95], v[56:59], v[244:247], v[80:95]\n"
      "s_add_u32 s89, s87, 4\n"
      "s_and_b32 s89, s89, 7\n"
      "s_lshl_b32 s88, s89, 7\n"
      "s_add_u32 s88, s88, s81\n"
      "s_add_u32 m0, s82, 0\n"
      "s_add_u32 s77, s88, 0\n"
      "buffer_load_dwordx4 v29, s[24:27], s77 offen lds\n"
      "s_mov_b32 s48, s18\n"
      "s_and_b32 s49, s19, 65535\n"
      "v_mfma_f32_16x16x32_bf16 v[172:175], v[168:171], v[56:59], v[172:175]\n"
      "ds_read_b64_tr_b16 v[72:73], v197 offset:64\n"
      "ds_read_b64_tr_b16 v[74:75], v197 offset:832\n"
      "s_waitcnt lgkmcnt(7)\n"
      "v_mfma_f32_32x32x16_bf16 v[96:111], v[60:63], v[244:247], v[96:111]\n"
      "s_add_u32 m0, s82, 1024\n"
      "s_add_u32 s77, s88, 8192\n"
      "buffer_load_dwordx4 v30, s[24:27], s77 offen lds\n"
      "s_mov_b32 s50, 2097152\n"
      "s_mov_b32 s51, 131072\n"
      "v_mfma_f32_16x16x32_bf16 v[176:179], v[168:171], v[60:63], v[176:179]\n"
      "ds_read_b64_tr_b16 v[76:77], v197 offset:128\n"
      "ds_read_b64_tr_b16 v[78:79], v197 offset:896\n"
      "s_waitcnt lgkmcnt(7)\n"
      "v_mfma_f32_32x32x16_bf16 v[112:127], v[244:247], v[64:67], v[112:127]\n"
      "s_lshl_b32 s13, s65, 13\n"
      "s_lshl_b32 s76, s66, 9\n"
      "v_mfma_f32_16x16x32_bf16 v[180:183], v[168:171], v[64:67], v[180:183]\n"
      "ds_read_b128 v[244:247], v206\n"
      "ds_read_b64_tr_b16 v[56:57], v198\n"
      "ds_read_b64_tr_b16 v[58:59], v198 offset:768\n"
      "s_waitcnt lgkmcnt(7)\n"
      "v_mfma_f32_32x32x16_bf16 v[80:95], v[68:71], v[248:251], v[80:95]\n"
      "s_add_u32 m0, s82, 2048\n"
      "s_add_u32 s77, s88, 16384\n"
      "buffer_load_dwordx4 v29, s[24:27], s77 offen lds\n"
      "s_add_u32 s13, s13, s76\n"
      "s_lshl_b32 s76, s62, 7\n"
      "ds_read_b64_tr_b16 v[60:61], v198 offset:64\n"
      "ds_read_b64_tr_b16 v[62:63], v198 offset:832\n"
      "s_waitcnt lgkmcnt(7)\n"
      "v_mfma_f32_32x32x16_bf16 v[96:111], v[72:75], v[248:251], v[96:111]\n"
      "s_add_u32 m0, s82, 3072\n"
      "s_add_u32 s77, s88, 24576\n"
      "buffer_load_dwordx4 v30, s[24:27], s77 offen lds\n"
      "s_add_u32 s13, s13, s76\n"
      "s_lshl_b32 s73, s65, 12\n"
      "ds_read_b64_tr_b16 v[64:65], v198 offset:128\n"
      "ds_read_b64_tr_b16 v[66:67], v198 offset:896\n"
      "s_waitcnt lgkmcnt(7)\n"
      "v_mfma_f32_32x32x16_bf16 v[112:127], v[248:251], v[76:79], v[112:127]\n"
      "s_lshl_b32 s76, s62, 10\n"
      "s_add_u32 s73, s73, s76\n"
      "ds_read_b128 v[248:251], v207\n"
      "ds_read_b64_tr_b16 v[68:69], v199\n"
      "ds_read_b64_tr_b16 v[70:71], v199 offset:768\n"
      "s_waitcnt lgkmcnt(7)\n"
      "v_mfma_f32_32x32x16_bf16 v[80:95], v[56:59], v[244:247], v[80:95]\n"
      "s_add_u32 m0, s84, 0\n"
      "s_mul_i32 s78, s89, 196608\n"
      "s_add_u32 s78, s78, s83\n"
      "buffer_load_dwordx4 v26, s[28:31], s78 offen lds\n"
      "s_mul_i32 s76, s66, 393216\n"
      "s_add_u32 s73, s73, s76\n"
      "ds_read_b64_tr_b16 v[72:73], v199 offset:64\n"
      "ds_read_b64_tr_b16 v[74:75], v199 offset:832\n"
      "s_waitcnt lgkmcnt(7)\n"
      "v_mfma_f32_32x32x16_bf16 v[96:111], v[60:63], v[244:247], v[96:111]\n"
      "buffer_load_dwordx4 v27, s[28:31], s78 offen offset:1024 lds\n"
      "s_add_u32 s73, s73, 1048576\n"
      "s_mul_i32 s74, s66, 3\n"
      "ds_read_b64_tr_b16 v[76:77], v199 offset:128\n"
      "ds_read_b64_tr_b16 v[78:79], v199 offset:896\n"
      "s_waitcnt lgkmcnt(7)\n"
      "v_mfma_f32_32x32x16_bf16 v[112:127], v[244:247], v[64:67], v[112:127]\n"
      "s_add_u32 s4, s73, 0\n"
      "s_add_u32 s5, s4, 65536\n"
      "s_waitcnt lgkmcnt(4)\n"
      "v_mfma_f32_32x32x16_bf16 v[80:95], v[68:71], v[248:251], v[80:95]\n"
      "buffer_load_dwordx4 v28, s[28:31], s78 offen offset:2048 lds\n"
      "s_cmp_lt_u32 s74, 16\n"
      "s_cselect_b32 s10, 1052289595, 1065353216\n"
      "s_add_u32 s6, s73, 131072\n"
      "s_waitcnt vmcnt(7) lgkmcnt(0)\n"
      "s_barrier\n"
      "ds_read_b128 v[244:247], v204 offset:28672\n"
      "ds_read_b64_tr_b16 v[56:57], v196 offset:28672\n"
      "ds_read_b64_tr_b16 v[58:59], v196 offset:29440\n"
      "ds_read_b64_tr_b16 v[60:61], v196 offset:28736\n"
      "ds_read_b64_tr_b16 v[62:63], v196 offset:29504\n"
      "ds_read_b64_tr_b16 v[64:65], v196 offset:28800\n"
      "ds_read_b64_tr_b16 v[66:67], v196 offset:29568\n"
      "v_mfma_f32_32x32x16_bf16 v[96:111], v[72:75], v[248:251], v[96:111]\n"
      "s_add_u32 s7, s6, 65536\n"
      "s_cmp_lt_u32 s74, 15\n"
      "s_cselect_b32 s11, 1052289595, 1065353216\n"
      "v_mfma_f32_32x32x16_bf16 v[112:127], v[248:251], v[76:79], v[112:127]\n"
      "s_add_u32 s8, s73, 262144\n"
      "s_add_u32 s9, s8, 65536\n"
      "ds_read_b128 v[248:251], v205 offset:28672\n"
      "ds_read_b64_tr_b16 v[68:69], v197 offset:28672\n"
      "ds_read_b64_tr_b16 v[70:71], v197 offset:29440\n"
      "s_waitcnt lgkmcnt(7)\n"
      "v_mfma_f32_32x32x16_bf16 v[80:95], v[56:59], v[244:247], v[80:95]\n"
      "s_add_u32 s89, s87, 5\n"
      "s_and_b32 s89, s89, 7\n"
      "s_lshl_b32 s88, s89, 7\n"
      "s_add_u32 s88, s88, s81\n"
      "s_add_u32 m0, s82, 28672\n"
      "s_add_u32 s77, s88, 0\n"
      "buffer_load_dwordx4 v29, s[24:27], s77 offen lds\n"
      "s_cmp_lt_u32 s74, 14\n"
      "s_cselect_b32 s12, 1052289595, 1065353216\n"
      "s_lshl_b32 s14, %[wg], 8\n"
      "v_mfma_f32_16x16x32_bf16 v[172:175], v[168:171], v[56:59], v[172:175]\n"
      "ds_read_b64_tr_b16 v[72:73], v197 offset:28736\n"
      "ds_read_b64_tr_b16 v[74:75], v197 offset:29504\n"
      "s_waitcnt lgkmcnt(7)\n"
      "v_mfma_f32_32x32x16_bf16 v[96:111], v[60:63], v[244:247], v[96:111]\n"
      "s_add_u32 m0, s82, 29696\n"
      "s_add_u32 s77, s88, 8192\n"
      "buffer_load_dwordx4 v30, s[24:27], s77 offen lds\n"
      "s_mul_i32 s76, s62, 1441792\n"
      "s_add_u32 s14, s14, s76\n"
      "v_mfma_f32_16x16x32_bf16 v[176:179], v[168:171], v[60:63], v[176:179]\n"
      "ds_read_b64_tr_b16 v[76:77], v197 offset:28800\n"
      "ds_read_b64_tr_b16 v[78:79], v197 offset:29568\n"
      "s_waitcnt lgkmcnt(7)\n"
      "v_mfma_f32_32x32x16_bf16 v[112:127], v[244:247], v[64:67], v[112:127]\n"
      "s_add_u32 s14, s14, 14680064\n"
      "s_lshl_b32 s15, %[wg], 7\n"
      "v_mfma_f32_16x16x32_bf16 v[180:183], v[168:171], v[64:67], v[180:183]\n"
      "ds_read_b128 v[244:247], v206 offset:28672\n"
      "ds_read_b64_tr_b16 v[56:57], v198 offset:28672\n"
      "ds_read_b64_tr_b16 v[58:59], v198 offset:29440\n"
      "s_waitcnt lgkmcnt(7)\n"
      "v_mfma_f32_32x32x16_bf16 v[80:95], v[68:71], v[248:251], v[80:95]\n"
      "s_add_u32 m0, s82, 30720\n"
      "s_add_u32 s77, s88, 16384\n"
      "buffer_load_dwordx4 v29, s[24:27], s77 offen lds\n"
      "s_lshl_b32 s76, s62, 15\n"
      "s_add_u32 s15, s15, s76\n"
      "ds_read_b64_tr_b16 v[60:61], v198 offset:28736\n"
      "ds_read_b64_tr_b16 v[62:63], v198 offset:29504\n"
      "s_waitcnt lgkmcnt(7)\n"
      "v_mfma_f32_32x32x16_bf16 v[96:111], v[72:75], v[248:251], v[96:111]\n"
      "s_add_u32 m0, s82, 31744\n"
      "s_add_u32 s77, s88, 24576\n"
      "buffer_load_dwordx4 v30, s[24:27], s77 offen lds\n"
      "s_and_b32 s74, s64, 3\n"
      "s_lshl_b32 s67, s63, 2\n"
      "ds_read_b64_tr_b16 v[64:65], v198 offset:28800\n"
      "ds_read_b64_tr_b16 v[66:67], v198 offset:29568\n"
      "s_waitcnt lgkmcnt(7)\n"
      "v_mfma_f32_32x32x16_bf16 v[112:127], v[248:251], v[76:79], v[112:127]\n"
      "s_add_u32 s67, s67, s74\n"
      "s_min_u32 s22, s68, 1\n"
      "ds_read_b128 v[248:251], v207 offset:28672\n"
      "ds_read_b64_tr_b16 v[68:69], v199 offset:28672\n"
      "ds_read_b64_tr_b16 v[70:71], v199 offset:29440\n"
      "s_waitcnt lgkmcnt(7)\n"
      "v_mfma_f32_32x32x16_bf16 v[80:95], v[56:59], v[244:247], v[80:95]\n"
      "s_add_u32 m0, s84, 28672\n"
      "s_mul_i32 s78, s89, 196608\n"
      "s_add_u32 s78, s78, s83\n"
      "buffer_load_dwordx4 v26, s[28:31], s78 offen lds\n"
      "s_lshl_b32 s97, s68, 3\n"
      "s_lshl_b32 s76, s62, 1\n"
      "ds_read_b64_tr_b16 v[72:73], v199 offset:28736\n"
      "ds_read_b64_tr_b16 v[74:75], v199 offset:29504\n"
      "s_waitcnt lgkmcnt(7)\n"
      "v_mfma_f32_32x32x16_bf16 v[96:111], v[60:63], v[244:247], v[96:111]\n"
      "buffer_load_dwordx4 v27, s[28:31], s78 offen offset:1024 lds\n"
      "s_add_u32 s97, s97, s76\n"
      "s_lshl_b32 s23, s22, 3\n"
      "ds_read_b64_tr_b16 v[76:77], v199 offset:28800\n"
      "ds_read_b64_tr_b16 v[78:79], v199 offset:29568\n"
      "s_waitcnt lgkmcnt(7)\n"
      "v_mfma_f32_32x32x16_bf16 v[112:127], v[244:247], v[64:67], v[112:127]\n"
      "s_sub_u32 s23, s97, s23\n"
      "s_lshl_b32 s16, s67, 16\n"
      "s_waitcnt lgkmcnt(4)\n"
      "v_mfma_f32_32x32x16_bf16 v[80:95], v[68:71], v[248:251], v[80:95]\n"
      "buffer_load_dwordx4 v28, s[28:31], s78 offen offset:2048 lds\n"
      "s_lshl_b32 s76, s23, 10\n"
      "s_add_u32 s16, s16, s76\n"
      "s_waitcnt vmcnt(7) lgkmcnt(0)\n"
      "s_barrier\n"
      "ds_read_b128 v[244:247], v200\n"
      "ds_read_b64_tr_b16 v[56:57], v192\n"
      "ds_read_b64_tr_b16 v[58:59], v192 offset:768\n"
      "ds_read_b64_tr_b16 v[60:61], v192 offset:64\n"
      "ds_read_b64_tr_b16 v[62:63], v192 offset:832\n"
      "ds_read_b64_tr_b16 v[64:65], v192 offset:128\n"
      "ds_read_b64_tr_b16 v[66:67], v192 offset:896\n"
      "v_mfma_f32_32x32x16_bf16 v[96:111], v[72:75], v[248:251], v[96:111]\n"
      "s_add_u32 s16, s16, 3145728\n"
      "s_add_u32 s17, s16, 2097152\n"
      "v_mfma_f32_32x32x16_bf16 v[112:127], v[248:251], v[76:79], v[112:127]\n"
      "s_lshl_b32 s18, s67, 16\n"
      "s_lshl_b32 s76, s97, 10\n"
      "ds_read_b128 v[248:251], v201\n"
      "ds_read_b64_tr_b16 v[68:69], v193\n"
      "ds_read_b64_tr_b16 v[70:71], v193 offset:768\n"
      "s_waitcnt lgkmcnt(7)\n"
      "v_mfma_f32_32x32x16_bf16 v[80:95], v[56:59], v[244:247], v[80:95]\n"
      "s_add_u32 s89, s87, 6\n"
      "s_and_b32 s89, s89, 7\n"
      "s_lshl_b32 s88, s89, 7\n"
      "s_add_u32 s88, s88, s81\n"
      "s_add_u32 m0, s82, 57344\n"
      "s_add_u32 s77, s88, 0\n"
      "buffer_load_dwordx4 v29, s[24:27], s77 offen lds\n"
      "s_add_u32 s18, s18, s76\n"
      "s_add_u32 s18, s18, 1048576\n"
      "v_mfma_f32_16x16x32_bf16 v[172:175], v[168:171], v[56:59], v[172:175]\n"
      "ds_read_b64_tr_b16 v[72:73], v193 offset:64\n"
      "ds_read_b64_tr_b16 v[74:75], v193 offset:832\n"
      "s_waitcnt lgkmcnt(7)\n"
      "v_mfma_f32_32x32x16_bf16 v[96:111], v[60:63], v[244:247], v[96:111]\n"
      "s_add_u32 m0, s82, 58368\n"
      "s_add_u32 s77, s88, 8192\n"
      "buffer_load_dwordx4 v30, s[24:27], s77 offen lds\n"
      "s_lshl_b32 s19, s62, 11\n"
      "s_add_u32 s19, s19, 65536\n"
      "v_mfma_f32_16x16x32_bf16 v[176:179], v[168:171], v[60:63], v[176:179]\n"
      "ds_read_b64_tr_b16 v[76:77], v193 offset:128\n"
      "ds_read_b64_tr_b16 v[78:79], v193 offset:896\n"
      "s_waitcnt lgkmcnt(7)\n"
      "v_mfma_f32_32x32x16_bf16 v[112:127], v[244:247], v[64:67], v[112:127]\n"
      "s_lshl_b32 s94, s69, 10\n"
      "s_lshl_b32 s76, s70, 7\n"
      "v_mfma_f32_16x16x32_bf16 v[180:183], v[168:171], v[64:67], v[180:183]\n"
      "ds_read_b128 v[244:247], v202\n"
      "ds_read_b64_tr_b16 v[56:57], v194\n"
      "ds_read_b64_tr_b16 v[58:59], v194 offset:768\n"
      "s_waitcnt lgkmcnt(7)\n"
      "v_mfma_f32_32x32x16_bf16 v[80:95], v[68:71], v[248:251], v[80:95]\n"
      "s_add_u32 m0, s82, 59392\n"
      "s_add_u32 s77, s88, 16384\n"
      "buffer_load_dwordx4 v29, s[24:27], s77 offen lds\n"
      "s_add_u32 s94, s94, s76\n"
      "s_lshl_b32 s76, s62, 5\n"
      "ds_read_b64_tr_b16 v[60:61], v194 offset:64\n"
      "ds_read_b64_tr_b16 v[62:63], v194 offset:832\n"
      "s_waitcnt lgkmcnt(7)\n"
      "v_mfma_f32_32x32x16_bf16 v[96:111], v[72:75], v[248:251], v[96:111]\n"
      "s_add_u32 m0, s82, 60416\n"
      "s_add_u32 s77, s88, 24576\n"
      "buffer_load_dwordx4 v30, s[24:27], s77 offen lds\n"
      "s_add_u32 s94, s94, s76\n"
      "ds_read_b64_tr_b16 v[64:65], v194 offset:128\n"
      "ds_read_b64_tr_b16 v[66:67], v194 offset:896\n"
      "s_waitcnt lgkmcnt(7)\n"
      "v_mfma_f32_32x32x16_bf16 v[112:127], v[248:251], v[76:79], v[112:127]\n"
      "ds_read_b128 v[248:251], v203\n"
      "ds_read_b64_tr_b16 v[68:69], v195\n"
      "ds_read_b64_tr_b16 v[70:71], v195 offset:768\n"
      "s_waitcnt lgkmcnt(7)\n"
      "v_mfma_f32_32x32x16_bf16 v[80:95], v[56:59], v[244:247], v[80:95]\n"
      "s_add_u32 m0, s84, 57344\n"
      "s_mul_i32 s78, s89, 196608\n"
      "s_add_u32 s78, s78, s83\n"
      "buffer_load_dwordx4 v26, s[28:31], s78 offen lds\n"
      "ds_read_b64_tr_b16 v[72:73], v195 offset:64\n"
      "ds_read_b64_tr_b16 v[74:75], v195 offset:832\n"
      "s_waitcnt lgkmcnt(7)\n"
      "v_mfma_f32_32x32x16_bf16 v[96:111], v[60:63], v[244:247], v[96:111]\n"
      "buffer_load_dwordx4 v27, s[28:31], s78 offen offset:1024 lds\n"
      "ds_read_b64_tr_b16 v[76:77], v195 offset:128\n"
      "ds_read_b64_tr_b16 v[78:79], v195 offset:896\n"
      "s_waitcnt lgkmcnt(7)\n"
      "v_mfma_f32_32x32x16_bf16 v[112:127], v[244:247], v[64:67], v[112:127]\n"
      "s_waitcnt lgkmcnt(4)\n"
      "v_mfma_f32_32x32x16_bf16 v[80:95], v[68:71], v[248:251], v[80:95]\n"
      "buffer_load_dwordx4 v28, s[28:31], s78 offen offset:2048 lds\n"
      "buffer_load_dwordx4 v[164:167], v48, s[44:47], 0 offen sc1\n"
      "s_waitcnt vmcnt(8) lgkmcnt(0)\n"
      "s_barrier\n"
      "ds_read_b128 v[244:247], v200 offset:28672\n"
      "ds_read_b64_tr_b16 v[56:57], v192 offset:28672\n"
      "ds_read_b64_tr_b16 v[58:59], v192 offset:29440\n"
      "ds_read_b64_tr_b16 v[60:61], v192 offset:28736\n"
      "ds_read_b64_tr_b16 v[62:63], v192 offset:29504\n"
      "ds_read_b64_tr_b16 v[64:65], v192 offset:28800\n"
      "ds_read_b64_tr_b16 v[66:67], v192 offset:29568\n"
      "v_mfma_f32_32x32x16_bf16 v[96:111], v[72:75], v[248:251], v[96:111]\n"
      "v_mfma_f32_32x32x16_bf16 v[112:127], v[248:251], v[76:79], v[112:127]\n"
      "ds_read_b128 v[248:251], v201 offset:28672\n"
      "ds_read_b64_tr_b16 v[68:69], v193 offset:28672\n"
      "ds_read_b64_tr_b16 v[70:71], v193 offset:29440\n"
      "s_waitcnt lgkmcnt(7)\n"
      "v_mfma_f32_32x32x16_bf16 v[80:95], v[56:59], v[244:247], v[80:95]\n"
      "s_add_u32 s89, s87, 7\n"
      "s_and_b32 s89, s89, 7\n"
      "s_lshl_b32 s88, s89, 7\n"
      "s_add_u32 s88, s88, s81\n"
      "s_add_u32 m0, s82, 86016\n"
      "s_add_u32 s77, s88, 0\n"
      "buffer_load_dwordx4 v29, s[24:27], s77 offen lds\n"
      "v_mfma_f32_16x16x32_bf16 v[172:175], v[168:171], v[56:59], v[172:175]\n"
      "ds_read_b64_tr_b16 v[72:73], v193 offset:28736\n"
      "ds_read_b64_tr_b16 v[74:75], v193 offset:29504\n"
      "s_waitcnt lgkmcnt(7)\n"
      "v_mfma_f32_32x32x16_bf16 v[96:111], v[60:63], v[244:247], v[96:111]\n"
      "s_add_u32 m0, s82, 87040\n"
      "s_add_u32 s77, s88, 8192\n"
      "buffer_load_dwordx4 v30, s[24:27], s77 offen lds\n"
      "v_mfma_f32_16x16x32_bf16 v[176:179], v[168:171], v[60:63], v[176:179]\n"
      "ds_read_b64_tr_b16 v[76:77], v193 offset:28800\n"
      "ds_read_b64_tr_b16 v[78:79], v193 offset:29568\n"
      "s_waitcnt lgkmcnt(7)\n"
      "v_mfma_f32_32x32x16_bf16 v[112:127], v[244:247], v[64:67], v[112:127]\n"
      "v_mfma_f32_16x16x32_bf16 v[180:183], v[168:171], v[64:67], v[180:183]\n"
      "ds_read_b128 v[244:247], v202 offset:28672\n"
      "ds_read_b64_tr_b16 v[56:57], v194 offset:28672\n"
      "ds_read_b64_tr_b16 v[58:59], v194 offset:29440\n"
      "s_waitcnt lgkmcnt(7)\n"
      "v_mfma_f32_32x32x16_bf16 v[80:95], v[68:71], v[248:251], v[80:95]\n"
      "s_add_u32 m0, s82, 88064\n"
      "s_add_u32 s77, s88, 16384\n"
      "buffer_load_dwordx4 v29, s[24:27], s77 offen lds\n"
      "ds_read_b64_tr_b16 v[60:61], v194 offset:28736\n"
      "ds_read_b64_tr_b16 v[62:63], v194 offset:29504\n"
      "s_waitcnt lgkmcnt(7)\n"
      "v_mfma_f32_32x32x16_bf16 v[96:111], v[72:75], v[248:251], v[96:111]\n"
      "s_add_u32 m0, s82, 89088\n"
      "s_add_u32 s77, s88, 24576\n"
      "buffer_load_dwordx4 v30, s[24:27], s77 offen lds\n"
      "ds_read_b64_tr_b16 v[64:65], v194 offset:28800\n"
      "ds_read_b64_tr_b16 v[66:67], v194 offset:29568\n"
      "s_waitcnt lgkmcnt(7)\n"
      "v_mfma_f32_32x32x16_bf16 v[112:127], v[248:251], v[76:79], v[112:127]\n"
      "ds_read_b128 v[248:251], v203 offset:28672\n"
      "ds_read_b64_tr_b16 v[68:69], v195 offset:28672\n"
      "ds_read_b64_tr_b16 v[70:71], v195 offset:29440\n"
      "s_waitcnt lgkmcnt(7)\n"
      "v_mfma_f32_32x32x16_bf16 v[80:95], v[56:59], v[244:247], v[80:95]\n"
      "s_add_u32 m0, s84, 86016\n"
      "s_mul_i32 s78, s89, 196608\n"
      "s_add_u32 s78, s78, s83\n"
      "buffer_load_dwordx4 v26, s[28:31], s78 offen lds\n"
      "ds_read_b64_tr_b16 v[72:73], v195 offset:28736\n"
      "ds_read_b64_tr_b16 v[74:75], v195 offset:29504\n"
      "s_waitcnt lgkmcnt(7)\n"
      "v_mfma_f32_32x32x16_bf16 v[96:111], v[60:63], v[244:247], v[96:111]\n"
      "buffer_load_dwordx4 v27, s[28:31], s78 offen offset:1024 lds\n"
      "ds_read_b64_tr_b16 v[76:77], v195 offset:28800\n"
      "ds_read_b64_tr_b16 v[78:79], v195 offset:29568\n"
      "s_waitcnt lgkmcnt(7)\n"
      "v_mfma_f32_32x32x16_bf16 v[112:127], v[244:247], v[64:67], v[112:127]\n"
      "s_waitcnt lgkmcnt(4)\n"
      "v_mfma_f32_32x32x16_bf16 v[80:95], v[68:71], v[248:251], v[80:95]\n"
      "buffer_load_dwordx4 v28, s[28:31], s78 offen offset:2048 lds\n"
      "s_waitcnt vmcnt(8) lgkmcnt(0)\n"
      "s_barrier\n"
      "ds_read_b128 v[244:247], v204\n"
      "ds_read_b64_tr_b16 v[56:57], v196\n"
      "ds_read_b64_tr_b16 v[58:59], v196 offset:768\n"
      "ds_read_b64_tr_b16 v[60:61], v196 offset:64\n"
      "ds_read_b64_tr_b16 v[62:63], v196 offset:832\n"
      "ds_read_b64_tr_b16 v[64:65], v196 offset:128\n"
      "ds_read_b64_tr_b16 v[66:67], v196 offset:896\n"
      "v_mfma_f32_32x32x16_bf16 v[96:111], v[72:75], v[248:251], v[96:111]\n"
      "v_mfma_f32_32x32x16_bf16 v[112:127], v[248:251], v[76:79], v[112:127]\n"
      "ds_read_b128 v[248:251], v205\n"
      "ds_read_b64_tr_b16 v[68:69], v197\n"
      "ds_read_b64_tr_b16 v[70:71], v197 offset:768\n"
      "s_waitcnt lgkmcnt(7)\n"
      "v_mfma_f32_32x32x16_bf16 v[80:95], v[56:59], v[244:247], v[80:95]\n"
      "s_mov_b64 exec, 65535\n"
      "buffer_load_dword a255, v45, s[32:35], s95 offen\n"
      "s_mov_b64 exec, -1\n"
      "v_mfma_f32_16x16x32_bf16 v[172:175], v[168:171], v[56:59], v[172:175]\n"
      "ds_read_b64_tr_b16 v[72:73], v197 offset:64\n"
      "ds_read_b64_tr_b16 v[74:75], v197 offset:832\n"
      "s_waitcnt lgkmcnt(7)\n"
      "v_mfma_f32_32x32x16_bf16 v[96:111], v[60:63], v[244:247], v[96:111]\n"
      "s_add_u32 m0, s91, 0\n"
      "s_add_u32 s77, s90, 0\n"
      "s_mov_b32 exec_hi, 0xfff\n"
      "buffer_load_dwordx4 v1, s[36:39], s77 offen nt lds\n"
      "s_mov_b32 exec_hi, -1\n"
      "v_mfma_f32_16x16x32_bf16 v[176:179], v[168:171], v[60:63], v[176:179]\n"
      "ds_read_b64_tr_b16 v[76:77], v197 offset:128\n"
      "ds_read_b64_tr_b16 v[78:79], v197 offset:896\n"
      "s_waitcnt lgkmcnt(7)\n"
      "v_mfma_f32_32x32x16_bf16 v[112:127], v[244:247], v[64:67], v[112:127]\n"
      "s_add_u32 m0, s91, 704\n"
      "s_add_u32 s77, s90, 2752\n"
      "s_mov_b32 exec_hi, 0xfff\n"
      "buffer_load_dwordx4 v1, s[36:39], s77 offen nt lds\n"
      "s_mov_b32 exec_hi, -1\n"
      "v_mfma_f32_16x16x32_bf16 v[180:183], v[168:171], v[64:67], v[180:183]\n"
      "ds_read_b128 v[244:247], v206\n"
      "ds_read_b64_tr_b16 v[56:57], v198\n"
      "ds_read_b64_tr_b16 v[58:59], v198 offset:768\n"
      "s_waitcnt lgkmcnt(7)\n"
      "v_mfma_f32_32x32x16_bf16 v[80:95], v[68:71], v[248:251], v[80:95]\n"
      "s_add_u32 m0, s91, 1408\n"
      "s_add_u32 s77, s90, 5504\n"
      "s_mov_b32 exec_hi, 0xfff\n"
      "buffer_load_dwordx4 v1, s[36:39], s77 offen nt lds\n"
      "s_mov_b32 exec_hi, -1\n"
      "ds_read_b64_tr_b16 v[60:61], v198 offset:64\n"
      "ds_read_b64_tr_b16 v[62:63], v198 offset:832\n"
      "s_waitcnt lgkmcnt(7)\n"
      "v_mfma_f32_32x32x16_bf16 v[96:111], v[72:75], v[248:251], v[96:111]\n"
      "s_add_u32 m0, s91, 2112\n"
      "s_add_u32 s77, s90, 8256\n"
      "s_mov_b32 exec_hi, 0xfff\n"
      "buffer_load_dwordx4 v1, s[36:39], s77 offen nt lds\n"
      "s_mov_b32 exec_hi, -1\n"
      "ds_read_b64_tr_b16 v[64:65], v198 offset:128\n"
      "ds_read_b64_tr_b16 v[66:67], v198 offset:896\n"
      "s_waitcnt lgkmcnt(7)\n"
      "v_mfma_f32_32x32x16_bf16 v[112:127], v[248:251], v[76:79], v[112:127]\n"
      "s_add_u32 m0, s91, 2816\n"
      "s_add_u32 s77, s90, 11008\n"
      "s_mov_b32 exec_hi, 0xfff\n"
      "buffer_load_dwordx4 v1, s[36:39], s77 offen nt lds\n"
      "s_mov_b32 exec_hi, -1\n"
      "ds_read_b128 v[248:251], v207\n"
      "ds_read_b64_tr_b16 v[68:69], v199\n"
      "ds_read_b64_tr_b16 v[70:71], v199 offset:768\n"
      "s_waitcnt lgkmcnt(7)\n"
      "v_mfma_f32_32x32x16_bf16 v[80:95], v[56:59], v[244:247], v[80:95]\n"
      "s_add_u32 m0, s91, 3520\n"
      "s_add_u32 s77, s90, 13760\n"
      "s_mov_b32 exec_hi, 0xfff\n"
      "buffer_load_dwordx4 v1, s[36:39], s77 offen nt lds\n"
      "s_mov_b32 exec_hi, -1\n"
      "s_waitcnt vmcnt(14)\n"
      "v_xor_b32 v20, s60, v165\n"
      "v_xor_b32 v21, s61, v167\n"
      "v_or_b32 v20, v20, v21\n"
      "v_cmp_ne_u32_e32 vcc, 0, v20\n"
      "s_cbranch_vccz .Lgok_8_%=\n"
      "s_mov_b32 s96, 131072\n"
      ".Lgspin_7_%=:\n"
      "buffer_load_dwordx4 v[164:167], v48, s[44:47], 0 offen sc1\n"
      "s_waitcnt vmcnt(0)\n"
      "v_xor_b32 v20, s60, v165\n"
      "v_xor_b32 v21, s61, v167\n"
      "v_or_b32 v20, v20, v21\n"
      "v_cmp_ne_u32_e32 vcc, 0, v20\n"
      "s_cbranch_vccz .Lgok_8_%=\n"
      "s_sub_u32 s96, s96, 1\n"
      "s_cbranch_scc0 .Lgspin_7_%=\n"
      ".Lgok_8_%=:\n"
      "v_mul_f32 v21, 989855744, v164\n"
      "v_mul_f32 v20, 989855744, v166\n"
      "ds_read_b64_tr_b16 v[72:73], v199 offset:64\n"
      "ds_read_b64_tr_b16 v[74:75], v199 offset:832\n"
      "s_waitcnt lgkmcnt(7)\n"
      "v_mfma_f32_32x32x16_bf16 v[96:111], v[60:63], v[244:247], v[96:111]\n"
      "s_add_u32 m0, s91, 4224\n"
      "s_add_u32 s77, s90, 16512\n"
      "s_mov_b32 exec_hi, 0xfff\n"
      "buffer_load_dwordx4 v1, s[36:39], s77 offen nt lds\n"
      "s_mov_b32 exec_hi, -1\n"
      "v_fma_f32 v20, -v21, v21, v20\n"
      "v_mul_f32 v43, -0.5, v21\n"
      "v_max_f32 v20, 0, v20\n"
      "ds_read_b64_tr_b16 v[76:77], v199 offset:128\n"
      "ds_read_b64_tr_b16 v[78:79], v199 offset:896\n"
      "s_waitcnt lgkmcnt(7)\n"
      "v_mfma_f32_32x32x16_bf16 v[112:127], v[244:247], v[64:67], v[112:127]\n"
      "s_add_u32 m0, s91, 4928\n"
      "s_add_u32 s77, s90, 19264\n"
      "s_mov_b32 exec_hi, 0xfff\n"
      "buffer_load_dwordx4 v1, s[36:39], s77 offen nt lds\n"
      "s_mov_b32 exec_hi, -1\n"
      "v_add_f32 v20, 925353388, v20\n"
      "v_rsq_f32 v44, v20\n"
      "s_waitcnt lgkmcnt(4)\n"
      "v_mfma_f32_32x32x16_bf16 v[80:95], v[68:71], v[248:251], v[80:95]\n"
      "v_mul_f32 v236, s10, v44\n"
      "s_waitcnt vmcnt(9) lgkmcnt(0)\n"
      "s_barrier\n"
      "ds_read_b128 v[244:247], v204 offset:28672\n"
      "ds_read_b64_tr_b16 v[56:57], v196 offset:28672\n"
      "ds_read_b64_tr_b16 v[58:59], v196 offset:29440\n"
      "ds_read_b64_tr_b16 v[60:61], v196 offset:28736\n"
      "ds_read_b64_tr_b16 v[62:63], v196 offset:29504\n"
      "ds_read_b64_tr_b16 v[64:65], v196 offset:28800\n"
      "ds_read_b64_tr_b16 v[66:67], v196 offset:29568\n"
      "v_mfma_f32_32x32x16_bf16 v[96:111], v[72:75], v[248:251], v[96:111]\n"
      "v_mul_f32 v238, s11, v44\n"
      "v_mfma_f32_32x32x16_bf16 v[112:127], v[248:251], v[76:79], v[112:127]\n"
      "ds_write_b32 v41, v44\n"
      "ds_read_b128 v[248:251], v205 offset:28672\n"
      "ds_read_b64_tr_b16 v[68:69], v197 offset:28672\n"
      "ds_read_b64_tr_b16 v[70:71], v197 offset:29440\n"
      "s_waitcnt lgkmcnt(8)\n"
      "v_mfma_f32_32x32x16_bf16 v[80:95], v[56:59], v[244:247], v[80:95]\n"
      "v_mfma_f32_16x16x32_bf16 v[172:175], v[168:171], v[56:59], v[172:175]\n"
      "ds_read_b64_tr_b16 v[72:73], v197 offset:28736\n"
      "ds_read_b64_tr_b16 v[74:75], v197 offset:29504\n"
      "s_waitcnt lgkmcnt(8)\n"
      "v_mfma_f32_32x32x16_bf16 v[96:111], v[60:63], v[244:247], v[96:111]\n"
      "v_mfma_f32_16x16x32_bf16 v[176:179], v[168:171], v[60:63], v[176:179]\n"
      "ds_read_b64_tr_b16 v[76:77], v197 offset:28800\n"
      "ds_read_b64_tr_b16 v[78:79], v197 offset:29568\n"
      "s_waitcnt lgkmcnt(8)\n"
      "v_mfma_f32_32x32x16_bf16 v[112:127], v[244:247], v[64:67], v[112:127]\n"
      "v_mfma_f32_16x16x32_bf16 v[180:183], v[168:171], v[64:67], v[180:183]\n"
      "ds_read_b128 v[244:247], v206 offset:28672\n"
      "ds_read_b64_tr_b16 v[56:57], v198 offset:28672\n"
      "ds_read_b64_tr_b16 v[58:59], v198 offset:29440\n"
      "s_waitcnt lgkmcnt(7)\n"
      "v_mfma_f32_32x32x16_bf16 v[80:95], v[68:71], v[248:251], v[80:95]\n"
      "s_mov_b64 exec, 65535\n"
      "ds_write_b32 v39, v172\n"
      "ds_write_b32 v39, v173 offset:256\n"
      "ds_write_b32 v39, v176 offset:512\n"
      "ds_write_b32 v39, v177 offset:768\n"
      "ds_write_b32 v39, v180 offset:1024\n"
      "ds_write_b32 v39, v181 offset:1280\n"
      "s_mov_b64 exec, -1\n"
      "ds_read_b64_tr_b16 v[60:61], v198 offset:28736\n"
      "ds_read_b64_tr_b16 v[62:63], v198 offset:29504\n"
      "s_waitcnt lgkmcnt(13)\n"
      "v_mfma_f32_32x32x16_bf16 v[96:111], v[72:75], v[248:251], v[96:111]\n"
      "ds_read_b128 v[220:223], v42\n"
      "ds_read_b128 v[224:227], v42 offset:32\n"
      "ds_read_b128 v[228:231], v42 offset:64\n"
      "ds_read_b128 v[232:235], v42 offset:96\n"
      "ds_read_b64_tr_b16 v[64:65], v198 offset:28800\n"
      "ds_read_b64_tr_b16 v[66:67], v198 offset:29568\n"
      "s_waitcnt lgkmcnt(15)\n"
      "v_mfma_f32_32x32x16_bf16 v[112:127], v[248:251], v[76:79], v[112:127]\n"
      "s_waitcnt lgkmcnt(0)\n"
      "s_barrier\n"
      "ds_read_b128 v[208:211], v40\n"
      "ds_read_b128 v[212:215], v40 offset:512\n"
      "ds_read_b128 v[216:219], v40 offset:1024\n"
      "ds_read_b128 v[248:251], v207 offset:28672\n"
      "ds_read_b64_tr_b16 v[68:69], v199 offset:28672\n"
      "ds_read_b64_tr_b16 v[70:71], v199 offset:29440\n"
      "v_mfma_f32_32x32x16_bf16 v[80:95], v[56:59], v[244:247], v[80:95]\n"
      "ds_read_b64_tr_b16 v[72:73], v199 offset:28736\n"
      "ds_read_b64_tr_b16 v[74:75], v199 offset:29504\n"
      "v_mfma_f32_32x32x16_bf16 v[96:111], v[60:63], v[244:247], v[96:111]\n"
      "ds_read_b64_tr_b16 v[76:77], v199 offset:28800\n"
      "ds_read_b64_tr_b16 v[78:79], v199 offset:29568\n"
      "v_mfma_f32_32x32x16_bf16 v[112:127], v[244:247], v[64:67], v[112:127]\n"
      "s_waitcnt lgkmcnt(9)\n"
      "v_add_f32 v208, v208, v209\n"
      "v_add_f32 v210, v210, v211\n"
      "v_add_f32 v35, v208, v210\n"
      "s_waitcnt lgkmcnt(8)\n"
      "v_add_f32 v212, v212, v213\n"
      "v_add_f32 v214, v214, v215\n"
      "v_add_f32 v36, v212, v214\n"
      "s_waitcnt lgkmcnt(7)\n"
      "v_add_f32 v216, v216, v217\n"
      "v_add_f32 v218, v218, v219\n"
      "v_add_f32 v37, v216, v218\n"
      "v_mfma_f32_32x32x2_f32 v[80:95], v35, v43, v[80:95]\n"
      "v_mfma_f32_32x32x2_f32 v[96:111], v36, v43, v[96:111]\n"
      "v_mfma_f32_32x32x2_f32 v[112:127], v43, v37, v[112:127]\n"
      "s_waitcnt lgkmcnt(4)\n"
      "v_mfma_f32_32x32x16_bf16 v[80:95], v[68:71], v[248:251], v[80:95]\n"
      "s_waitcnt lgkmcnt(2)\n"
      "v_mfma_f32_32x32x16_bf16 v[96:111], v[72:75], v[248:251], v[96:111]\n"
      "s_waitcnt lgkmcnt(0)\n"
      "v_mfma_f32_32x32x16_bf16 v[112:127], v[248:251], v[76:79], v[112:127]\n"
      "s_nop 9\n"
      "v_pk_mul_f32 v[80:81], v[80:81], v[236:237] op_sel_hi:[1,0]\n"
      "v_pk_mul_f32 v[82:83], v[82:83], v[236:237] op_sel_hi:[1,0]\n"
      "v_pk_mul_f32 v[84:85], v[84:85], v[236:237] op_sel_hi:[1,0]\n"
      "v_pk_mul_f32 v[86:87], v[86:87], v[236:237] op_sel_hi:[1,0]\n"
      "v_pk_mul_f32 v[88:89], v[88:89], v[236:237] op_sel_hi:[1,0]\n"
      "v_pk_mul_f32 v[90:91], v[90:91], v[236:237] op_sel_hi:[1,0]\n"
      "v_pk_mul_f32 v[92:93], v[92:93], v[236:237] op_sel_hi:[1,0]\n"
      "v_pk_mul_f32 v[94:95], v[94:95], v[236:237] op_sel_hi:[1,0]\n"
      "v_cvt_pk_bf16_f32 v56, v80, v81\n"
      "v_cvt_pk_bf16_f32 v57, v82, v83\n"
      "v_cvt_pk_bf16_f32 v58, v84, v85\n"
      "v_cvt_pk_bf16_f32 v59, v86, v87\n"
      "buffer_store_dwordx4 v[56:59], v1, s[44:47], s4 offen sc1\n"
      "v_cvt_pk_bf16_f32 v60, v88, v89\n"
      "v_cvt_pk_bf16_f32 v61, v90, v91\n"
      "v_cvt_pk_bf16_f32 v62, v92, v93\n"
      "v_cvt_pk_bf16_f32 v63, v94, v95\n"
      "buffer_store_dwordx4 v[60:63], v1, s[44:47], s5 offen sc1\n"
      "v_pk_mul_f32 v[96:97], v[96:97], v[238:239] op_sel_hi:[1,0]\n"
      "v_pk_mul_f32 v[98:99], v[98:99], v[238:239] op_sel_hi:[1,0]\n"
      "v_pk_mul_f32 v[100:101], v[100:101], v[238:239] op_sel_hi:[1,0]\n"
      "v_pk_mul_f32 v[102:103], v[102:103], v[238:239] op_sel_hi:[1,0]\n"
      "v_pk_mul_f32 v[104:105], v[104:105], v[238:239] op_sel_hi:[1,0]\n"
      "v_pk_mul_f32 v[106:107], v[106:107], v[238:239] op_sel_hi:[1,0]\n"
      "v_pk_mul_f32 v[108:109], v[108:109], v[238:239] op_sel_hi:[1,0]\n"
      "v_pk_mul_f32 v[110:111], v[110:111], v[238:239] op_sel_hi:[1,0]\n"
      "v_cvt_pk_bf16_f32 v64, v96, v97\n"
      "v_cvt_pk_bf16_f32 v65, v98, v99\n"
      "v_cvt_pk_bf16_f32 v66, v100, v101\n"
      "v_cvt_pk_bf16_f32 v67, v102, v103\n"
      "buffer_store_dwordx4 v[64:67], v1, s[44:47], s6 offen sc1\n"
      "v_cvt_pk_bf16_f32 v68, v104, v105\n"
      "v_cvt_pk_bf16_f32 v69, v106, v107\n"
      "v_cvt_pk_bf16_f32 v70, v108, v109\n"
      "v_cvt_pk_bf16_f32 v71, v110, v111\n"
      "buffer_store_dwordx4 v[68:71], v1, s[44:47], s7 offen sc1\n"
      "v_pk_mul_f32 v[112:113], v[112:113], v[220:221]\n"
      "v_pk_mul_f32 v[114:115], v[114:115], v[222:223]\n"
      "v_pk_mul_f32 v[116:117], v[116:117], v[224:225]\n"
      "v_pk_mul_f32 v[118:119], v[118:119], v[226:227]\n"
      "v_pk_mul_f32 v[120:121], v[120:121], v[228:229]\n"
      "v_pk_mul_f32 v[122:123], v[122:123], v[230:231]\n"
      "v_pk_mul_f32 v[124:125], v[124:125], v[232:233]\n"
      "v_pk_mul_f32 v[126:127], v[126:127], v[234:235]\n"
      "v_cvt_pk_bf16_f32 v72, v112, v113\n"
      "v_cvt_pk_bf16_f32 v73, v114, v115\n"
      "v_cvt_pk_bf16_f32 v74, v116, v117\n"
      "v_cvt_pk_bf16_f32 v75, v118, v119\n"
      "buffer_store_dwordx4 v[72:75], v38, s[44:47], s8 offen sc1\n"
      "v_cvt_pk_bf16_f32 v76, v120, v121\n"
      "v_cvt_pk_bf16_f32 v77, v122, v123\n"
      "v_cvt_pk_bf16_f32 v78, v124, v125\n"
      "v_cvt_pk_bf16_f32 v79, v126, v127\n"
      "buffer_store_dwordx4 v[76:79], v38, s[44:47], s8 offen offset:512 sc1\n"
      "s_nop 1\n"
      "s_branch .Lp1end_4_%=\n"
      ".Lp1VVV_3_%=:\n"
      "s_waitcnt vmcnt(8)\n"
      "s_barrier\n"
      "ds_read_b128 v[244:247], v200\n"
      "ds_read_b64_tr_b16 v[56:57], v192\n"
      "ds_read_b64_tr_b16 v[58:59], v192 offset:768\n"
      "ds_read_b64_tr_b16 v[60:61], v192 offset:64\n"
      "ds_read_b64_tr_b16 v[62:63], v192 offset:832\n"
      "ds_read_b64_tr_b16 v[64:65], v192 offset:128\n"
      "ds_read_b64_tr_b16 v[66:67], v192 offset:896\n"
      "ds_read_b128 v[248:251], v201\n"
      "ds_read_b64_tr_b16 v[68:69], v193\n"
      "ds_read_b64_tr_b16 v[70:71], v193 offset:768\n"
      "s_waitcnt lgkmcnt(7)\n"
      "v_mfma_f32_32x32x16_bf16 v[80:95], v[244:247], v[56:59], 0\n"
      "s_add_u32 s89, s87, 2\n"
      "s_and_b32 s89, s89, 7\n"
      "s_lshl_b32 s88, s89, 7\n"
      "s_add_u32 s88, s88, s81\n"
      "s_add_u32 m0, s82, 57344\n"
      "s_add_u32 s77, s88, 0\n"
      "buffer_load_dwordx4 v29, s[24:27], s77 offen lds\n"
      "s_mov_b32 s36, s10\n"
      "s_and_b32 s37, s11, 65535\n"
      "v_mfma_f32_16x16x32_bf16 v[172:175], v[168:171], v[56:59], 0\n"
      "ds_read_b64_tr_b16 v[72:73], v193 offset:64\n"
      "ds_read_b64_tr_b16 v[74:75], v193 offset:832\n"
      "s_waitcnt lgkmcnt(7)\n"
      "v_mfma_f32_32x32x16_bf16 v[96:111], v[244:247], v[60:63], 0\n"
      "s_add_u32 m0, s82, 58368\n"
      "s_add_u32 s77, s88, 8192\n"
      "buffer_load_dwordx4 v30, s[24:27], s77 offen lds\n"
      "s_mov_b32 s38, 5636096\n"
      "s_mov_b32 s39, 131072\n"
      "v_mfma_f32_16x16x32_bf16 v[176:179], v[168:171], v[60:63], 0\n"
      "ds_read_b64_tr_b16 v[76:77], v193 offset:128\n"
      "ds_read_b64_tr_b16 v[78:79], v193 offset:896\n"
      "s_waitcnt lgkmcnt(7)\n"
      "v_mfma_f32_32x32x16_bf16 v[112:127], v[244:247], v[64:67], 0\n"
      "s_and_b32 s75, s64, 7\n"
      "s_lshl_b32 s69, s79, 3\n"
      "v_mfma_f32_16x16x32_bf16 v[180:183], v[168:171], v[64:67], 0\n"
      "ds_read_b128 v[244:247], v202\n"
      "ds_read_b64_tr_b16 v[56:57], v194\n"
      "ds_read_b64_tr_b16 v[58:59], v194 offset:768\n"
      "s_waitcnt lgkmcnt(7)\n"
      "v_mfma_f32_32x32x16_bf16 v[80:95], v[248:251], v[68:71], v[80:95]\n"
      "s_add_u32 m0, s82, 59392\n"
      "s_add_u32 s77, s88, 16384\n"
      "buffer_load_dwordx4 v29, s[24:27], s77 offen lds\n"
      "s_add_u32 s69, s69, s75\n"
      "s_lshr_b32 s74, s64, 3\n"
      "ds_read_b64_tr_b16 v[60:61], v194 offset:64\n"
      "ds_read_b64_tr_b16 v[62:63], v194 offset:832\n"
      "s_waitcnt lgkmcnt(7)\n"
      "v_mfma_f32_32x32x16_bf16 v[96:111], v[248:251], v[72:75], v[96:111]\n"
      "s_add_u32 m0, s82, 60416\n"
      "s_add_u32 s77, s88, 24576\n"
      "buffer_load_dwordx4 v30, s[24:27], s77 offen lds\n"
      "s_lshl_b32 s70, s80, 2\n"
      "s_add_u32 s70, s70, s74\n"
      "ds_read_b64_tr_b16 v[64:65], v194 offset:128\n"
      "ds_read_b64_tr_b16 v[66:67], v194 offset:896\n"
      "s_waitcnt lgkmcnt(7)\n"
      "v_mfma_f32_32x32x16_bf16 v[112:127], v[248:251], v[76:79], v[112:127]\n"
      "s_cmp_eq_u32 s79, 3\n"
      "s_cselect_b32 s74, 4, 6\n"
      "s_min_u32 s73, s75, s74\n"
      "ds_read_b128 v[248:251], v203\n"
      "ds_read_b64_tr_b16 v[68:69], v195\n"
      "ds_read_b64_tr_b16 v[70:71], v195 offset:768\n"
      "s_waitcnt lgkmcnt(7)\n"
      "v_mfma_f32_32x32x16_bf16 v[80:95], v[244:247], v[56:59], v[80:95]\n"
      "s_add_u32 m0, s84, 57344\n"
      "s_mul_i32 s78, s89, 196608\n"
      "s_add_u32 s78, s78, s83\n"
      "buffer_load_dwordx4 v26, s[28:31], s78 offen lds\n"
      "s_mul_i32 s71, s79, 22\n"
      "s_lshl_b32 s76, s75, 1\n"
      "ds_read_b64_tr_b16 v[72:73], v195 offset:64\n"
      "ds_read_b64_tr_b16 v[74:75], v195 offset:832\n"
      "s_waitcnt lgkmcnt(7)\n"
      "v_mfma_f32_32x32x16_bf16 v[96:111], v[244:247], v[60:63], v[96:111]\n"
      "buffer_load_dwordx4 v27, s[28:31], s78 offen offset:1024 lds\n"
      "s_add_u32 s71, s71, s76\n"
      "s_add_u32 s71, s71, s73\n"
      "ds_read_b64_tr_b16 v[76:77], v195 offset:128\n"
      "ds_read_b64_tr_b16 v[78:79], v195 offset:896\n"
      "s_waitcnt lgkmcnt(7)\n"
      "v_mfma_f32_32x32x16_bf16 v[112:127], v[244:247], v[64:67], v[112:127]\n"
      "s_cmp_lt_u32 s75, s74\n"
      "s_cselect_b32 s72, 0, 1073741824\n"
      "s_mul_i32 s90, %[wg], 22016\n"
      "s_waitcnt lgkmcnt(4)\n"
      "v_mfma_f32_32x32x16_bf16 v[80:95], v[248:251], v[68:71], v[80:95]\n"
      "buffer_load_dwordx4 v28, s[28:31], s78 offen offset:2048 lds\n"
      "s_mul_i32 s76, s62, 704\n"
      "s_add_u32 s90, s90, s76\n"
      "s_waitcnt vmcnt(8) lgkmcnt(0)\n"
      "s_barrier\n"
      "ds_read_b128 v[244:247], v200 offset:28672\n"
      "ds_read_b64_tr_b16 v[56:57], v192 offset:28672\n"
      "ds_read_b64_tr_b16 v[58:59], v192 offset:29440\n"
      "ds_read_b64_tr_b16 v[60:61], v192 offset:28736\n"
      "ds_read_b64_tr_b16 v[62:63], v192 offset:29504\n"
      "ds_read_b64_tr_b16 v[64:65], v192 offset:28800\n"
      "ds_read_b64_tr_b16 v[66:67], v192 offset:29568\n"
      "v_mfma_f32_32x32x16_bf16 v[96:111], v[248:251], v[72:75], v[96:111]\n"
      "s_mov_b32 s32, s8\n"
      "s_and_b32 s33, s9, 65535\n"
      "v_mfma_f32_32x32x16_bf16 v[112:127], v[248:251], v[76:79], v[112:127]\n"
      "s_mov_b32 s34, 524288\n"
      "s_mov_b32 s35, 131072\n"
      "ds_read_b128 v[248:251], v201 offset:28672\n"
      "ds_read_b64_tr_b16 v[68:69], v193 offset:28672\n"
      "ds_read_b64_tr_b16 v[70:71], v193 offset:29440\n"
      "s_waitcnt lgkmcnt(7)\n"
      "v_mfma_f32_32x32x16_bf16 v[80:95], v[244:247], v[56:59], v[80:95]\n"
      "s_add_u32 s89, s87, 3\n"
      "s_and_b32 s89, s89, 7\n"
      "s_lshl_b32 s88, s89, 7\n"
      "s_add_u32 s88, s88, s81\n"
      "s_add_u32 m0, s82, 86016\n"
      "s_add_u32 s77, s88, 0\n"
      "buffer_load_dwordx4 v29, s[24:27], s77 offen lds\n"
      "s_and_b32 s73, s69, 7\n"
      "s_lshl_b32 s95, s73, 16\n"
      "v_mfma_f32_16x16x32_bf16 v[172:175], v[168:171], v[56:59], v[172:175]\n"
      "ds_read_b64_tr_b16 v[72:73], v193 offset:28736\n"
      "ds_read_b64_tr_b16 v[74:75], v193 offset:29504\n"
      "s_waitcnt lgkmcnt(7)\n"
      "v_mfma_f32_32x32x16_bf16 v[96:111], v[244:247], v[60:63], v[96:111]\n"
      "s_add_u32 m0, s82, 87040\n"
      "s_add_u32 s77, s88, 8192\n"
      "buffer_load_dwordx4 v30, s[24:27], s77 offen lds\n"
      "s_lshl_b32 s76, s62, 14\n"
      "s_add_u32 s95, s95, s76\n"
      "v_mfma_f32_16x16x32_bf16 v[176:179], v[168:171], v[60:63], v[176:179]\n"
      "ds_read_b64_tr_b16 v[76:77], v193 offset:28800\n"
      "ds_read_b64_tr_b16 v[78:79], v193 offset:29568\n"
      "s_waitcnt lgkmcnt(7)\n"
      "v_mfma_f32_32x32x16_bf16 v[112:127], v[244:247], v[64:67], v[112:127]\n"
      "s_lshl_b32 s76, s70, 7\n"
      "s_add_u32 s95, s95, s76\n"
      "v_mfma_f32_16x16x32_bf16 v[180:183], v[168:171], v[64:67], v[180:183]\n"
      "ds_read_b128 v[244:247], v202 offset:28672\n"
      "ds_read_b64_tr_b16 v[56:57], v194 offset:28672\n"
      "ds_read_b64_tr_b16 v[58:59], v194 offset:29440\n"
      "s_waitcnt lgkmcnt(7)\n"
      "v_mfma_f32_32x32x16_bf16 v[80:95], v[248:251], v[68:71], v[80:95]\n"
      "s_add_u32 m0, s82, 88064\n"
      "s_add_u32 s77, s88, 16384\n"
      "buffer_load_dwordx4 v29, s[24:27], s77 offen lds\n"
      "s_mul_i32 s91, s62, 12288\n"
      "s_add_u32 s91, s91, 114688\n"
      "ds_read_b64_tr_b16 v[60:61], v194 offset:28736\n"
      "ds_read_b64_tr_b16 v[62:63], v194 offset:29504\n"
      "s_waitcnt lgkmcnt(7)\n"
      "v_mfma_f32_32x32x16_bf16 v[96:111], v[248:251], v[72:75], v[96:111]\n"
      "s_add_u32 m0, s82, 89088\n"
      "s_add_u32 s77, s88, 24576\n"
      "buffer_load_dwordx4 v30, s[24:27], s77 offen lds\n"
      "s_lshl_b32 s92, s62, 14\n"
      "s_mov_b32 s40, s14\n"
      "ds_read_b64_tr_b16 v[64:65], v194 offset:28800\n"
      "ds_read_b64_tr_b16 v[66:67], v194 offset:29568\n"
      "s_waitcnt lgkmcnt(7)\n"
      "v_mfma_f32_32x32x16_bf16 v[112:127], v[248:251], v[76:79], v[112:127]\n"
      "s_and_b32 s41, s15, 65535\n"
      "s_mov_b32 s42, 5636096\n"
      "ds_read_b128 v[248:251], v203 offset:28672\n"
      "ds_read_b64_tr_b16 v[68:69], v195 offset:28672\n"
      "ds_read_b64_tr_b16 v[70:71], v195 offset:29440\n"
      "s_waitcnt lgkmcnt(7)\n"
      "v_mfma_f32_32x32x16_bf16 v[80:95], v[244:247], v[56:59], v[80:95]\n"
      "s_add_u32 m0, s84, 86016\n"
      "s_mul_i32 s78, s89, 196608\n"
      "s_add_u32 s78, s78, s83\n"
      "buffer_load_dwordx4 v26, s[28:31], s78 offen lds\n"
      "s_mov_b32 s43, 131072\n"
      "s_mul_i32 s93, s62, 1441792\n"
      "ds_read_b64_tr_b16 v[72:73], v195 offset:28736\n"
      "ds_read_b64_tr_b16 v[74:75], v195 offset:29504\n"
      "s_waitcnt lgkmcnt(7)\n"
      "v_mfma_f32_32x32x16_bf16 v[96:111], v[244:247], v[60:63], v[96:111]\n"
      "buffer_load_dwordx4 v27, s[28:31], s78 offen offset:1024 lds\n"
      "s_lshl_b32 s76, s69, 7\n"
      "s_add_u32 s93, s93, s76\n"
      "ds_read_b64_tr_b16 v[76:77], v195 offset:28800\n"
      "ds_read_b64_tr_b16 v[78:79], v195 offset:29568\n"
      "s_waitcnt lgkmcnt(7)\n"
      "v_mfma_f32_32x32x16_bf16 v[112:127], v[244:247], v[64:67], v[112:127]\n"
      "s_mov_b32 s52, s12\n"
      "s_and_b32 s53, s13, 65535\n"
      "s_waitcnt lgkmcnt(4)\n"
      "v_mfma_f32_32x32x16_bf16 v[80:95], v[248:251], v[68:71], v[80:95]\n"
      "buffer_load_dwordx4 v28, s[28:31], s78 offen offset:2048 lds\n"
      "s_mov_b32 s54, 2752\n"
      "s_mov_b32 s55, 131072\n"
      "s_waitcnt vmcnt(7) lgkmcnt(0)\n"
      "s_barrier\n"
      "ds_read_b128 v[244:247], v204\n"
      "ds_read_b64_tr_b16 v[56:57], v196\n"
      "ds_read_b64_tr_b16 v[58:59], v196 offset:768\n"
      "ds_read_b64_tr_b16 v[60:61], v196 offset:64\n"
      "ds_read_b64_tr_b16 v[62:63], v196 offset:832\n"
      "ds_read_b64_tr_b16 v[64:65], v196 offset:128\n"
      "ds_read_b64_tr_b16 v[66:67], v196 offset:896\n"
      "v_mfma_f32_32x32x16_bf16 v[96:111], v[248:251], v[72:75], v[96:111]\n"
      "s_mov_b32 s56, s16\n"
      "s_and_b32 s57, s17, 65535\n"
      "v_mfma_f32_32x32x16_bf16 v[112:127], v[248:251], v[76:79], v[112:127]\n"
      "s_mov_b32 s58, 4096\n"
      "s_mov_b32 s59, 131072\n"
      "ds_read_b128 v[248:251], v205\n"
      "ds_read_b64_tr_b16 v[68:69], v197\n"
      "ds_read_b64_tr_b16 v[70:71], v197 offset:768\n"
      "s_waitcnt lgkmcnt(7)\n"
      "v_mfma_f32_32x32x16_bf16 v[80:95], v[244:247], v[56:59], v[80:95]\n"
      "s_add_u32 s89, s87, 4\n"
      "s_and_b32 s89, s89, 7\n"
      "s_lshl_b32 s88, s89, 7\n"
      "s_add_u32 s88, s88, s81\n"
      "s_add_u32 m0, s82, 0\n"
      "s_add_u32 s77, s88, 0\n"
      "buffer_load_dwordx4 v29, s[24:27], s77 offen lds\n"
      "s_mov_b32 s48, s18\n"
      "s_and_b32 s49, s19, 65535\n"
      "v_mfma_f32_16x16x32_bf16 v[172:175], v[168:171], v[56:59], v[172:175]\n"
      "ds_read_b64_tr_b16 v[72:73], v197 offset:64\n"
      "ds_read_b64_tr_b16 v[74:75], v197 offset:832\n"
      "s_waitcnt lgkmcnt(7)\n"
      "v_mfma_f32_32x32x16_bf16 v[96:111], v[244:247], v[60:63], v[96:111]\n"
      "s_add_u32 m0, s82, 1024\n"
      "s_add_u32 s77, s88, 8192\n"
      "buffer_load_dwordx4 v30, s[24:27], s77 offen lds\n"
      "s_mov_b32 s50, 2097152\n"
      "s_mov_b32 s51, 131072\n"
      "v_mfma_f32_16x16x32_bf16 v[176:179], v[168:171], v[60:63], v[176:179]\n"
      "ds_read_b64_tr_b16 v[76:77], v197 offset:128\n"
      "ds_read_b64_tr_b16 v[78:79], v197 offset:896\n"
      "s_waitcnt lgkmcnt(7)\n"
      "v_mfma_f32_32x32x16_bf16 v[112:127], v[244:247], v[64:67], v[112:127]\n"
      "s_lshl_b32 s13, s65, 13\n"
      "s_lshl_b32 s76, s66, 9\n"
      "v_mfma_f32_16x16x32_bf16 v[180:183], v[168:171], v[64:67], v[180:183]\n"
      "ds_read_b128 v[244:247], v206\n"
      "ds_read_b64_tr_b16 v[56:57], v198\n"
      "ds_read_b64_tr_b16 v[58:59], v198 offset:768\n"
      "s_waitcnt lgkmcnt(7)\n"
      "v_mfma_f32_32x32x16_bf16 v[80:95], v[248:251], v[68:71], v[80:95]\n"
      "s_add_u32 m0, s82, 2048\n"
      "s_add_u32 s77, s88, 16384\n"
      "buffer_load_dwordx4 v29, s[24:27], s77 offen lds\n"
      "s_add_u32 s13, s13, s76\n"
      "s_lshl_b32 s76, s62, 7\n"
      "ds_read_b64_tr_b16 v[60:61], v198 offset:64\n"
      "ds_read_b64_tr_b16 v[62:63], v198 offset:832\n"
      "s_waitcnt lgkmcnt(7)\n"
      "v_mfma_f32_32x32x16_bf16 v[96:111], v[248:251], v[72:75], v[96:111]\n"
      "s_add_u32 m0, s82, 3072\n"
      "s_add_u32 s77, s88, 24576\n"
      "buffer_load_dwordx4 v30, s[24:27], s77 offen lds\n"
      "s_add_u32 s13, s13, s76\n"
      "s_lshl_b32 s73, s65, 12\n"
      "ds_read_b64_tr_b16 v[64:65], v198 offset:128\n"
      "ds_read_b64_tr_b16 v[66:67], v198 offset:896\n"
      "s_waitcnt lgkmcnt(7)\n"
      "v_mfma_f32_32x32x16_bf16 v[112:127], v[248:251], v[76:79], v[112:127]\n"
      "s_lshl_b32 s76, s62, 10\n"
      "s_add_u32 s73, s73, s76\n"
      "ds_read_b128 v[248:251], v207\n"
      "ds_read_b64_tr_b16 v[68:69], v199\n"
      "ds_read_b64_tr_b16 v[70:71], v199 offset:768\n"
      "s_waitcnt lgkmcnt(7)\n"
      "v_mfma_f32_32x32x16_bf16 v[80:95], v[244:247], v[56:59], v[80:95]\n"
      "s_add_u32 m0, s84, 0\n"
      "s_mul_i32 s78, s89, 196608\n"
      "s_add_u32 s78, s78, s83\n"
      "buffer_load_dwordx4 v26, s[28:31], s78 offen lds\n"
      "s_mul_i32 s76, s66, 393216\n"
      "s_add_u32 s73, s73, s76\n"
      "ds_read_b64_tr_b16 v[72:73], v199 offset:64\n"
      "ds_read_b64_tr_b16 v[74:75], v199 offset:832\n"
      "s_waitcnt lgkmcnt(7)\n"
      "v_mfma_f32_32x32x16_bf16 v[96:111], v[244:247], v[60:63], v[96:111]\n"
      "buffer_load_dwordx4 v27, s[28:31], s78 offen offset:1024 lds\n"
      "s_add_u32 s73, s73, 1048576\n"
      "s_mul_i32 s74, s66, 3\n"
      "ds_read_b64_tr_b16 v[76:77], v199 offset:128\n"
      "ds_read_b64_tr_b16 v[78:79], v199 offset:896\n"
      "s_waitcnt lgkmcnt(7)\n"
      "v_mfma_f32_32x32x16_bf16 v[112:127], v[244:247], v[64:67], v[112:127]\n"
      "s_add_u32 s4, s73, 0\n"
      "s_add_u32 s5, s4, 65536\n"
      "s_waitcnt lgkmcnt(4)\n"
      "v_mfma_f32_32x32x16_bf16 v[80:95], v[248:251], v[68:71], v[80:95]\n"
      "buffer_load_dwordx4 v28, s[28:31], s78 offen offset:2048 lds\n"
      "s_cmp_lt_u32 s74, 16\n"
      "s_cselect_b32 s10, 1052289595, 1065353216\n"
      "s_add_u32 s6, s73, 131072\n"
      "s_waitcnt vmcnt(7) lgkmcnt(0)\n"
      "s_barrier\n"
      "ds_read_b128 v[244:247], v204 offset:28672\n"
      "ds_read_b64_tr_b16 v[56:57], v196 offset:28672\n"
      "ds_read_b64_tr_b16 v[58:59], v196 offset:29440\n"
      "ds_read_b64_tr_b16 v[60:61], v196 offset:28736\n"
      "ds_read_b64_tr_b16 v[62:63], v196 offset:29504\n"
      "ds_read_b64_tr_b16 v[64:65], v196 offset:28800\n"
      "ds_read_b64_tr_b16 v[66:67], v196 offset:29568\n"
      "v_mfma_f32_32x32x16_bf16 v[96:111], v[248:251], v[72:75], v[96:111]\n"
      "s_add_u32 s7, s6, 65536\n"
      "s_cmp_lt_u32 s74, 15\n"
      "s_cselect_b32 s11, 1052289595, 1065353216\n"
      "v_mfma_f32_32x32x16_bf16 v[112:127], v[248:251], v[76:79], v[112:127]\n"
      "s_add_u32 s8, s73, 262144\n"
      "s_add_u32 s9, s8, 65536\n"
      "ds_read_b128 v[248:251], v205 offset:28672\n"
      "ds_read_b64_tr_b16 v[68:69], v197 offset:28672\n"
      "ds_read_b64_tr_b16 v[70:71], v197 offset:29440\n"
      "s_waitcnt lgkmcnt(7)\n"
      "v_mfma_f32_32x32x16_bf16 v[80:95], v[244:247], v[56:59], v[80:95]\n"
      "s_add_u32 s89, s87, 5\n"
      "s_and_b32 s89, s89, 7\n"
      "s_lshl_b32 s88, s89, 7\n"
      "s_add_u32 s88, s88, s81\n"
      "s_add_u32 m0, s82, 28672\n"
      "s_add_u32 s77, s88, 0\n"
      "buffer_load_dwordx4 v29, s[24:27], s77 offen lds\n"
      "s_cmp_lt_u32 s74, 14\n"
      "s_cselect_b32 s12, 1052289595, 1065353216\n"
      "s_lshl_b32 s14, %[wg], 8\n"
      "v_mfma_f32_16x16x32_bf16 v[172:175], v[168:171], v[56:59], v[172:175]\n"
      "ds_read_b64_tr_b16 v[72:73], v197 offset:28736\n"
      "ds_read_b64_tr_b16 v[74:75], v197 offset:29504\n"
      "s_waitcnt lgkmcnt(7)\n"
      "v_mfma_f32_32x32x16_bf16 v[96:111], v[244:247], v[60:63], v[96:111]\n"
      "s_add_u32 m0, s82, 29696\n"
      "s_add_u32 s77, s88, 8192\n"
      "buffer_load_dwordx4 v30, s[24:27], s77 offen lds\n"
      "s_mul_i32 s76, s62, 1441792\n"
      "s_add_u32 s14, s14, s76\n"
      "v_mfma_f32_16x16x32_bf16 v[176:179], v[168:171], v[60:63], v[176:179]\n"
      "ds_read_b64_tr_b16 v[76:77], v197 offset:28800\n"
      "ds_read_b64_tr_b16 v[78:79], v197 offset:29568\n"
      "s_waitcnt lgkmcnt(7)\n"
      "v_mfma_f32_32x32x16_bf16 v[112:127], v[244:247], v[64:67], v[112:127]\n"
      "s_add_u32 s14, s14, 14680064\n"
      "s_lshl_b32 s15, %[wg], 7\n"
      "v_mfma_f32_16x16x32_bf16 v[180:183], v[168:171], v[64:67], v[180:183]\n"
      "ds_read_b128 v[244:247], v206 offset:28672\n"
      "ds_read_b64_tr_b16 v[56:57], v198 offset:28672\n"
      "ds_read_b64_tr_b16 v[58:59], v198 offset:29440\n"
      "s_waitcnt lgkmcnt(7)\n"
      "v_mfma_f32_32x32x16_bf16 v[80:95], v[248:251], v[68:71], v[80:95]\n"
      "s_add_u32 m0, s82, 30720\n"
      "s_add_u32 s77, s88, 16384\n"
      "buffer_load_dwordx4 v29, s[24:27], s77 offen lds\n"
      "s_lshl_b32 s76, s62, 15\n"
      "s_add_u32 s15, s15, s76\n"
      "ds_read_b64_tr_b16 v[60:61], v198 offset:28736\n"
      "ds_read_b64_tr_b16 v[62:63], v198 offset:29504\n"
      "s_waitcnt lgkmcnt(7)\n"
      "v_mfma_f32_32x32x16_bf16 v[96:111], v[248:251], v[72:75], v[96:111]\n"
      "s_add_u32 m0, s82, 31744\n"
      "s_add_u32 s77, s88, 24576\n"
      "buffer_load_dwordx4 v30, s[24:27], s77 offen lds\n"
      "s_and_b32 s74, s64, 3\n"
      "s_lshl_b32 s67, s63, 2\n"
      "ds_read_b64_tr_b16 v[64:65], v198 offset:28800\n"
      "ds_read_b64_tr_b16 v[66:67], v198 offset:29568\n"
      "s_waitcnt lgkmcnt(7)\n"
      "v_mfma_f32_32x32x16_bf16 v[112:127], v[248:251], v[76:79], v[112:127]\n"
      "s_add_u32 s67, s67, s74\n"
      "s_min_u32 s22, s68, 1\n"
      "ds_read_b128 v[248:251], v207 offset:28672\n"
      "ds_read_b64_tr_b16 v[68:69], v199 offset:28672\n"
      "ds_read_b64_tr_b16 v[70:71], v199 offset:29440\n"
      "s_waitcnt lgkmcnt(7)\n"
      "v_mfma_f32_32x32x16_bf16 v[80:95], v[244:247], v[56:59], v[80:95]\n"
      "s_add_u32 m0, s84, 28672\n"
      "s_mul_i32 s78, s89, 196608\n"
      "s_add_u32 s78, s78, s83\n"
      "buffer_load_dwordx4 v26, s[28:31], s78 offen lds\n"
      "s_lshl_b32 s97, s68, 3\n"
      "s_lshl_b32 s76, s62, 1\n"
      "ds_read_b64_tr_b16 v[72:73], v199 offset:28736\n"
      "ds_read_b64_tr_b16 v[74:75], v199 offset:29504\n"
      "s_waitcnt lgkmcnt(7)\n"
      "v_mfma_f32_32x32x16_bf16 v[96:111], v[244:247], v[60:63], v[96:111]\n"
      "buffer_load_dwordx4 v27, s[28:31], s78 offen offset:1024 lds\n"
      "s_add_u32 s97, s97, s76\n"
      "s_lshl_b32 s23, s22, 3\n"
      "ds_read_b64_tr_b16 v[76:77], v199 offset:28800\n"
      "ds_read_b64_tr_b16 v[78:79], v199 offset:29568\n"
      "s_waitcnt lgkmcnt(7)\n"
      "v_mfma_f32_32x32x16_bf16 v[112:127], v[244:247], v[64:67], v[112:127]\n"
      "s_sub_u32 s23, s97, s23\n"
      "s_lshl_b32 s16, s67, 16\n"
      "s_waitcnt lgkmcnt(4)\n"
      "v_mfma_f32_32x32x16_bf16 v[80:95], v[248:251], v[68:71], v[80:95]\n"
      "buffer_load_dwordx4 v28, s[28:31], s78 offen offset:2048 lds\n"
      "s_lshl_b32 s76, s23, 10\n"
      "s_add_u32 s16, s16, s76\n"
      "s_waitcnt vmcnt(7) lgkmcnt(0)\n"
      "s_barrier\n"
      "ds_read_b128 v[244:247], v200\n"
      "ds_read_b64_tr_b16 v[56:57], v192\n"
      "ds_read_b64_tr_b16 v[58:59], v192 offset:768\n"
      "ds_read_b64_tr_b16 v[60:61], v192 offset:64\n"
      "ds_read_b64_tr_b16 v[62:63], v192 offset:832\n"
      "ds_read_b64_tr_b16 v[64:65], v192 offset:128\n"
      "ds_read_b64_tr_b16 v[66:67], v192 offset:896\n"
      "v_mfma_f32_32x32x16_bf16 v[96:111], v[248:251], v[72:75], v[96:111]\n"
      "s_add_u32 s16, s16, 3145728\n"
      "s_add_u32 s17, s16, 2097152\n"
      "v_mfma_f32_32x32x16_bf16 v[112:127], v[248:251], v[76:79], v[112:127]\n"
      "s_lshl_b32 s18, s67, 16\n"
      "s_lshl_b32 s76, s97, 10\n"
      "ds_read_b128 v[248:251], v201\n"
      "ds_read_b64_tr_b16 v[68:69], v193\n"
      "ds_read_b64_tr_b16 v[70:71], v193 offset:768\n"
      "s_waitcnt lgkmcnt(7)\n"
      "v_mfma_f32_32x32x16_bf16 v[80:95], v[244:247], v[56:59], v[80:95]\n"
      "s_add_u32 s89, s87, 6\n"
      "s_and_b32 s89, s89, 7\n"
      "s_lshl_b32 s88, s89, 7\n"
      "s_add_u32 s88, s88, s81\n"
      "s_add_u32 m0, s82, 57344\n"
      "s_add_u32 s77, s88, 0\n"
      "buffer_load_dwordx4 v29, s[24:27], s77 offen lds\n"
      "s_add_u32 s18, s18, s76\n"
      "s_add_u32 s18, s18, 1048576\n"
      "v_mfma_f32_16x16x32_bf16 v[172:175], v[168:171], v[56:59], v[172:175]\n"
      "ds_read_b64_tr_b16 v[72:73], v193 offset:64\n"
      "ds_read_b64_tr_b16 v[74:75], v193 offset:832\n"
      "s_waitcnt lgkmcnt(7)\n"
      "v_mfma_f32_32x32x16_bf16 v[96:111], v[244:247], v[60:63], v[96:111]\n"
      "s_add_u32 m0, s82, 58368\n"
      "s_add_u32 s77, s88, 8192\n"
      "buffer_load_dwordx4 v30, s[24:27], s77 offen lds\n"
      "s_lshl_b32 s19, s62, 11\n"
      "s_add_u32 s19, s19, 65536\n"
      "v_mfma_f32_16x16x32_bf16 v[176:179], v[168:171], v[60:63], v[176:179]\n"
      "ds_read_b64_tr_b16 v[76:77], v193 offset:128\n"
      "ds_read_b64_tr_b16 v[78:79], v193 offset:896\n"
      "s_waitcnt lgkmcnt(7)\n"
      "v_mfma_f32_32x32x16_bf16 v[112:127], v[244:247], v[64:67], v[112:127]\n"
      "s_lshl_b32 s94, s69, 10\n"
      "s_lshl_b32 s76, s70, 7\n"
      "v_mfma_f32_16x16x32_bf16 v[180:183], v[168:171], v[64:67], v[180:183]\n"
      "ds_read_b128 v[244:247], v202\n"
      "ds_read_b64_tr_b16 v[56:57], v194\n"
      "ds_read_b64_tr_b16 v[58:59], v194 offset:768\n"
      "s_waitcnt lgkmcnt(7)\n"
      "v_mfma_f32_32x32x16_bf16 v[80:95], v[248:251], v[68:71], v[80:95]\n"
      "s_add_u32 m0, s82, 59392\n"
      "s_add_u32 s77, s88, 16384\n"
      "buffer_load_dwordx4 v29, s[24:27], s77 offen lds\n"
      "s_add_u32 s94, s94, s76\n"
      "s_lshl_b32 s76, s62, 5\n"
      "ds_read_b64_tr_b16 v[60:61], v194 offset:64\n"
      "ds_read_b64_tr_b16 v[62:63], v194 offset:832\n"
      "s_waitcnt lgkmcnt(7)\n"
      "v_mfma_f32_32x32x16_bf16 v[96:111], v[248:251], v[72:75], v[96:111]\n"
      "s_add_u32 m0, s82, 60416\n"
      "s_add_u32 s77, s88, 24576\n"
      "buffer_load_dwordx4 v30, s[24:27], s77 offen lds\n"
      "s_add_u32 s94, s94, s76\n"
      "ds_read_b64_tr_b16 v[64:65], v194 offset:128\n"
      "ds_read_b64_tr_b16 v[66:67], v194 offset:896\n"
      "s_waitcnt lgkmcnt(7)\n"
      "v_mfma_f32_32x32x16_bf16 v[112:127], v[248:251], v[76:79], v[112:127]\n"
      "ds_read_b128 v[248:251], v203\n"
      "ds_read_b64_tr_b16 v[68:69], v195\n"
      "ds_read_b64_tr_b16 v[70:71], v195 offset:768\n"
      "s_waitcnt lgkmcnt(7)\n"
      "v_mfma_f32_32x32x16_bf16 v[80:95], v[244:247], v[56:59], v[80:95]\n"
      "s_add_u32 m0, s84, 57344\n"
      "s_mul_i32 s78, s89, 196608\n"
      "s_add_u32 s78, s78, s83\n"
      "buffer_load_dwordx4 v26, s[28:31], s78 offen lds\n"
      "ds_read_b64_tr_b16 v[72:73], v195 offset:64\n"
      "ds_read_b64_tr_b16 v[74:75], v195 offset:832\n"
      "s_waitcnt lgkmcnt(7)\n"
      "v_mfma_f32_32x32x16_bf16 v[96:111], v[244:247], v[60:63], v[96:111]\n"
      "buffer_load_dwordx4 v27, s[28:31], s78 offen offset:1024 lds\n"
      "ds_read_b64_tr_b16 v[76:77], v195 offset:128\n"
      "ds_read_b64_tr_b16 v[78:79], v195 offset:896\n"
      "s_waitcnt lgkmcnt(7)\n"
      "v_mfma_f32_32x32x16_bf16 v[112:127], v[244:247], v[64:67], v[112:127]\n"
      "s_waitcnt lgkmcnt(4)\n"
      "v_mfma_f32_32x32x16_bf16 v[80:95], v[248:251], v[68:71], v[80:95]\n"
      "buffer_load_dwordx4 v28, s[28:31], s78 offen offset:2048 lds\n"
      "buffer_load_dwordx4 v[164:167], v48, s[44:47], 0 offen sc1\n"
      "s_waitcnt vmcnt(8) lgkmcnt(0)\n"
      "s_barrier\n"
      "ds_read_b128 v[244:247], v200 offset:28672\n"
      "ds_read_b64_tr_b16 v[56:57], v192 offset:28672\n"
      "ds_read_b64_tr_b16 v[58:59], v192 offset:29440\n"
      "ds_read_b64_tr_b16 v[60:61], v192 offset:28736\n"
      "ds_read_b64_tr_b16 v[62:63], v192 offset:29504\n"
      "ds_read_b64_tr_b16 v[64:65], v192 offset:28800\n"
      "ds_read_b64_tr_b16 v[66:67], v192 offset:29568\n"
      "v_mfma_f32_32x32x16_bf16 v[96:111], v[248:251], v[72:75], v[96:111]\n"
      "v_mfma_f32_32x32x16_bf16 v[112:127], v[248:251], v[76:79], v[112:127]\n"
      "ds_read_b128 v[248:251], v201 offset:28672\n"
      "ds_read_b64_tr_b16 v[68:69], v193 offset:28672\n"
      "ds_read_b64_tr_b16 v[70:71], v193 offset:29440\n"
      "s_waitcnt lgkmcnt(7)\n"
      "v_mfma_f32_32x32x16_bf16 v[80:95], v[244:247], v[56:59], v[80:95]\n"
      "s_add_u32 s89, s87, 7\n"
      "s_and_b32 s89, s89, 7\n"
      "s_lshl_b32 s88, s89, 7\n"
      "s_add_u32 s88, s88, s81\n"
      "s_add_u32 m0, s82, 86016\n"
      "s_add_u32 s77, s88, 0\n"
      "buffer_load_dwordx4 v29, s[24:27], s77 offen lds\n"
      "v_mfma_f32_16x16x32_bf16 v[172:175], v[168:171], v[56:59], v[172:175]\n"
      "ds_read_b64_tr_b16 v[72:73], v193 offset:28736\n"
      "ds_read_b64_tr_b16 v[74:75], v193 offset:29504\n"
      "s_waitcnt lgkmcnt(7)\n"
      "v_mfma_f32_32x32x16_bf16 v[96:111], v[244:247], v[60:63], v[96:111]\n"
      "s_add_u32 m0, s82, 87040\n"
      "s_add_u32 s77, s88, 8192\n"
      "buffer_load_dwordx4 v30, s[24:27], s77 offen lds\n"
      "v_mfma_f32_16x16x32_bf16 v[176:179], v[168:171], v[60:63], v[176:179]\n"
      "ds_read_b64_tr_b16 v[76:77], v193 offset:28800\n"
      "ds_read_b64_tr_b16 v[78:79], v193 offset:29568\n"
      "s_waitcnt lgkmcnt(7)\n"
      "v_mfma_f32_32x32x16_bf16 v[112:127], v[244:247], v[64:67], v[112:127]\n"
      "v_mfma_f32_16x16x32_bf16 v[180:183], v[168:171], v[64:67], v[180:183]\n"
      "ds_read_b128 v[244:247], v202 offset:28672\n"
      "ds_read_b64_tr_b16 v[56:57], v194 offset:28672\n"
      "ds_read_b64_tr_b16 v[58:59], v194 offset:29440\n"
      "s_waitcnt lgkmcnt(7)\n"
      "v_mfma_f32_32x32x16_bf16 v[80:95], v[248:251], v[68:71], v[80:95]\n"
      "s_add_u32 m0, s82, 88064\n"
      "s_add_u32 s77, s88, 16384\n"
      "buffer_load_dwordx4 v29, s[24:27], s77 offen lds\n"
      "ds_read_b64_tr_b16 v[60:61], v194 offset:28736\n"
      "ds_read_b64_tr_b16 v[62:63], v194 offset:29504\n"
      "s_waitcnt lgkmcnt(7)\n"
      "v_mfma_f32_32x32x16_bf16 v[96:111], v[248:251], v[72:75], v[96:111]\n"
      "s_add_u32 m0, s82, 89088\n"
      "s_add_u32 s77, s88, 24576\n"
      "buffer_load_dwordx4 v30, s[24:27], s77 offen lds\n"
      "ds_read_b64_tr_b16 v[64:65], v194 offset:28800\n"
      "ds_read_b64_tr_b16 v[66:67], v194 offset:29568\n"
      "s_waitcnt lgkmcnt(7)\n"
      "v_mfma_f32_32x32x16_bf16 v[112:127], v[248:251], v[76:79], v[112:127]\n"
      "ds_read_b128 v[248:251], v203 offset:28672\n"
      "ds_read_b64_tr_b16 v[68:69], v195 offset:28672\n"
      "ds_read_b64_tr_b16 v[70:71], v195 offset:29440\n"
      "s_waitcnt lgkmcnt(7)\n"
      "v_mfma_f32_32x32x16_bf16 v[80:95], v[244:247], v[56:59], v[80:95]\n"
      "s_add_u32 m0, s84, 86016\n"
      "s_mul_i32 s78, s89, 196608\n"
      "s_add_u32 s78, s78, s83\n"
      "buffer_load_dwordx4 v26, s[28:31], s78 offen lds\n"
      "ds_read_b64_tr_b16 v[72:73], v195 offset:28736\n"
      "ds_read_b64_tr_b16 v[74:75], v195 offset:29504\n"
      "s_waitcnt lgkmcnt(7)\n"
      "v_mfma_f32_32x32x16_bf16 v[96:111], v[244:247], v[60:63], v[96:111]\n"
      "buffer_load_dwordx4 v27, s[28:31], s78 offen offset:1024 lds\n"
      "ds_read_b64_tr_b16 v[76:77], v195 offset:28800\n"
      "ds_read_b64_tr_b16 v[78:79], v195 offset:29568\n"
      "s_waitcnt lgkmcnt(7)\n"
      "v_mfma_f32_32x32x16_bf16 v[112:127], v[244:247], v[64:67], v[112:127]\n"
      "s_waitcnt lgkmcnt(4)\n"
      "v_mfma_f32_32x32x16_bf16 v[80:95], v[248:251], v[68:71], v[80:95]\n"
      "buffer_load_dwordx4 v28, s[28:31], s78 offen offset:2048 lds\n"
      "s_waitcnt vmcnt(8) lgkmcnt(0)\n"
      "s_barrier\n"
      "ds_read_b128 v[244:247], v204\n"
      "ds_read_b64_tr_b16 v[56:57], v196\n"
      "ds_read_b64_tr_b16 v[58:59], v196 offset:768\n"
      "ds_read_b64_tr_b16 v[60:61], v196 offset:64\n"
      "ds_read_b64_tr_b16 v[62:63], v196 offset:832\n"
      "ds_read_b64_tr_b16 v[64:65], v196 offset:128\n"
      "ds_read_b64_tr_b16 v[66:67], v196 offset:896\n"
      "v_mfma_f32_32x32x16_bf16 v[96:111], v[248:251], v[72:75], v[96:111]\n"
      "v_mfma_f32_32x32x16_bf16 v[112:127], v[248:251], v[76:79], v[112:127]\n"
      "ds_read_b128 v[248:251], v205\n"
      "ds_read_b64_tr_b16 v[68:69], v197\n"
      "ds_read_b64_tr_b16 v[70:71], v197 offset:768\n"
      "s_waitcnt lgkmcnt(7)\n"
      "v_mfma_f32_32x32x16_bf16 v[80:95], v[244:247], v[56:59], v[80:95]\n"
      "s_mov_b64 exec, 65535\n"
      "buffer_load_dword a255, v45, s[32:35], s95 offen\n"
      "s_mov_b64 exec, -1\n"
      "v_mfma_f32_16x16x32_bf16 v[172:175], v[168:171], v[56:59], v[172:175]\n"
      "ds_read_b64_tr_b16 v[72:73], v197 offset:64\n"
      "ds_read_b64_tr_b16 v[74:75], v197 offset:832\n"
      "s_waitcnt lgkmcnt(7)\n"
      "v_mfma_f32_32x32x16_bf16 v[96:111], v[244:247], v[60:63], v[96:111]\n"
      "s_add_u32 m0, s91, 0\n"
      "s_add_u32 s77, s90, 0\n"
      "s_mov_b32 exec_hi, 0xfff\n"
      "buffer_load_dwordx4 v1, s[36:39], s77 offen nt lds\n"
      "s_mov_b32 exec_hi, -1\n"
      "v_mfma_f32_16x16x32_bf16 v[176:179], v[168:171], v[60:63], v[176:179]\n"
      "ds_read_b64_tr_b16 v[76:77], v197 offset:128\n"
      "ds_read_b64_tr_b16 v[78:79], v197 offset:896\n"
      "s_waitcnt lgkmcnt(7)\n"
      "v_mfma_f32_32x32x16_bf16 v[112:127], v[244:247], v[64:67], v[112:127]\n"
      "s_add_u32 m0, s91, 704\n"
      "s_add_u32 s77, s90, 2752\n"
      "s_mov_b32 exec_hi, 0xfff\n"
      "buffer_load_dwordx4 v1, s[36:39], s77 offen nt lds\n"
      "s_mov_b32 exec_hi, -1\n"
      "v_mfma_f32_16x16x32_bf16 v[180:183], v[168:171], v[64:67], v[180:183]\n"
      "ds_read_b128 v[244:247], v206\n"
      "ds_read_b64_tr_b16 v[56:57], v198\n"
      "ds_read_b64_tr_b16 v[58:59], v198 offset:768\n"
      "s_waitcnt lgkmcnt(7)\n"
      "v_mfma_f32_32x32x16_bf16 v[80:95], v[248:251], v[68:71], v[80:95]\n"
      "s_add_u32 m0, s91, 1408\n"
      "s_add_u32 s77, s90, 5504\n"
      "s_mov_b32 exec_hi, 0xfff\n"
      "buffer_load_dwordx4 v1, s[36:39], s77 offen nt lds\n"
      "s_mov_b32 exec_hi, -1\n"
      "ds_read_b64_tr_b16 v[60:61], v198 offset:64\n"
      "ds_read_b64_tr_b16 v[62:63], v198 offset:832\n"
      "s_waitcnt lgkmcnt(7)\n"
      "v_mfma_f32_32x32x16_bf16 v[96:111], v[248:251], v[72:75], v[96:111]\n"
      "s_add_u32 m0, s91, 2112\n"
      "s_add_u32 s77, s90, 8256\n"
      "s_mov_b32 exec_hi, 0xfff\n"
      "buffer_load_dwordx4 v1, s[36:39], s77 offen nt lds\n"
      "s_mov_b32 exec_hi, -1\n"
      "ds_read_b64_tr_b16 v[64:65], v198 offset:128\n"
      "ds_read_b64_tr_b16 v[66:67], v198 offset:896\n"
      "s_waitcnt lgkmcnt(7)\n"
      "v_mfma_f32_32x32x16_bf16 v[112:127], v[248:251], v[76:79], v[112:127]\n"
      "s_add_u32 m0, s91, 2816\n"
      "s_add_u32 s77, s90, 11008\n"
      "s_mov_b32 exec_hi, 0xfff\n"
      "buffer_load_dwordx4 v1, s[36:39], s77 offen nt lds\n"
      "s_mov_b32 exec_hi, -1\n"
      "ds_read_b128 v[248:251], v207\n"
      "ds_read_b64_tr_b16 v[68:69], v199\n"
      "ds_read_b64_tr_b16 v[70:71], v199 offset:768\n"
      "s_waitcnt lgkmcnt(7)\n"
      "v_mfma_f32_32x32x16_bf16 v[80:95], v[244:247], v[56:59], v[80:95]\n"
      "s_add_u32 m0, s91, 3520\n"
      "s_add_u32 s77, s90, 13760\n"
      "s_mov_b32 exec_hi, 0xfff\n"
      "buffer_load_dwordx4 v1, s[36:39], s77 offen nt lds\n"
      "s_mov_b32 exec_hi, -1\n"
      "s_waitcnt vmcnt(14)\n"
      "v_xor_b32 v20, s60, v165\n"
      "v_xor_b32 v21, s61, v167\n"
      "v_or_b32 v20, v20, v21\n"
      "v_cmp_ne_u32_e32 vcc, 0, v20\n"
      "s_cbranch_vccz .Lgok_10_%=\n"
      "s_mov_b32 s96, 131072\n"
      ".Lgspin_9_%=:\n"
      "buffer_load_dwordx4 v[164:167], v48, s[44:47], 0 offen sc1\n"
      "s_waitcnt vmcnt(0)\n"
      "v_xor_b32 v20, s60, v165\n"
      "v_xor_b32 v21, s61, v167\n"
      "v_or_b32 v20, v20, v21\n"
      "v_cmp_ne_u32_e32 vcc, 0, v20\n"
      "s_cbranch_vccz .Lgok_10_%=\n"
      "s_sub_u32 s96, s96, 1\n"
      "s_cbranch_scc0 .Lgspin_9_%=\n"
      ".Lgok_10_%=:\n"
      "v_mul_f32 v21, 989855744, v164\n"
      "v_mul_f32 v20, 989855744, v166\n"
      "ds_read_b64_tr_b16 v[72:73], v199 offset:64\n"
      "ds_read_b64_tr_b16 v[74:75], v199 offset:832\n"
      "s_waitcnt lgkmcnt(7)\n"
      "v_mfma_f32_32x32x16_bf16 v[96:111], v[244:247], v[60:63], v[96:111]\n"
      "s_add_u32 m0, s91, 4224\n"
      "s_add_u32 s77, s90, 16512\n"
      "s_mov_b32 exec_hi, 0xfff\n"
      "buffer_load_dwordx4 v1, s[36:39], s77 offen nt lds\n"
      "s_mov_b32 exec_hi, -1\n"
      "v_fma_f32 v20, -v21, v21, v20\n"
      "v_mul_f32 v43, -0.5, v21\n"
      "v_max_f32 v20, 0, v20\n"
      "ds_read_b64_tr_b16 v[76:77], v199 offset:128\n"
      "ds_read_b64_tr_b16 v[78:79], v199 offset:896\n"
      "s_waitcnt lgkmcnt(7)\n"
      "v_mfma_f32_32x32x16_bf16 v[112:127], v[244:247], v[64:67], v[112:127]\n"
      "s_add_u32 m0, s91, 4928\n"
      "s_add_u32 s77, s90, 19264\n"
      "s_mov_b32 exec_hi, 0xfff\n"
      "buffer_load_dwordx4 v1, s[36:39], s77 offen nt lds\n"
      "s_mov_b32 exec_hi, -1\n"
      "v_add_f32 v20, 925353388, v20\n"
      "v_rsq_f32 v44, v20\n"
      "s_waitcnt lgkmcnt(4)\n"
      "v_mfma_f32_32x32x16_bf16 v[80:95], v[248:251], v[68:71], v[80:95]\n"
      "ds_write_b32 v41, v44\n"
      "s_waitcnt vmcnt(9) lgkmcnt(1)\n"
      "s_barrier\n"
      "ds_read_b128 v[244:247], v204 offset:28672\n"
      "ds_read_b64_tr_b16 v[56:57], v196 offset:28672\n"
      "ds_read_b64_tr_b16 v[58:59], v196 offset:29440\n"
      "ds_read_b64_tr_b16 v[60:61], v196 offset:28736\n"
      "ds_read_b64_tr_b16 v[62:63], v196 offset:29504\n"
      "ds_read_b64_tr_b16 v[64:65], v196 offset:28800\n"
      "ds_read_b64_tr_b16 v[66:67], v196 offset:29568\n"
      "v_mfma_f32_32x32x16_bf16 v[96:111], v[248:251], v[72:75], v[96:111]\n"
      "v_mfma_f32_32x32x16_bf16 v[112:127], v[248:251], v[76:79], v[112:127]\n"
      "ds_read_b128 v[248:251], v205 offset:28672\n"
      "ds_read_b64_tr_b16 v[68:69], v197 offset:28672\n"
      "ds_read_b64_tr_b16 v[70:71], v197 offset:29440\n"
      "s_waitcnt lgkmcnt(7)\n"
      "v_mfma_f32_32x32x16_bf16 v[80:95], v[244:247], v[56:59], v[80:95]\n"
      "v_mfma_f32_16x16x32_bf16 v[172:175], v[168:171], v[56:59], v[172:175]\n"
      "ds_read_b64_tr_b16 v[72:73], v197 offset:28736\n"
      "ds_read_b64_tr_b16 v[74:75], v197 offset:29504\n"
      "s_waitcnt lgkmcnt(7)\n"
      "v_mfma_f32_32x32x16_bf16 v[96:111], v[244:247], v[60:63], v[96:111]\n"
      "v_mfma_f32_16x16x32_bf16 v[176:179], v[168:171], v[60:63], v[176:179]\n"
      "ds_read_b64_tr_b16 v[76:77], v197 offset:28800\n"
      "ds_read_b64_tr_b16 v[78:79], v197 offset:29568\n"
      "s_waitcnt lgkmcnt(7)\n"
      "v_mfma_f32_32x32x16_bf16 v[112:127], v[244:247], v[64:67], v[112:127]\n"
      "v_mfma_f32_16x16x32_bf16 v[180:183], v[168:171], v[64:67], v[180:183]\n"
      "ds_read_b128 v[244:247], v206 offset:28672\n"
      "ds_read_b64_tr_b16 v[56:57], v198 offset:28672\n"
      "ds_read_b64_tr_b16 v[58:59], v198 offset:29440\n"
      "s_waitcnt lgkmcnt(7)\n"
      "v_mfma_f32_32x32x16_bf16 v[80:95], v[248:251], v[68:71], v[80:95]\n"
      "s_mov_b64 exec, 65535\n"
      "ds_write_b32 v39, v172\n"
      "ds_write_b32 v39, v173 offset:256\n"
      "ds_write_b32 v39, v176 offset:512\n"
      "ds_write_b32 v39, v177 offset:768\n"
      "ds_write_b32 v39, v180 offset:1024\n"
      "ds_write_b32 v39, v181 offset:1280\n"
      "s_mov_b64 exec, -1\n"
      "ds_read_b64_tr_b16 v[60:61], v198 offset:28736\n"
      "ds_read_b64_tr_b16 v[62:63], v198 offset:29504\n"
      "s_waitcnt lgkmcnt(13)\n"
      "v_mfma_f32_32x32x16_bf16 v[96:111], v[248:251], v[72:75], v[96:111]\n"
      "ds_read_b128 v[220:223], v42\n"
      "ds_read_b128 v[224:227], v42 offset:32\n"
      "ds_read_b128 v[228:231], v42 offset:64\n"
      "ds_read_b128 v[232:235], v42 offset:96\n"
      "ds_read_b64_tr_b16 v[64:65], v198 offset:28800\n"
      "ds_read_b64_tr_b16 v[66:67], v198 offset:29568\n"
      "s_waitcnt lgkmcnt(15)\n"
      "v_mfma_f32_32x32x16_bf16 v[112:127], v[248:251], v[76:79], v[112:127]\n"
      "s_waitcnt lgkmcnt(0)\n"
      "s_barrier\n"
      "ds_read_b128 v[208:211], v40\n"
      "ds_read_b128 v[212:215], v40 offset:512\n"
      "ds_read_b128 v[216:219], v40 offset:1024\n"
      "ds_read_b128 v[248:251], v207 offset:28672\n"
      "ds_read_b64_tr_b16 v[68:69], v199 offset:28672\n"
      "ds_read_b64_tr_b16 v[70:71], v199 offset:29440\n"
      "v_mfma_f32_32x32x16_bf16 v[80:95], v[244:247], v[56:59], v[80:95]\n"
      "ds_read_b64_tr_b16 v[72:73], v199 offset:28736\n"
      "ds_read_b64_tr_b16 v[74:75], v199 offset:29504\n"
      "v_mfma_f32_32x32x16_bf16 v[96:111], v[244:247], v[60:63], v[96:111]\n"
      "ds_read_b64_tr_b16 v[76:77], v199 offset:28800\n"
      "ds_read_b64_tr_b16 v[78:79], v199 offset:29568\n"
      "v_mfma_f32_32x32x16_bf16 v[112:127], v[244:247], v[64:67], v[112:127]\n"
      "s_waitcnt lgkmcnt(9)\n"
      "v_add_f32 v208, v208, v209\n"
      "v_add_f32 v210, v210, v211\n"
      "v_add_f32 v35, v208, v210\n"
      "s_waitcnt lgkmcnt(8)\n"
      "v_add_f32 v212, v212, v213\n"
      "v_add_f32 v214, v214, v215\n"
      "v_add_f32 v36, v212, v214\n"
      "s_waitcnt lgkmcnt(7)\n"
      "v_add_f32 v216, v216, v217\n"
      "v_add_f32 v218, v218, v219\n"
      "v_add_f32 v37, v216, v218\n"
      "v_mfma_f32_32x32x2_f32 v[80:95], v43, v35, v[80:95]\n"
      "v_mfma_f32_32x32x2_f32 v[96:111], v43, v36, v[96:111]\n"
      "v_mfma_f32_32x32x2_f32 v[112:127], v43, v37, v[112:127]\n"
      "s_waitcnt lgkmcnt(4)\n"
      "v_mfma_f32_32x32x16_bf16 v[80:95], v[248:251], v[68:71], v[80:95]\n"
      "s_waitcnt lgkmcnt(2)\n"
      "v_mfma_f32_32x32x16_bf16 v[96:111], v[248:251], v[72:75], v[96:111]\n"
      "s_waitcnt lgkmcnt(0)\n"
      "v_mfma_f32_32x32x16_bf16 v[112:127], v[248:251], v[76:79], v[112:127]\n"
      "s_nop 9\n"
      "v_pk_mul_f32 v[80:81], v[80:81], v[220:221]\n"
      "v_pk_mul_f32 v[82:83], v[82:83], v[222:223]\n"
      "v_pk_mul_f32 v[84:85], v[84:85], v[224:225]\n"
      "v_pk_mul_f32 v[86:87], v[86:87], v[226:227]\n"
      "v_pk_mul_f32 v[88:89], v[88:89], v[228:229]\n"
      "v_pk_mul_f32 v[90:91], v[90:91], v[230:231]\n"
      "v_pk_mul_f32 v[92:93], v[92:93], v[232:233]\n"
      "v_pk_mul_f32 v[94:95], v[94:95], v[234:235]\n"
      "v_cvt_pk_bf16_f32 v56, v80, v81\n"
      "v_cvt_pk_bf16_f32 v57, v82, v83\n"
      "v_cvt_pk_bf16_f32 v58, v84, v85\n"
      "v_cvt_pk_bf16_f32 v59, v86, v87\n"
      "buffer_store_dwordx4 v[56:59], v38, s[44:47], s4 offen sc1\n"
      "v_cvt_pk_bf16_f32 v60, v88, v89\n"
      "v_cvt_pk_bf16_f32 v61, v90, v91\n"
      "v_cvt_pk_bf16_f32 v62, v92, v93\n"
      "v_cvt_pk_bf16_f32 v63, v94, v95\n"
      "buffer_store_dwordx4 v[60:63], v38, s[44:47], s4 offen offset:512 sc1\n"
      "v_pk_mul_f32 v[96:97], v[96:97], v[220:221]\n"
      "v_pk_mul_f32 v[98:99], v[98:99], v[222:223]\n"
      "v_pk_mul_f32 v[100:101], v[100:101], v[224:225]\n"
      "v_pk_mul_f32 v[102:103], v[102:103], v[226:227]\n"
      "v_pk_mul_f32 v[104:105], v[104:105], v[228:229]\n"
      "v_pk_mul_f32 v[106:107], v[106:107], v[230:231]\n"
      "v_pk_mul_f32 v[108:109], v[108:109], v[232:233]\n"
      "v_pk_mul_f32 v[110:111], v[110:111], v[234:235]\n"
      "v_cvt_pk_bf16_f32 v64, v96, v97\n"
      "v_cvt_pk_bf16_f32 v65, v98, v99\n"
      "v_cvt_pk_bf16_f32 v66, v100, v101\n"
      "v_cvt_pk_bf16_f32 v67, v102, v103\n"
      "buffer_store_dwordx4 v[64:67], v38, s[44:47], s6 offen sc1\n"
      "v_cvt_pk_bf16_f32 v68, v104, v105\n"
      "v_cvt_pk_bf16_f32 v69, v106, v107\n"
      "v_cvt_pk_bf16_f32 v70, v108, v109\n"
      "v_cvt_pk_bf16_f32 v71, v110, v111\n"
      "buffer_store_dwordx4 v[68:71], v38, s[44:47], s6 offen offset:512 sc1\n"
      "v_pk_mul_f32 v[112:113], v[112:113], v[220:221]\n"
      "v_pk_mul_f32 v[114:115], v[114:115], v[222:223]\n"
      "v_pk_mul_f32 v[116:117], v[116:117], v[224:225]\n"
      "v_pk_mul_f32 v[118:119], v[118:119], v[226:227]\n"
      "v_pk_mul_f32 v[120:121], v[120:121], v[228:229]\n"
      "v_pk_mul_f32 v[122:123], v[122:123], v[230:231]\n"
      "v_pk_mul_f32 v[124:125], v[124:125], v[232:233]\n"
      "v_pk_mul_f32 v[126:127], v[126:127], v[234:235]\n"
      "v_cvt_pk_bf16_f32 v72, v112, v113\n"
      "v_cvt_pk_bf16_f32 v73, v114, v115\n"
      "v_cvt_pk_bf16_f32 v74, v116, v117\n"
      "v_cvt_pk_bf16_f32 v75, v118, v119\n"
      "buffer_store_dwordx4 v[72:75], v38, s[44:47], s8 offen sc1\n"
      "v_cvt_pk_bf16_f32 v76, v120, v121\n"
      "v_cvt_pk_bf16_f32 v77, v122, v123\n"
      "v_cvt_pk_bf16_f32 v78, v124, v125\n"
      "v_cvt_pk_bf16_f32 v79, v126, v127\n"
      "buffer_store_dwordx4 v[76:79], v38, s[44:47], s8 offen offset:512 sc1\n"
      "s_nop 1\n"
      ".Lp1end_4_%=:\n"
      "v_lshrrev_b32 v20, 3, v6\n"
      "v_bfe_u32 v21, v6, 4, 2\n"
      "v_bfe_u32 v22, v6, 1, 2\n"
      "v_xor_b32 v21, v21, v22\n"
      "v_lshlrev_b32 v21, 5, v21\n"
      "v_and_b32 v22, 1, v6\n"
      "v_lshl_add_u32 v21, v22, 4, v21\n"
      "v_lshl_add_u32 v16, v20, 10, v21\n"
      "v_lshl_add_u32 v0, v20, 12, v21\n"
      "s_lshl_b32 s83, s62, 17\n"
      "s_lshl_b32 s76, s70, 7\n"
      "s_add_u32 s83, s83, s76\n"
      "s_lshl_b32 s89, s69, 1\n"
      "s_barrier\n"
      "s_add_u32 s77, s89, 0\n"
      "s_and_b32 s77, s77, 15\n"
      "s_lshl_b32 s77, s77, 10\n"
      "s_add_u32 m0, s92, s77\n"
      "s_lshl_b32 s77, s77, 3\n"
      "s_add_u32 s77, s77, s83\n"
      "buffer_load_dwordx4 v16, s[32:35], s77 offen lds\n"
      "s_sleep 1\n"
      "s_add_u32 s77, s89, 1\n"
      "s_and_b32 s77, s77, 15\n"
      "s_lshl_b32 s77, s77, 10\n"
      "s_add_u32 m0, s92, s77\n"
      "s_lshl_b32 s77, s77, 3\n"
      "s_add_u32 s77, s77, s83\n"
      "buffer_load_dwordx4 v16, s[32:35], s77 offen lds\n"
      "s_sleep 1\n"
      "s_add_u32 s77, s89, 2\n"
      "s_and_b32 s77, s77, 15\n"
      "s_lshl_b32 s77, s77, 10\n"
      "s_add_u32 m0, s92, s77\n"
      "s_lshl_b32 s77, s77, 3\n"
      "s_add_u32 s77, s77, s83\n"
      "buffer_load_dwordx4 v16, s[32:35], s77 offen lds\n"
      "s_sleep 1\n"
      "s_add_u32 s77, s89, 3\n"
      "s_and_b32 s77, s77, 15\n"
      "s_lshl_b32 s77, s77, 10\n"
      "s_add_u32 m0, s92, s77\n"
      "s_lshl_b32 s77, s77, 3\n"
      "s_add_u32 s77, s77, s83\n"
      "buffer_load_dwordx4 v16, s[32:35], s77 offen lds\n"
      "s_sleep 1\n"
      "v_and_b32 v22, 511, v18\n"
      "v_and_b32 v20, 11, v6\n"
      "v_cmp_eq_u32_e32 vcc, 0, v20\n"
      "v_mov_b32 v20, 1040\n"
      "v_mov_b32 v21, 1024\n"
      "v_cndmask_b32_e32 v20, v20, v21, vcc\n"
      "v_and_b32 v21, 16, v6\n"
      "v_cmp_eq_u32_e32 vcc, 0, v21\n"
      "v_add_u32 v21, 512, v22\n"
      "s_mul_i32 s73, s62, 2112\n"
      "s_add_u32 s73, s73, 81920\n"
      "v_cndmask_b32_e32 v22, v20, v22, vcc\n"
      "v_cndmask_b32_e32 v21, v20, v21, vcc\n"
      "v_add_u32 v50, s73, v22\n"
      "v_add_u32 v51, s73, v21\n"
      "s_lshl_b32 s73, s62, 11\n"
      "s_add_u32 s73, s73, 65536\n"
      "v_add_u32 v49, s73, v1\n"
      "s_mul_i32 s84, s62, 2112\n"
      "s_add_u32 s84, s84, 81920\n"
      "v_bfe_u32 v21, v6, 1, 1\n"
      "v_bfe_u32 v22, v6, 2, 1\n"
      "v_lshl_add_u32 v21, v22, 3, v21\n"
      "v_mul_u32_u24 v21, 1056, v21\n"
      "v_and_b32 v20, 1, v6\n"
      "v_lshl_add_u32 v21, v20, 4, v21\n"
      "v_add_u32 v53, s84, v21\n"
      "v_or_b32 v22, s22, v22\n"
      "v_cmp_gt_u32_e32 vcc, v22, v20\n"
      "v_mov_b32 v21, 1065369472\n"
      "s_xor_b32 s73, s22, 1\n"
      "s_lshl_b32 s73, s73, 30\n"
      "v_cndmask_b32_e32 v160, v2, v21, vcc\n"
      "v_or_b32 v52, s73, v1\n"
      "v_mov_b32 v161, v160\n"
      "v_mov_b32 v162, v160\n"
      "v_mov_b32 v163, v160\n"
      "s_mov_b64 exec, 255\n"
      "ds_write_b128 v53, v[160:163] offset:1024\n"
      "s_mov_b64 exec, -1\n"
      "s_lshr_b32 s73, s67, 1\n"
      "s_add_u32 s74, s73, 0\n"
      "s_mul_i32 s74, s74, 43691\n"
      "s_lshr_b32 s74, s74, 17\n"
      "v_mov_b32 v21, s74\n"
      "s_add_u32 s74, s73, 16\n"
      "s_mul_i32 s74, s74, 43691\n"
      "s_lshr_b32 s74, s74, 17\n"
      "v_mov_b32 v22, s74\n"
      "s_add_u32 s74, s73, 32\n"
      "s_mul_i32 s74, s74, 43691\n"
      "s_lshr_b32 s74, s74, 17\n"
      "v_mov_b32 v23, s74\n"
      "v_and_b32 v20, 4, v6\n"
      "v_cmp_eq_u32_e32 vcc, 0, v20\n"
      "v_mov_b32 v24, s23\n"
      "v_mov_b32 v25, s97\n"
      "v_cndmask_b32_e32 v22, v23, v22, vcc\n"
      "v_cmp_gt_u32_e32 vcc, 8, v6\n"
      "v_and_b32 v20, 10, v6\n"
      "s_nop 0\n"
      "v_cndmask_b32_e32 v21, v21, v22, vcc\n"
      "v_cmp_eq_u32_e32 vcc, 0, v20\n"
      "v_and_b32 v20, 1, v6\n"
      "s_nop 0\n"
      "v_cndmask_b32_e32 v24, v25, v24, vcc\n"
      "v_add_u32 v24, v24, v20\n"
      "v_lshrrev_b32 v20, 2, v24\n"
      "v_lshl_add_u32 v20, v20, 4, v21\n"
      "v_and_b32 v24, 3, v24\n"
      "v_lshl_add_u32 v20, v20, 2, v24\n"
      "v_lshlrev_b32 v54, 7, v20\n"
      "s_mov_b32 s77, 704\n"
      "v_lshrrev_b32 v20, 5, v6\n"
      "v_bfe_u32 v21, v6, 4, 1\n"
      "v_bfe_u32 v22, v6, 2, 2\n"
      "v_lshl_add_u32 v23, v21, 2, v22\n"
      "v_lshl_add_u32 v20, v20, 4, v23\n"
      "v_and_b32 v21, 3, v6\n"
      "v_lshlrev_b32 v21, 3, v21\n"
      "v_lshrrev_b32 v24, 4, v6\n"
      "v_lshl_add_u32 v25, v24, 5, v21\n"
      "v_mad_u32_u24 v47, v22, s77, v25\n"
      "v_add_u32 v47, s91, v47\n"
      "v_and_b32 v25, 15, v6\n"
      "v_lshlrev_b32 v25, 4, v25\n"
      "v_lshl_add_u32 v45, v24, 16, v25\n"
      "s_waitcnt vmcnt(10)\n"
      "ds_read_b64_tr_b16 v[80:81], v47\n"
      "ds_read_b64_tr_b16 v[82:83], v47 offset:2816\n"
      "ds_read_b64_tr_b16 v[84:85], v47 offset:128\n"
      "ds_read_b64_tr_b16 v[86:87], v47 offset:2944\n"
      "ds_read_b64_tr_b16 v[88:89], v47 offset:256\n"
      "ds_read_b64_tr_b16 v[90:91], v47 offset:3072\n"
      "ds_read_b64_tr_b16 v[92:93], v47 offset:384\n"
      "ds_read_b64_tr_b16 v[94:95], v47 offset:3200\n"
      "ds_read_b64_tr_b16 v[96:97], v47 offset:512\n"
      "ds_read_b64_tr_b16 v[98:99], v47 offset:3328\n"
      "ds_read_b64_tr_b16 v[100:101], v47 offset:640\n"
      "ds_read_b64_tr_b16 v[102:103], v47 offset:3456\n"
      "v_lshl_add_u32 v20, v20, 7, v21\n"
      "v_lshrrev_b32 v23, 1, v23\n"
      "v_xor_b32 v21, 0, v23\n"
      "v_lshl_add_u32 v21, v21, 5, v20\n"
      "v_add_u32 v8, s92, v21\n"
      "v_add_u32 v12, s91, v21\n"
      "v_xor_b32 v21, 1, v23\n"
      "v_lshl_add_u32 v21, v21, 5, v20\n"
      "v_add_u32 v9, s92, v21\n"
      "v_add_u32 v13, s91, v21\n"
      "v_xor_b32 v21, 2, v23\n"
      "v_lshl_add_u32 v21, v21, 5, v20\n"
      "v_add_u32 v10, s92, v21\n"
      "v_add_u32 v14, s91, v21\n"
      "v_xor_b32 v21, 3, v23\n"
      "v_lshl_add_u32 v21, v21, 5, v20\n"
      "v_add_u32 v11, s92, v21\n"
      "v_add_u32 v15, s91, v21\n"
      "s_cmp_eq_u32 s62, 3\n"
      "s_cselect_b32 s73, 1073741824, 0\n"
      "v_or_b32 v17, s73, v1\n"
      "v_or_b32 v46, s73, v45\n"
      "s_waitcnt vmcnt(4)\n"
      "s_mov_b64 exec, 1\n"
      "buffer_store_dwordx2 v[4:5], v2, s[44:47], s13 offen sc1\n"
      "s_mov_b64 exec, -1\n"
      "s_add_u32 s77, s89, 4\n"
      "s_and_b32 s77, s77, 15\n"
      "s_lshl_b32 s77, s77, 10\n"
      "s_add_u32 m0, s92, s77\n"
      "s_lshl_b32 s77, s77, 3\n"
      "s_add_u32 s77, s77, s83\n"
      "buffer_load_dwordx4 v16, s[32:35], s77 offen lds\n"
      "s_sleep 1\n"
      "s_add_u32 s77, s89, 5\n"
      "s_and_b32 s77, s77, 15\n"
      "s_lshl_b32 s77, s77, 10\n"
      "s_add_u32 m0, s92, s77\n"
      "s_lshl_b32 s77, s77, 3\n"
      "s_add_u32 s77, s77, s83\n"
      "buffer_load_dwordx4 v16, s[32:35], s77 offen lds\n"
      "s_sleep 1\n"
      "s_add_u32 s77, s89, 6\n"
      "s_and_b32 s77, s77, 15\n"
      "s_lshl_b32 s77, s77, 10\n"
      "s_add_u32 m0, s92, s77\n"
      "s_lshl_b32 s77, s77, 3\n"
      "s_add_u32 s77, s77, s83\n"
      "buffer_load_dwordx4 v16, s[32:35], s77 offen lds\n"
      "s_sleep 1\n"
      "s_add_u32 s77, s89, 7\n"
      "s_and_b32 s77, s77, 15\n"
      "s_lshl_b32 s77, s77, 10\n"
      "s_add_u32 m0, s92, s77\n"
      "s_lshl_b32 s77, s77, 3\n"
      "s_add_u32 s77, s77, s83\n"
      "buffer_load_dwordx4 v16, s[32:35], s77 offen lds\n"
      "s_sleep 1\n"
      "s_add_u32 s77, s89, 8\n"
      "s_and_b32 s77, s77, 15\n"
      "s_lshl_b32 s77, s77, 10\n"
      "s_add_u32 m0, s92, s77\n"
      "s_lshl_b32 s77, s77, 3\n"
      "s_add_u32 s77, s77, s83\n"
      "buffer_load_dwordx4 v16, s[32:35], s77 offen lds\n"
      "s_sleep 1\n"
      "s_add_u32 s77, s89, 9\n"
      "s_and_b32 s77, s77, 15\n"
      "s_lshl_b32 s77, s77, 10\n"
      "s_add_u32 m0, s92, s77\n"
      "s_lshl_b32 s77, s77, 3\n"
      "s_add_u32 s77, s77, s83\n"
      "buffer_load_dwordx4 v16, s[32:35], s77 offen lds\n"
      "s_sleep 1\n"
      "s_add_u32 s77, s89, 10\n"
      "s_and_b32 s77, s77, 15\n"
      "s_lshl_b32 s77, s77, 10\n"
      "s_add_u32 m0, s92, s77\n"
      "s_lshl_b32 s77, s77, 3\n"
      "s_add_u32 s77, s77, s83\n"
      "buffer_load_dwordx4 v16, s[32:35], s77 offen lds\n"
      "s_sleep 1\n"
      "s_add_u32 s77, s89, 11\n"
      "s_and_b32 s77, s77, 15\n"
      "s_lshl_b32 s77, s77, 10\n"
      "s_add_u32 m0, s92, s77\n"
      "s_lshl_b32 s77, s77, 3\n"
      "s_add_u32 s77, s77, s83\n"
      "buffer_load_dwordx4 v16, s[32:35], s77 offen lds\n"
      "s_sleep 1\n"
      "s_add_u32 s77, s89, 12\n"
      "s_and_b32 s77, s77, 15\n"
      "s_lshl_b32 s77, s77, 10\n"
      "s_add_u32 m0, s92, s77\n"
      "s_lshl_b32 s77, s77, 3\n"
      "s_add_u32 s77, s77, s83\n"
      "buffer_load_dwordx4 v16, s[32:35], s77 offen lds\n"
      "s_sleep 1\n"
      "s_add_u32 s77, s89, 13\n"
      "s_and_b32 s77, s77, 15\n"
      "s_lshl_b32 s77, s77, 10\n"
      "s_add_u32 m0, s92, s77\n"
      "s_lshl_b32 s77, s77, 3\n"
      "s_add_u32 s77, s77, s83\n"
      "buffer_load_dwordx4 v16, s[32:35], s77 offen lds\n"
      "s_sleep 1\n"
      "s_add_u32 s77, s89, 14\n"
      "s_and_b32 s77, s77, 15\n"
      "s_lshl_b32 s77, s77, 10\n"
      "s_add_u32 m0, s92, s77\n"
      "s_lshl_b32 s77, s77, 3\n"
      "s_add_u32 s77, s77, s83\n"
      "buffer_load_dwordx4 v16, s[32:35], s77 offen lds\n"
      "s_sleep 1\n"
      "s_add_u32 s77, s89, 15\n"
      "s_and_b32 s77, s77, 15\n"
      "s_lshl_b32 s77, s77, 10\n"
      "s_add_u32 m0, s92, s77\n"
      "s_lshl_b32 s77, s77, 3\n"
      "s_add_u32 s77, s77, s83\n"
      "buffer_load_dwordx4 v16, s[32:35], s77 offen lds\n"
      "s_sleep 1\n"
      "v_and_b32 v20, 31, v6\n"
      "v_lshrrev_b32 v21, 5, v6\n"
      "v_lshlrev_b32 v21, 2, v21\n"
      "v_sub_u32 v20, v20, v21\n"
      "v_mov_b32 v22, 4286578688\n"
      "v_cmp_gt_i32_e32 vcc, 0, v20\n"
      "v_cmp_gt_i32_e64 s[98:99], 1, v20\n"
      "s_nop 0\n"
      "v_cndmask_b32_e32 v128, v22, v2, vcc\n"
      "v_cndmask_b32_e32 v144, v2, v22, vcc\n"
      "v_cmp_gt_i32_e32 vcc, 2, v20\n"
      "v_cndmask_b32_e64 v129, v22, v2, s[98:99]\n"
      "v_cndmask_b32_e64 v145, v2, v22, s[98:99]\n"
      "v_cmp_gt_i32_e64 s[98:99], 3, v20\n"
      "v_cndmask_b32_e32 v130, v22, v2, vcc\n"
      "v_cndmask_b32_e32 v146, v2, v22, vcc\n"
      "v_cmp_gt_i32_e32 vcc, 8, v20\n"
      "v_cndmask_b32_e64 v131, v22, v2, s[98:99]\n"
      "v_cndmask_b32_e64 v147, v2, v22, s[98:99]\n"
      "v_cmp_gt_i32_e64 s[98:99], 9, v20\n"
      "v_cndmask_b32_e32 v132, v22, v2, vcc\n"
      "v_cndmask_b32_e32 v148, v2, v22, vcc\n"
      "v_cmp_gt_i32_e32 vcc, 10, v20\n"
      "v_cndmask_b32_e64 v133, v22, v2, s[98:99]\n"
      "v_cndmask_b32_e64 v149, v2, v22, s[98:99]\n"
      "v_cmp_gt_i32_e64 s[98:99], 11, v20\n"
      "v_cndmask_b32_e32 v134, v22, v2, vcc\n"
      "v_cndmask_b32_e32 v150, v2, v22, vcc\n"
      "v_cmp_gt_i32_e32 vcc, 16, v20\n"
      "v_cndmask_b32_e64 v135, v22, v2, s[98:99]\n"
      "v_cndmask_b32_e64 v151, v2, v22, s[98:99]\n"
      "v_cmp_gt_i32_e64 s[98:99], 17, v20\n"
      "v_cndmask_b32_e32 v136, v22, v2, vcc\n"
      "v_cndmask_b32_e32 v152, v2, v22, vcc\n"
      "v_cmp_gt_i32_e32 vcc, 18, v20\n"
      "v_cndmask_b32_e64 v137, v22, v2, s[98:99]\n"
      "v_cndmask_b32_e64 v153, v2, v22, s[98:99]\n"
      "v_cmp_gt_i32_e64 s[98:99], 19, v20\n"
      "v_cndmask_b32_e32 v138, v22, v2, vcc\n"
      "v_cndmask_b32_e32 v154, v2, v22, vcc\n"
      "v_cmp_gt_i32_e32 vcc, 24, v20\n"
      "v_cndmask_b32_e64 v139, v22, v2, s[98:99]\n"
      "v_cndmask_b32_e64 v155, v2, v22, s[98:99]\n"
      "v_cmp_gt_i32_e64 s[98:99], 25, v20\n"
      "v_cndmask_b32_e32 v140, v22, v2, vcc\n"
      "v_cndmask_b32_e32 v156, v2, v22, vcc\n"
      "v_cmp_gt_i32_e32 vcc, 26, v20\n"
      "v_cndmask_b32_e64 v141, v22, v2, s[98:99]\n"
      "v_cndmask_b32_e64 v157, v2, v22, s[98:99]\n"
      "v_cmp_gt_i32_e64 s[98:99], 27, v20\n"
      "v_cndmask_b32_e32 v142, v22, v2, vcc\n"
      "v_cndmask_b32_e32 v158, v2, v22, vcc\n"
      "s_add_u32 s15, s15, 524288\n"
      "v_cndmask_b32_e64 v143, v22, v2, s[98:99]\n"
      "v_cndmask_b32_e64 v159, v2, v22, s[98:99]\n"
      "s_sleep 12\n"
      "s_mov_b32 s96, 131072\n"
      "s_mov_b64 exec, 1023\n"
      ".Lpoll_11_%=:\n"
      "buffer_load_dwordx2 v[24:25], v54, s[44:47], 0 offen sc1\n"
      "s_waitcnt vmcnt(0)\n"
      "v_cmp_ne_u64_e32 vcc, s[60:61], v[24:25]\n"
      "s_cbranch_vccz .Lpdone_12_%=\n"
      "s_sub_u32 s96, s96, 1\n"
      "s_cbranch_scc0 .Lpoll_11_%=\n"
      ".Lpdone_12_%=:\n"
      "s_mov_b64 exec, -1\n"
      "s_mov_b32 m0, s19\n"
      "s_nop 0\n"
      "buffer_load_dwordx4 v52, s[44:47], s16 offen sc1 lds\n"
      "buffer_load_dwordx4 v[224:227], v1, s[44:47], s18 offen sc1\n"
      "buffer_load_dwordx4 v52, s[44:47], s16 offen offset:1024 sc1 lds\n"
      "s_mov_b32 m0, s84\n"
      "s_nop 0\n"
      "buffer_load_dwordx4 v52, s[44:47], s17 offen sc1 lds\n"
      "buffer_load_dwordx4 v[228:231], v1, s[44:47], s18 offen offset:1024 sc1\n"
      "s_add_u32 m0, s84, 32\n"
      "s_nop 0\n"
      "buffer_load_dwordx4 v52, s[44:47], s17 offen offset:1024 sc1 lds\n"
      "s_add_u32 s77, s18, 2097152\n"
      "s_add_u32 m0, s19, 8192\n"
      "s_nop 0\n"
      "buffer_load_dwordx4 v1, s[44:47], s77 offen sc1 lds\n"
      "buffer_load_dwordx4 v1, s[44:47], s77 offen offset:1024 sc1 lds\n"
      "s_add_u32 s78, s18, 4194304\n"
      "s_add_u32 m0, s84, 8448\n"
      "s_nop 0\n"
      "buffer_load_dwordx4 v1, s[44:47], s78 offen sc1 lds\n"
      "s_add_u32 m0, s84, 8480\n"
      "s_nop 0\n"
      "buffer_load_dwordx4 v1, s[44:47], s78 offen offset:1024 sc1 lds\n"
      "s_lshl_b32 s81, s97, 15\n"
      "s_lshl_b32 s76, s67, 9\n"
      "s_add_u32 s81, s81, s76\n"
      "s_add_u32 s81, s81, 7340032\n"
      "s_add_u32 s79, s81, 32768\n"
      "s_lshl_b32 s80, s67, 3\n"
      "s_lshl_b32 s76, s68, 10\n"
      "s_add_u32 s80, s80, s76\n"
      "s_lshl_b32 s76, s62, 8\n"
      "s_add_u32 s80, s80, s76\n"
      "s_add_u32 s80, s80, 131072\n"
      "s_lshl_b32 s82, s69, 2\n"
      "s_add_u32 s82, s82, s62\n"
      "s_lshr_b32 s74, s69, 3\n"
      "s_lshl_b32 s73, s62, 13\n"
      "s_lshl_b32 s76, s74, 15\n"
      "s_add_u32 s73, s73, s76\n"
      "s_add_u32 s73, s73, 524288\n"
      "v_lshl_add_u32 v55, v6, 7, s73\n"
      "s_lshl_b32 s12, s71, 16\n"
      "s_lshl_b32 s76, s62, 14\n"
      "s_add_u32 s12, s12, s76\n"
      "s_add_u32 s12, s12, 14680064\n"
      "s_mov_b32 s75, 0\n"
      "s_add_u32 s73, s75, 0\n"
      "s_and_b32 s73, s73, 7\n"
      "s_lshl_b32 s4, s73, 11\n"
      "s_add_u32 s73, s75, 1\n"
      "s_and_b32 s73, s73, 7\n"
      "s_lshl_b32 s5, s73, 11\n"
      "s_add_u32 s73, s75, 2\n"
      "s_and_b32 s73, s73, 7\n"
      "s_lshl_b32 s6, s73, 11\n"
      "s_add_u32 s73, s75, 3\n"
      "s_and_b32 s73, s73, 7\n"
      "s_lshl_b32 s7, s73, 11\n"
      "s_add_u32 s73, s75, 4\n"
      "s_and_b32 s73, s73, 7\n"
      "s_lshl_b32 s8, s73, 11\n"
      "s_add_u32 s73, s75, 5\n"
      "s_and_b32 s73, s73, 7\n"
      "s_lshl_b32 s9, s73, 11\n"
      "s_add_u32 s73, s75, 6\n"
      "s_and_b32 s73, s73, 7\n"
      "s_lshl_b32 s10, s73, 11\n"
      "s_add_u32 s73, s75, 7\n"
      "s_and_b32 s73, s73, 7\n"
      "s_lshl_b32 s11, s73, 11\n"
      "s_cmp_eq_u32 s62, 3\n"
      "s_cselect_b32 s74, 10, 11\n"
      "s_mov_b32 s16, 0\n"
      "s_lshl_b32 s74, s74, 10\n"
      "s_add_u32 s17, s16, 1024\n"
      "s_cmp_eq_u32 s17, s74\n"
      "s_cselect_b32 s17, 0, s17\n"
      "s_add_u32 s18, s17, 1024\n"
      "s_cmp_eq_u32 s18, s74\n"
      "s_cselect_b32 s18, 0, s18\n"
      "s_add_u32 s19, s18, 1024\n"
      "s_cmp_eq_u32 s19, s74\n"
      "s_cselect_b32 s19, 0, s19\n"
      "s_add_u32 s88, s19, 1024\n"
      "s_cmp_eq_u32 s88, s74\n"
      "s_cselect_b32 s88, 0, s88\n"
      "s_add_u32 s87, s88, 1024\n"
      "s_cmp_eq_u32 s87, s74\n"
      "s_cselect_b32 s87, 0, s87\n"
      "s_add_u32 s13, s87, 1024\n"
      "s_cmp_eq_u32 s13, s74\n"
      "s_cselect_b32 s13, 0, s13\n"
      "s_add_u32 s95, s13, 1024\n"
      "s_cmp_eq_u32 s95, s74\n"
      "s_cselect_b32 s95, 0, s95\n"
      "s_add_u32 s90, s95, 1024\n"
      "s_cmp_eq_u32 s90, s74\n"
      "s_cselect_b32 s90, 0, s90\n"
      "s_add_u32 s23, s90, 1024\n"
      "s_cmp_eq_u32 s23, s74\n"
      "s_cselect_b32 s23, 0, s23\n"
      "s_add_u32 s22, s23, 1024\n"
      "s_cmp_eq_u32 s22, s74\n"
      "s_cselect_b32 s22, 0, s22\n"
      "s_cmp_eq_u32 s62, 3\n"
      "s_cselect_b32 s22, 10240, s22\n"
      "v_and_b32 v20, 15, v6\n"
      "v_lshlrev_b32 v253, 1, v20\n"
      "v_lshrrev_b32 v21, 4, v6\n"
      "v_lshlrev_b32 v252, 3, v21\n"
      "v_or_b32 v48, s72, v1\n"
      "v_lshrrev_b32 v20, 5, v6\n"
      "v_lshlrev_b32 v20, 16, v20\n"
      "v_and_b32 v21, 31, v6\n"
      "v_lshl_add_u32 v254, v21, 7, v20\n"
      "s_waitcnt vmcnt(4)\n"
      "s_waitcnt lgkmcnt(0)\n"
      "ds_read_b128 v[232:235], v49\n"
      "ds_read_b128 v[236:239], v49 offset:1024\n"
      "ds_read_b128 v[56:59], v50\n"
      "ds_read_b128 v[60:63], v51\n"
      "buffer_store_dwordx4 v[80:83], v45, s[44:47], s14 offen sc1\n"
      "s_add_u32 s14, s14, 262144\n"
      "buffer_store_dwordx4 v[84:87], v45, s[44:47], s14 offen sc1\n"
      "s_add_u32 s14, s14, 262144\n"
      "s_waitcnt lgkmcnt(3)\n"
      "v_mfma_f32_32x32x16_bf16 v[160:175], v[232:235], v[224:227], v[128:143]\n"
      "s_nop 11\n"
      "v_exp_f32 v160, v160\n"
      "v_exp_f32 v161, v161\n"
      "v_exp_f32 v162, v162\n"
      "v_exp_f32 v163, v163\n"
      "s_waitcnt lgkmcnt(2)\n"
      "v_mfma_f32_32x32x16_bf16 v[176:191], v[236:239], v[224:227], 0\n"
      "v_exp_f32 v164, v164\n"
      "v_exp_f32 v165, v165\n"
      "v_exp_f32 v166, v166\n"
      "v_exp_f32 v167, v167\n"
      "v_exp_f32 v168, v168\n"
      "buffer_store_dwordx4 v[88:91], v45, s[44:47], s14 offen sc1\n"
      "s_add_u32 s14, s14, 262144\n"
      "v_cvt_pk_bf16_f32 v244, v160, v161\n"
      "v_cvt_pk_bf16_f32 v245, v162, v163\n"
      "v_cvt_pk_bf16_f32 v246, v164, v165\n"
      "v_cvt_pk_bf16_f32 v247, v166, v167\n"
      "v_exp_f32 v169, v169\n"
      "v_exp_f32 v170, v170\n"
      "s_waitcnt lgkmcnt(0)\n"
      "v_mfma_f32_32x32x16_bf16 v[192:207], v[56:59], v[244:247], 0\n"
      "v_exp_f32 v171, v171\n"
      "v_exp_f32 v172, v172\n"
      "v_exp_f32 v173, v173\n"
      "v_exp_f32 v174, v174\n"
      "v_exp_f32 v175, v175\n"
      "buffer_store_dwordx4 v[92:95], v45, s[44:47], s14 offen sc1\n"
      "s_add_u32 s14, s14, 262144\n"
      "v_cvt_pk_bf16_f32 v248, v168, v169\n"
      "v_cvt_pk_bf16_f32 v249, v170, v171\n"
      "v_cvt_pk_bf16_f32 v250, v172, v173\n"
      "v_cvt_pk_bf16_f32 v251, v174, v175\n"
      "ds_read_b128 v[64:67], v50 offset:1056\n"
      "ds_read_b128 v[68:71], v51 offset:1056\n"
      "v_mfma_f32_32x32x16_bf16 v[192:207], v[60:63], v[248:251], v[192:207]\n"
      "v_exp_f32 v176, v176\n"
      "v_exp_f32 v177, v177\n"
      "v_exp_f32 v178, v178\n"
      "v_exp_f32 v179, v179\n"
      "v_mfma_f32_32x32x16_bf16 v[160:175], v[236:239], v[228:231], v[128:143]\n"
      "v_exp_f32 v180, v180\n"
      "v_exp_f32 v181, v181\n"
      "v_exp_f32 v182, v182\n"
      "v_exp_f32 v183, v183\n"
      "v_exp_f32 v184, v184\n"
      "buffer_store_dwordx4 v[96:99], v45, s[44:47], s14 offen sc1\n"
      "s_add_u32 s14, s14, 262144\n"
      "v_cvt_pk_bf16_f32 v244, v176, v177\n"
      "v_cvt_pk_bf16_f32 v245, v178, v179\n"
      "v_cvt_pk_bf16_f32 v246, v180, v181\n"
      "v_cvt_pk_bf16_f32 v247, v182, v183\n"
      "v_exp_f32 v185, v185\n"
      "v_exp_f32 v186, v186\n"
      "s_waitcnt lgkmcnt(0)\n"
      "v_mfma_f32_32x32x16_bf16 v[192:207], v[64:67], v[244:247], v[192:207]\n"
      "v_exp_f32 v187, v187\n"
      "v_exp_f32 v188, v188\n"
      "v_exp_f32 v189, v189\n"
      "v_exp_f32 v190, v190\n"
      "v_exp_f32 v191, v191\n"
      "s_mov_b32 exec_hi, 0\n"
      "buffer_store_dwordx4 v[100:103], v46, s[44:47], s14 offen sc1\n"
      "s_mov_b64 exec, -1\n"
      "v_cvt_pk_bf16_f32 v248, v184, v185\n"
      "v_cvt_pk_bf16_f32 v249, v186, v187\n"
      "v_cvt_pk_bf16_f32 v250, v188, v189\n"
      "v_cvt_pk_bf16_f32 v251, v190, v191\n"
      "s_waitcnt vmcnt(6)\n"
      "s_barrier\n"
      "ds_read_b128 v[240:243], v49 offset:2048\n"
      "ds_read_b128 v[56:59], v50 offset:2112\n"
      "ds_read_b128 v[60:63], v51 offset:2112\n"
      "ds_read_b128 v[232:235], v49 offset:3072\n"
      "v_mfma_f32_32x32x16_bf16 v[192:207], v[68:71], v[248:251], v[192:207]\n"
      "v_exp_f32 v160, v160\n"
      "v_exp_f32 v161, v161\n"
      "v_exp_f32 v162, v162\n"
      "v_exp_f32 v163, v163\n"
      "s_waitcnt lgkmcnt(3)\n"
      "v_mfma_f32_32x32x16_bf16 v[176:191], v[240:243], v[224:227], 0\n"
      "v_exp_f32 v164, v164\n"
      "v_exp_f32 v165, v165\n"
      "v_exp_f32 v166, v166\n"
      "v_exp_f32 v167, v167\n"
      "v_exp_f32 v168, v168\n"
      "s_and_b32 s77, s70, 3\n"
      "s_mul_i32 s77, s77, 229376\n"
      "s_add_u32 s77, s77, s93\n"
      "s_add_u32 s77, s77, 0\n"
      "s_mov_b32 exec_hi, 16777215\n"
      "buffer_load_dword a255, v255, s[40:43], s77 offen\n"
      "s_mov_b64 exec, -1\n"
      "v_cvt_pk_bf16_f32 v244, v160, v161\n"
      "v_cvt_pk_bf16_f32 v245, v162, v163\n"
      "v_cvt_pk_bf16_f32 v246, v164, v165\n"
      "v_cvt_pk_bf16_f32 v247, v166, v167\n"
      "v_exp_f32 v169, v169\n"
      "v_exp_f32 v170, v170\n"
      "v_mfma_f32_32x32x16_bf16 v[208:223], v[64:67], v[244:247], 0\n"
      "v_exp_f32 v171, v171\n"
      "v_exp_f32 v172, v172\n"
      "v_exp_f32 v173, v173\n"
      "v_exp_f32 v174, v174\n"
      "v_exp_f32 v175, v175\n"
      "v_cvt_pk_bf16_f32 v248, v168, v169\n"
      "v_cvt_pk_bf16_f32 v249, v170, v171\n"
      "v_cvt_pk_bf16_f32 v250, v172, v173\n"
      "v_cvt_pk_bf16_f32 v251, v174, v175\n"
      "s_nop 1\n"
      "v_mfma_f32_32x32x16_bf16 v[208:223], v[68:71], v[248:251], v[208:223]\n"
      "v_exp_f32 v176, v176\n"
      "v_exp_f32 v177, v177\n"
      "v_exp_f32 v178, v178\n"
      "v_exp_f32 v179, v179\n"
      "v_mfma_f32_32x32x16_bf16 v[160:175], v[240:243], v[228:231], 0\n"
      "v_exp_f32 v180, v180\n"
      "v_exp_f32 v181, v181\n"
      "v_exp_f32 v182, v182\n"
      "v_exp_f32 v183, v183\n"
      "v_exp_f32 v184, v184\n"
      "s_lshl_b32 s78, s71, 5\n"
      "buffer_load_dwordx2 v[34:35], v252, s[52:55], s78 offen\n"
      "v_cvt_pk_bf16_f32 v244, v176, v177\n"
      "v_cvt_pk_bf16_f32 v245, v178, v179\n"
      "v_cvt_pk_bf16_f32 v246, v180, v181\n"
      "v_cvt_pk_bf16_f32 v247, v182, v183\n"
      "v_exp_f32 v185, v185\n"
      "v_exp_f32 v186, v186\n"
      "s_waitcnt lgkmcnt(1)\n"
      "v_mfma_f32_32x32x16_bf16 v[192:207], v[56:59], v[244:247], v[192:207]\n"
      "v_exp_f32 v187, v187\n"
      "v_exp_f32 v188, v188\n"
      "v_exp_f32 v189, v189\n"
      "v_exp_f32 v190, v190\n"
      "v_exp_f32 v191, v191\n"
      "buffer_load_dwordx2 v[36:37], v252, s[52:55], s78 offen offset:32\n"
      "v_cvt_pk_bf16_f32 v248, v184, v185\n"
      "v_cvt_pk_bf16_f32 v249, v186, v187\n"
      "v_cvt_pk_bf16_f32 v250, v188, v189\n"
      "v_cvt_pk_bf16_f32 v251, v190, v191\n"
      "ds_read_b128 v[64:67], v50 offset:3168\n"
      "ds_read_b128 v[68:71], v51 offset:3168\n"
      "ds_read_b128 v[236:239], v49 offset:4096\n"
      "v_mfma_f32_32x32x16_bf16 v[192:207], v[60:63], v[248:251], v[192:207]\n"
      "v_exp_f32 v160, v160\n"
      "v_exp_f32 v161, v161\n"
      "v_exp_f32 v162, v162\n"
      "v_exp_f32 v163, v163\n"
      "s_waitcnt lgkmcnt(3)\n"
      "v_mfma_f32_32x32x16_bf16 v[176:191], v[232:235], v[224:227], 0\n"
      "v_exp_f32 v164, v164\n"
      "v_exp_f32 v165, v165\n"
      "v_exp_f32 v166, v166\n"
      "v_exp_f32 v167, v167\n"
      "v_exp_f32 v168, v168\n"
      "buffer_load_dwordx2 v[38:39], v252, s[52:55], s78 offen offset:64\n"
      "v_cvt_pk_bf16_f32 v244, v160, v161\n"
      "v_cvt_pk_bf16_f32 v245, v162, v163\n"
      "v_cvt_pk_bf16_f32 v246, v164, v165\n"
      "v_cvt_pk_bf16_f32 v247, v166, v167\n"
      "v_exp_f32 v169, v169\n"
      "v_exp_f32 v170, v170\n"
      "v_mfma_f32_32x32x16_bf16 v[208:223], v[56:59], v[244:247], v[208:223]\n"
      "v_exp_f32 v171, v171\n"
      "v_exp_f32 v172, v172\n"
      "v_exp_f32 v173, v173\n"
      "v_exp_f32 v174, v174\n"
      "v_exp_f32 v175, v175\n"
      "s_lshl_b32 s78, s69, 7\n"
      "buffer_load_ushort v40, v253, s[56:59], s78 offen\n"
      "v_cvt_pk_bf16_f32 v248, v168, v169\n"
      "v_cvt_pk_bf16_f32 v249, v170, v171\n"
      "v_cvt_pk_bf16_f32 v250, v172, v173\n"
      "v_cvt_pk_bf16_f32 v251, v174, v175\n"
      "s_nop 1\n"
      "v_mfma_f32_32x32x16_bf16 v[208:223], v[60:63], v[248:251], v[208:223]\n"
      "v_exp_f32 v176, v176\n"
      "v_exp_f32 v177, v177\n"
      "v_exp_f32 v178, v178\n"
      "v_exp_f32 v179, v179\n"
      "v_mfma_f32_32x32x16_bf16 v[160:175], v[232:235], v[228:231], 0\n"
      "v_exp_f32 v180, v180\n"
      "v_exp_f32 v181, v181\n"
      "v_exp_f32 v182, v182\n"
      "v_exp_f32 v183, v183\n"
      "v_exp_f32 v184, v184\n"
      "buffer_load_ushort v41, v253, s[56:59], s78 offen offset:32\n"
      "v_cvt_pk_bf16_f32 v244, v176, v177\n"
      "v_cvt_pk_bf16_f32 v245, v178, v179\n"
      "v_cvt_pk_bf16_f32 v246, v180, v181\n"
      "v_cvt_pk_bf16_f32 v247, v182, v183\n"
      "v_exp_f32 v185, v185\n"
      "v_exp_f32 v186, v186\n"
      "s_waitcnt lgkmcnt(1)\n"
      "v_mfma_f32_32x32x16_bf16 v[192:207], v[64:67], v[244:247], v[192:207]\n"
      "v_exp_f32 v187, v187\n"
      "v_exp_f32 v188, v188\n"
      "v_exp_f32 v189, v189\n"
      "v_exp_f32 v190, v190\n"
      "v_exp_f32 v191, v191\n"
      "buffer_load_ushort v42, v253, s[56:59], s78 offen offset:64\n"
      "v_cvt_pk_bf16_f32 v248, v184, v185\n"
      "v_cvt_pk_bf16_f32 v249, v186, v187\n"
      "v_cvt_pk_bf16_f32 v250, v188, v189\n"
      "v_cvt_pk_bf16_f32 v251, v190, v191\n"
      "ds_read_b128 v[56:59], v50 offset:4224\n"
      "ds_read_b128 v[60:63], v51 offset:4224\n"
      "ds_read_b128 v[240:243], v49 offset:5120\n"
      "v_mfma_f32_32x32x16_bf16 v[192:207], v[68:71], v[248:251], v[192:207]\n"
      "v_exp_f32 v160, v160\n"
      "v_exp_f32 v161, v161\n"
      "v_exp_f32 v162, v162\n"
      "v_exp_f32 v163, v163\n"
      "s_waitcnt lgkmcnt(3)\n"
      "v_mfma_f32_32x32x16_bf16 v[176:191], v[236:239], v[224:227], 0\n"
      "v_exp_f32 v164, v164\n"
      "v_exp_f32 v165, v165\n"
      "v_exp_f32 v166, v166\n"
      "v_exp_f32 v167, v167\n"
      "v_exp_f32 v168, v168\n"
      "buffer_load_ushort v43, v253, s[56:59], s78 offen offset:96\n"
      "v_cvt_pk_bf16_f32 v244, v160, v161\n"
      "v_cvt_pk_bf16_f32 v245, v162, v163\n"
      "v_cvt_pk_bf16_f32 v246, v164, v165\n"
      "v_cvt_pk_bf16_f32 v247, v166, v167\n"
      "v_exp_f32 v169, v169\n"
      "v_exp_f32 v170, v170\n"
      "v_mfma_f32_32x32x16_bf16 v[208:223], v[64:67], v[244:247], v[208:223]\n"
      "v_exp_f32 v171, v171\n"
      "v_exp_f32 v172, v172\n"
      "v_exp_f32 v173, v173\n"
      "v_exp_f32 v174, v174\n"
      "v_exp_f32 v175, v175\n"
      "v_cvt_pk_bf16_f32 v248, v168, v169\n"
      "v_cvt_pk_bf16_f32 v249, v170, v171\n"
      "v_cvt_pk_bf16_f32 v250, v172, v173\n"
      "v_cvt_pk_bf16_f32 v251, v174, v175\n"
      "s_nop 1\n"
      "v_mfma_f32_32x32x16_bf16 v[208:223], v[68:71], v[248:251], v[208:223]\n"
      "v_exp_f32 v176, v176\n"
      "v_exp_f32 v177, v177\n"
      "v_exp_f32 v178, v178\n"
      "v_exp_f32 v179, v179\n"
      "v_mfma_f32_32x32x16_bf16 v[160:175], v[236:239], v[228:231], 0\n"
      "v_exp_f32 v180, v180\n"
      "v_exp_f32 v181, v181\n"
      "v_exp_f32 v182, v182\n"
      "v_exp_f32 v183, v183\n"
      "v_exp_f32 v184, v184\n"
      "s_waitcnt vmcnt(8)\n"
      "s_mov_b64 exec, 1\n"
      "buffer_store_dwordx2 v[4:5], v2, s[44:47], s15 offen sc1\n"
      "s_mov_b64 exec, -1\n"
      "v_cvt_pk_bf16_f32 v244, v176, v177\n"
      "v_cvt_pk_bf16_f32 v245, v178, v179\n"
      "v_cvt_pk_bf16_f32 v246, v180, v181\n"
      "v_cvt_pk_bf16_f32 v247, v182, v183\n"
      "v_exp_f32 v185, v185\n"
      "v_exp_f32 v186, v186\n"
      "s_waitcnt lgkmcnt(1)\n"
      "v_mfma_f32_32x32x16_bf16 v[192:207], v[56:59], v[244:247], v[192:207]\n"
      "v_exp_f32 v187, v187\n"
      "v_exp_f32 v188, v188\n"
      "v_exp_f32 v189, v189\n"
      "v_exp_f32 v190, v190\n"
      "v_exp_f32 v191, v191\n"
      "v_cvt_pk_bf16_f32 v248, v184, v185\n"
      "v_cvt_pk_bf16_f32 v249, v186, v187\n"
      "v_cvt_pk_bf16_f32 v250, v188, v189\n"
      "v_cvt_pk_bf16_f32 v251, v190, v191\n"
      "ds_read_b128 v[64:67], v50 offset:5280\n"
      "ds_read_b128 v[68:71], v51 offset:5280\n"
      "ds_read_b128 v[232:235], v49 offset:6144\n"
      "v_mfma_f32_32x32x16_bf16 v[192:207], v[60:63], v[248:251], v[192:207]\n"
      "v_exp_f32 v160, v160\n"
      "v_exp_f32 v161, v161\n"
      "v_exp_f32 v162, v162\n"
      "v_exp_f32 v163, v163\n"
      "s_waitcnt lgkmcnt(3)\n"
      "v_mfma_f32_32x32x16_bf16 v[176:191], v[240:243], v[224:227], 0\n"
      "v_exp_f32 v164, v164\n"
      "v_exp_f32 v165, v165\n"
      "v_exp_f32 v166, v166\n"
      "v_exp_f32 v167, v167\n"
      "v_exp_f32 v168, v168\n"
      "s_lshl_b32 s78, s82, 14\n"
      "s_lshl_b32 s77, s70, 7\n"
      "s_add_u32 s78, s78, s77\n"
      "s_lshl_b32 s77, s62, 11\n"
      "s_add_u32 s77, s77, 106496\n"
      "s_mov_b32 m0, s77\n"
      "s_nop 0\n"
      "buffer_load_dwordx4 v16, s[24:27], s78 offen lds\n"
      "v_cvt_pk_bf16_f32 v244, v160, v161\n"
      "v_cvt_pk_bf16_f32 v245, v162, v163\n"
      "v_cvt_pk_bf16_f32 v246, v164, v165\n"
      "v_cvt_pk_bf16_f32 v247, v166, v167\n"
      "v_exp_f32 v169, v169\n"
      "v_exp_f32 v170, v170\n"
      "v_mfma_f32_32x32x16_bf16 v[208:223], v[56:59], v[244:247], v[208:223]\n"
      "v_exp_f32 v171, v171\n"
      "v_exp_f32 v172, v172\n"
      "v_exp_f32 v173, v173\n"
      "v_exp_f32 v174, v174\n"
      "v_exp_f32 v175, v175\n"
      "s_lshl_b32 s78, s82, 14\n"
      "s_lshl_b32 s77, s70, 7\n"
      "s_add_u32 s78, s78, s77\n"
      "s_add_u32 s78, s78, 8192\n"
      "s_lshl_b32 s77, s62, 11\n"
      "s_add_u32 s77, s77, 107520\n"
      "s_mov_b32 m0, s77\n"
      "s_nop 0\n"
      "buffer_load_dwordx4 v16, s[24:27], s78 offen lds\n"
      "v_cvt_pk_bf16_f32 v248, v168, v169\n"
      "v_cvt_pk_bf16_f32 v249, v170, v171\n"
      "v_cvt_pk_bf16_f32 v250, v172, v173\n"
      "v_cvt_pk_bf16_f32 v251, v174, v175\n"
      "s_nop 1\n"
      "v_mfma_f32_32x32x16_bf16 v[208:223], v[60:63], v[248:251], v[208:223]\n"
      "v_exp_f32 v176, v176\n"
      "v_exp_f32 v177, v177\n"
      "v_exp_f32 v178, v178\n"
      "v_exp_f32 v179, v179\n"
      "v_mfma_f32_32x32x16_bf16 v[160:175], v[240:243], v[228:231], 0\n"
      "v_exp_f32 v180, v180\n"
      "v_exp_f32 v181, v181\n"
      "v_exp_f32 v182, v182\n"
      "v_exp_f32 v183, v183\n"
      "v_exp_f32 v184, v184\n"
      "v_cvt_pk_bf16_f32 v244, v176, v177\n"
      "v_cvt_pk_bf16_f32 v245, v178, v179\n"
      "v_cvt_pk_bf16_f32 v246, v180, v181\n"
      "v_cvt_pk_bf16_f32 v247, v182, v183\n"
      "v_exp_f32 v185, v185\n"
      "v_exp_f32 v186, v186\n"
      "s_waitcnt lgkmcnt(1)\n"
      "v_mfma_f32_32x32x16_bf16 v[192:207], v[64:67], v[244:247], v[192:207]\n"
      "v_exp_f32 v187, v187\n"
      "v_exp_f32 v188, v188\n"
      "v_exp_f32 v189, v189\n"
      "v_exp_f32 v190, v190\n"
      "v_exp_f32 v191, v191\n"
      "v_cvt_pk_bf16_f32 v248, v184, v185\n"
      "v_cvt_pk_bf16_f32 v249, v186, v187\n"
      "v_cvt_pk_bf16_f32 v250, v188, v189\n"
      "v_cvt_pk_bf16_f32 v251, v190, v191\n"
      "ds_read_b128 v[56:59], v50 offset:6336\n"
      "ds_read_b128 v[60:63], v51 offset:6336\n"
      "ds_read_b128 v[236:239], v49 offset:7168\n"
      "v_mfma_f32_32x32x16_bf16 v[192:207], v[68:71], v[248:251], v[192:207]\n"
      "v_exp_f32 v160, v160\n"
      "v_exp_f32 v161, v161\n"
      "v_exp_f32 v162, v162\n"
      "v_exp_f32 v163, v163\n"
      "s_waitcnt lgkmcnt(3)\n"
      "v_mfma_f32_32x32x16_bf16 v[176:191], v[232:235], v[224:227], 0\n"
      "v_exp_f32 v164, v164\n"
      "v_exp_f32 v165, v165\n"
      "v_exp_f32 v166, v166\n"
      "v_exp_f32 v167, v167\n"
      "v_exp_f32 v168, v168\n"
      "v_cvt_pk_bf16_f32 v244, v160, v161\n"
      "v_cvt_pk_bf16_f32 v245, v162, v163\n"
      "v_cvt_pk_bf16_f32 v246, v164, v165\n"
      "v_cvt_pk_bf16_f32 v247, v166, v167\n"
      "v_exp_f32 v169, v169\n"
      "v_exp_f32 v170, v170\n"
      "v_mfma_f32_32x32x16_bf16 v[208:223], v[64:67], v[244:247], v[208:223]\n"
      "v_exp_f32 v171, v171\n"
      "v_exp_f32 v172, v172\n"
      "v_exp_f32 v173, v173\n"
      "v_exp_f32 v174, v174\n"
      "v_exp_f32 v175, v175\n"
      "v_cvt_pk_bf16_f32 v248, v168, v169\n"
      "v_cvt_pk_bf16_f32 v249, v170, v171\n"
      "v_cvt_pk_bf16_f32 v250, v172, v173\n"
      "v_cvt_pk_bf16_f32 v251, v174, v175\n"
      "s_nop 1\n"
      "v_mfma_f32_32x32x16_bf16 v[208:223], v[68:71], v[248:251], v[208:223]\n"
      "v_exp_f32 v176, v176\n"
      "v_exp_f32 v177, v177\n"
      "v_exp_f32 v178, v178\n"
      "v_exp_f32 v179, v179\n"
      "v_mfma_f32_32x32x16_bf16 v[160:175], v[232:235], v[228:231], 0\n"
      "v_exp_f32 v180, v180\n"
      "v_exp_f32 v181, v181\n"
      "v_exp_f32 v182, v182\n"
      "v_exp_f32 v183, v183\n"
      "v_exp_f32 v184, v184\n"
      "v_cvt_pk_bf16_f32 v244, v176, v177\n"
      "v_cvt_pk_bf16_f32 v245, v178, v179\n"
      "v_cvt_pk_bf16_f32 v246, v180, v181\n"
      "v_cvt_pk_bf16_f32 v247, v182, v183\n"
      "v_exp_f32 v185, v185\n"
      "v_exp_f32 v186, v186\n"
      "s_waitcnt lgkmcnt(1)\n"
      "v_mfma_f32_32x32x16_bf16 v[192:207], v[56:59], v[244:247], v[192:207]\n"
      "v_exp_f32 v187, v187\n"
      "v_exp_f32 v188, v188\n"
      "v_exp_f32 v189, v189\n"
      "v_exp_f32 v190, v190\n"
      "v_exp_f32 v191, v191\n"
      "v_cvt_pk_bf16_f32 v248, v184, v185\n"
      "v_cvt_pk_bf16_f32 v249, v186, v187\n"
      "v_cvt_pk_bf16_f32 v250, v188, v189\n"
      "v_cvt_pk_bf16_f32 v251, v190, v191\n"
      "ds_read_b128 v[64:67], v50 offset:7392\n"
      "ds_read_b128 v[68:71], v51 offset:7392\n"
      "ds_read_b128 v[240:243], v49 offset:8192\n"
      "v_mfma_f32_32x32x16_bf16 v[192:207], v[60:63], v[248:251], v[192:207]\n"
      "v_exp_f32 v160, v160\n"
      "v_exp_f32 v161, v161\n"
      "v_exp_f32 v162, v162\n"
      "v_exp_f32 v163, v163\n"
      "s_waitcnt lgkmcnt(3)\n"
      "v_mfma_f32_32x32x16_bf16 v[176:191], v[236:239], v[224:227], 0\n"
      "v_exp_f32 v164, v164\n"
      "v_exp_f32 v165, v165\n"
      "v_exp_f32 v166, v166\n"
      "v_exp_f32 v167, v167\n"
      "v_exp_f32 v168, v168\n"
      "v_cvt_pk_bf16_f32 v244, v160, v161\n"
      "v_cvt_pk_bf16_f32 v245, v162, v163\n"
      "v_cvt_pk_bf16_f32 v246, v164, v165\n"
      "v_cvt_pk_bf16_f32 v247, v166, v167\n"
      "v_exp_f32 v169, v169\n"
      "v_exp_f32 v170, v170\n"
      "v_mfma_f32_32x32x16_bf16 v[208:223], v[56:59], v[244:247], v[208:223]\n"
      "v_exp_f32 v171, v171\n"
      "v_exp_f32 v172, v172\n"
      "v_exp_f32 v173, v173\n"
      "v_exp_f32 v174, v174\n"
      "v_exp_f32 v175, v175\n"
      "v_cvt_pk_bf16_f32 v248, v168, v169\n"
      "v_cvt_pk_bf16_f32 v249, v170, v171\n"
      "v_cvt_pk_bf16_f32 v250, v172, v173\n"
      "v_cvt_pk_bf16_f32 v251, v174, v175\n"
      "s_nop 1\n"
      "v_mfma_f32_32x32x16_bf16 v[208:223], v[60:63], v[248:251], v[208:223]\n"
      "v_exp_f32 v176, v176\n"
      "v_exp_f32 v177, v177\n"
      "v_exp_f32 v178, v178\n"
      "v_exp_f32 v179, v179\n"
      "v_mfma_f32_32x32x16_bf16 v[160:175], v[236:239], v[228:231], 0\n"
      "v_exp_f32 v180, v180\n"
      "v_exp_f32 v181, v181\n"
      "v_exp_f32 v182, v182\n"
      "v_exp_f32 v183, v183\n"
      "v_exp_f32 v184, v184\n"
      "v_cvt_pk_bf16_f32 v244, v176, v177\n"
      "v_cvt_pk_bf16_f32 v245, v178, v179\n"
      "v_cvt_pk_bf16_f32 v246, v180, v181\n"
      "v_cvt_pk_bf16_f32 v247, v182, v183\n"
      "v_exp_f32 v185, v185\n"
      "v_exp_f32 v186, v186\n"
      "s_waitcnt lgkmcnt(1)\n"
      "v_mfma_f32_32x32x16_bf16 v[192:207], v[64:67], v[244:247], v[192:207]\n"
      "v_exp_f32 v187, v187\n"
      "v_exp_f32 v188, v188\n"
      "v_exp_f32 v189, v189\n"
      "v_exp_f32 v190, v190\n"
      "v_exp_f32 v191, v191\n"
      "v_cvt_pk_bf16_f32 v248, v184, v185\n"
      "v_cvt_pk_bf16_f32 v249, v186, v187\n"
      "v_cvt_pk_bf16_f32 v250, v188, v189\n"
      "v_cvt_pk_bf16_f32 v251, v190, v191\n"
      "ds_read_b128 v[56:59], v50 offset:8448\n"
      "ds_read_b128 v[60:63], v51 offset:8448\n"
      "ds_read_b128 v[232:235], v49 offset:9216\n"
      "v_mfma_f32_32x32x16_bf16 v[192:207], v[68:71], v[248:251], v[192:207]\n"
      "v_exp_f32 v160, v160\n"
      "v_exp_f32 v161, v161\n"
      "v_exp_f32 v162, v162\n"
      "v_exp_f32 v163, v163\n"
      "s_waitcnt lgkmcnt(3)\n"
      "v_mfma_f32_32x32x16_bf16 v[176:191], v[240:243], v[224:227], v[144:159]\n"
      "v_exp_f32 v164, v164\n"
      "v_exp_f32 v165, v165\n"
      "v_exp_f32 v166, v166\n"
      "v_exp_f32 v167, v167\n"
      "v_exp_f32 v168, v168\n"
      "v_cvt_pk_bf16_f32 v244, v160, v161\n"
      "v_cvt_pk_bf16_f32 v245, v162, v163\n"
      "v_cvt_pk_bf16_f32 v246, v164, v165\n"
      "v_cvt_pk_bf16_f32 v247, v166, v167\n"
      "v_exp_f32 v169, v169\n"
      "v_exp_f32 v170, v170\n"
      "v_mfma_f32_32x32x16_bf16 v[208:223], v[64:67], v[244:247], v[208:223]\n"
      "v_exp_f32 v171, v171\n"
      "v_exp_f32 v172, v172\n"
      "v_exp_f32 v173, v173\n"
      "v_exp_f32 v174, v174\n"
      "v_exp_f32 v175, v175\n"
      "v_cvt_pk_bf16_f32 v248, v168, v169\n"
      "v_cvt_pk_bf16_f32 v249, v170, v171\n"
      "v_cvt_pk_bf16_f32 v250, v172, v173\n"
      "v_cvt_pk_bf16_f32 v251, v174, v175\n"
      "s_nop 1\n"
      "v_mfma_f32_32x32x16_bf16 v[208:223], v[68:71], v[248:251], v[208:223]\n"
      "v_exp_f32 v176, v176\n"
      "v_exp_f32 v177, v177\n"
      "v_exp_f32 v178, v178\n"
      "v_exp_f32 v179, v179\n"
      "v_mfma_f32_32x32x16_bf16 v[160:175], v[240:243], v[228:231], 0\n"
      "v_exp_f32 v180, v180\n"
      "v_exp_f32 v181, v181\n"
      "v_exp_f32 v182, v182\n"
      "v_exp_f32 v183, v183\n"
      "v_exp_f32 v184, v184\n"
      "v_cvt_pk_bf16_f32 v244, v176, v177\n"
      "v_cvt_pk_bf16_f32 v245, v178, v179\n"
      "v_cvt_pk_bf16_f32 v246, v180, v181\n"
      "v_cvt_pk_bf16_f32 v247, v182, v183\n"
      "v_exp_f32 v185, v185\n"
      "v_exp_f32 v186, v186\n"
      "s_waitcnt lgkmcnt(1)\n"
      "v_mfma_f32_32x32x16_bf16 v[192:207], v[56:59], v[244:247], v[192:207]\n"
      "v_exp_f32 v187, v187\n"
      "v_exp_f32 v188, v188\n"
      "v_exp_f32 v189, v189\n"
      "v_exp_f32 v190, v190\n"
      "v_exp_f32 v191, v191\n"
      "v_cvt_pk_bf16_f32 v248, v184, v185\n"
      "v_cvt_pk_bf16_f32 v249, v186, v187\n"
      "v_cvt_pk_bf16_f32 v250, v188, v189\n"
      "v_cvt_pk_bf16_f32 v251, v190, v191\n"
      "ds_read_b128 v[64:67], v50 offset:9504\n"
      "ds_read_b128 v[68:71], v51 offset:9504\n"
      "v_mfma_f32_32x32x16_bf16 v[192:207], v[60:63], v[248:251], v[192:207]\n"
      "v_exp_f32 v160, v160\n"
      "v_exp_f32 v161, v161\n"
      "v_exp_f32 v162, v162\n"
      "v_exp_f32 v163, v163\n"
      "s_waitcnt lgkmcnt(2)\n"
      "v_mfma_f32_32x32x16_bf16 v[176:191], v[232:235], v[228:231], v[144:159]\n"
      "v_exp_f32 v164, v164\n"
      "v_exp_f32 v165, v165\n"
      "v_exp_f32 v166, v166\n"
      "v_exp_f32 v167, v167\n"
      "v_exp_f32 v168, v168\n"
      "buffer_load_dwordx2 v[24:25], v55, s[44:47], 0 offen sc1\n"
      "v_cvt_pk_bf16_f32 v244, v160, v161\n"
      "v_cvt_pk_bf16_f32 v245, v162, v163\n"
      "v_cvt_pk_bf16_f32 v246, v164, v165\n"
      "v_cvt_pk_bf16_f32 v247, v166, v167\n"
      "v_exp_f32 v169, v169\n"
      "v_exp_f32 v170, v170\n"
      "v_mfma_f32_32x32x16_bf16 v[208:223], v[56:59], v[244:247], v[208:223]\n"
      "v_exp_f32 v171, v171\n"
      "v_exp_f32 v172, v172\n"
      "v_exp_f32 v173, v173\n"
      "v_exp_f32 v174, v174\n"
      "v_exp_f32 v175, v175\n"
      "v_cvt_pk_bf16_f32 v248, v168, v169\n"
      "v_cvt_pk_bf16_f32 v249, v170, v171\n"
      "v_cvt_pk_bf16_f32 v250, v172, v173\n"
      "v_cvt_pk_bf16_f32 v251, v174, v175\n"
      "s_nop 1\n"
      "v_mfma_f32_32x32x16_bf16 v[208:223], v[60:63], v[248:251], v[208:223]\n"
      "v_exp_f32 v176, v176\n"
      "v_exp_f32 v177, v177\n"
      "v_exp_f32 v178, v178\n"
      "v_exp_f32 v179, v179\n"
      "v_exp_f32 v180, v180\n"
      "v_exp_f32 v181, v181\n"
      "v_exp_f32 v182, v182\n"
      "v_exp_f32 v183, v183\n"
      "v_exp_f32 v184, v184\n"
      "v_cvt_pk_bf16_f32 v244, v176, v177\n"
      "v_cvt_pk_bf16_f32 v245, v178, v179\n"
      "v_cvt_pk_bf16_f32 v246, v180, v181\n"
      "v_cvt_pk_bf16_f32 v247, v182, v183\n"
      "v_exp_f32 v185, v185\n"
      "v_exp_f32 v186, v186\n"
      "s_waitcnt lgkmcnt(0)\n"
      "v_mfma_f32_32x32x16_bf16 v[208:223], v[64:67], v[244:247], v[208:223]\n"
      "v_exp_f32 v187, v187\n"
      "v_exp_f32 v188, v188\n"
      "v_exp_f32 v189, v189\n"
      "v_exp_f32 v190, v190\n"
      "v_exp_f32 v191, v191\n"
      "v_cvt_pk_bf16_f32 v248, v184, v185\n"
      "v_cvt_pk_bf16_f32 v249, v186, v187\n"
      "v_cvt_pk_bf16_f32 v250, v188, v189\n"
      "v_cvt_pk_bf16_f32 v251, v190, v191\n"
      "s_nop 1\n"
      "v_mfma_f32_32x32x16_bf16 v[208:223], v[68:71], v[248:251], v[208:223]\n"
      "s_mov_b32 s73, 729808896\n"
      "s_mov_b32 s74, 1400897536\n"
      "v_cmp_le_f32_e32 vcc, s73, v200\n"
      "v_cmp_ge_f32_e64 s[98:99], s74, v200\n"
      "v_rcp_f32 v20, v200\n"
      "s_and_b64 s[28:29], vcc, s[98:99]\n"
      "s_nop 5\n"
      "v_cmp_le_f32_e32 vcc, s73, v216\n"
      "v_cmp_ge_f32_e64 s[98:99], s74, v216\n"
      "v_rcp_f32 v22, v216\n"
      "s_and_b64 s[28:29], s[28:29], vcc\n"
      "s_and_b64 s[28:29], s[28:29], s[98:99]\n"
      "s_cmp_eq_u64 s[28:29], -1\n"
      "s_cbranch_scc0 .Lfallback_13_%=\n"
      ".Lattn_epi_14_%=:\n"
      "v_pk_mul_f32 v[192:193], v[192:193], v[20:21] op_sel_hi:[1,0]\n"
      "v_pk_mul_f32 v[194:195], v[194:195], v[20:21] op_sel_hi:[1,0]\n"
      "v_pk_mul_f32 v[196:197], v[196:197], v[20:21] op_sel_hi:[1,0]\n"
      "v_pk_mul_f32 v[198:199], v[198:199], v[20:21] op_sel_hi:[1,0]\n"
      "v_cvt_pk_bf16_f32 v244, v192, v193\n"
      "v_cvt_pk_bf16_f32 v245, v194, v195\n"
      "v_cvt_pk_bf16_f32 v246, v196, v197\n"
      "v_cvt_pk_bf16_f32 v247, v198, v199\n"
      "buffer_store_dwordx4 v[244:247], v18, s[44:47], s81 offen sc1\n"
      "v_pk_mul_f32 v[208:209], v[208:209], v[22:23] op_sel_hi:[1,0]\n"
      "v_pk_mul_f32 v[210:211], v[210:211], v[22:23] op_sel_hi:[1,0]\n"
      "v_pk_mul_f32 v[212:213], v[212:213], v[22:23] op_sel_hi:[1,0]\n"
      "v_pk_mul_f32 v[214:215], v[214:215], v[22:23] op_sel_hi:[1,0]\n"
      "v_cvt_pk_bf16_f32 v248, v208, v209\n"
      "v_cvt_pk_bf16_f32 v249, v210, v211\n"
      "v_cvt_pk_bf16_f32 v250, v212, v213\n"
      "v_cvt_pk_bf16_f32 v251, v214, v215\n"
      "buffer_store_dwordx4 v[248:251], v18, s[44:47], s79 offen sc1\n"
      "s_add_u32 s65, s62, 1\n"
      "s_and_b32 s65, s65, 3\n"
      "s_add_u32 s66, s62, 2\n"
      "s_and_b32 s66, s66, 3\n"
      "s_add_u32 s63, s62, 3\n"
      "s_and_b32 s63, s63, 3\n"
      "s_add_u32 s64, s62, 4\n"
      "s_and_b32 s64, s64, 3\n"
      "s_lshl_b32 s81, s82, 14\n"
      "s_lshl_b32 s76, s64, 12\n"
      "s_add_u32 s81, s81, s76\n"
      "s_add_u32 s81, s81, 7340032\n"
      "s_lshl_b32 s77, s64, 14\n"
      "v_add_u32 v49, s77, v1\n"
      "s_lshl_b32 s79, s82, 14\n"
      "s_lshl_b32 s76, s65, 12\n"
      "s_add_u32 s79, s79, s76\n"
      "s_add_u32 s79, s79, 7340032\n"
      "s_lshl_b32 s77, s65, 14\n"
      "v_add_u32 v109, s77, v1\n"
      "s_lshl_b32 s84, s82, 14\n"
      "s_lshl_b32 s76, s66, 12\n"
      "s_add_u32 s84, s84, s76\n"
      "s_add_u32 s84, s84, 7340032\n"
      "s_lshl_b32 s77, s66, 14\n"
      "v_add_u32 v110, s77, v1\n"
      "s_lshl_b32 s83, s82, 14\n"
      "s_lshl_b32 s76, s63, 12\n"
      "s_add_u32 s83, s83, s76\n"
      "s_add_u32 s83, s83, 7340032\n"
      "s_lshl_b32 s77, s63, 14\n"
      "v_add_u32 v111, s77, v1\n"
      "s_lshl_b32 s73, s62, 6\n"
      "s_lshl_b32 s76, s69, 8\n"
      "s_add_u32 s73, s73, s76\n"
      "s_add_u32 s73, s73, 131072\n"
      "v_mul_u32_u24 v20, 8, v6\n"
      "v_add_u32 v51, s73, v20\n"
      "s_waitcnt vmcnt(0)\n"
      "s_mov_b64 exec, 1\n"
      "buffer_store_dwordx2 v[4:5], v2, s[44:47], s80 offen sc1\n"
      "s_mov_b64 exec, -1\n"
      "ds_read_b64_tr_b16 a[192:193], v8\n"
      "ds_read_b64_tr_b16 a[194:195], v8 offset:1024\n"
      "ds_read_b64_tr_b16 a[196:197], v9\n"
      "ds_read_b64_tr_b16 a[198:199], v9 offset:1024\n"
      "ds_read_b64_tr_b16 a[200:201], v10\n"
      "ds_read_b64_tr_b16 a[202:203], v10 offset:1024\n"
      "ds_read_b64_tr_b16 a[204:205], v11\n"
      "ds_read_b64_tr_b16 a[206:207], v11 offset:1024\n"
      "ds_read_b64_tr_b16 a[208:209], v8 offset:4096\n"
      "ds_read_b64_tr_b16 a[210:211], v8 offset:5120\n"
      "ds_read_b64_tr_b16 a[212:213], v9 offset:4096\n"
      "ds_read_b64_tr_b16 a[214:215], v9 offset:5120\n"
      "ds_read_b64_tr_b16 a[216:217], v10 offset:4096\n"
      "ds_read_b64_tr_b16 a[218:219], v10 offset:5120\n"
      "ds_read_b64_tr_b16 a[220:221], v11 offset:4096\n"
      "ds_read_b64_tr_b16 a[222:223], v11 offset:5120\n"
      "ds_read_b64_tr_b16 a[224:225], v8 offset:8192\n"
      "ds_read_b64_tr_b16 a[226:227], v8 offset:9216\n"
      "ds_read_b64_tr_b16 a[228:229], v9 offset:8192\n"
      "ds_read_b64_tr_b16 a[230:231], v9 offset:9216\n"
      "ds_read_b64_tr_b16 a[232:233], v10 offset:8192\n"
      "ds_read_b64_tr_b16 a[234:235], v10 offset:9216\n"
      "ds_read_b64_tr_b16 a[236:237], v11 offset:8192\n"
      "ds_read_b64_tr_b16 a[238:239], v11 offset:9216\n"
      "ds_read_b64_tr_b16 a[240:241], v8 offset:12288\n"
      "ds_read_b64_tr_b16 a[242:243], v8 offset:13312\n"
      "ds_read_b64_tr_b16 a[244:245], v9 offset:12288\n"
      "ds_read_b64_tr_b16 a[246:247], v9 offset:13312\n"
      "ds_read_b64_tr_b16 a[248:249], v10 offset:12288\n"
      "ds_read_b64_tr_b16 a[250:251], v10 offset:13312\n"
      "ds_read_b64_tr_b16 a[252:253], v11 offset:12288\n"
      "ds_read_b64_tr_b16 a[254:255], v11 offset:13312\n"
      "v_cmp_ne_u64_e32 vcc, s[60:61], v[24:25]\n"
      "s_cbranch_vccz .Lw1ok_15_%=\n"
      "s_mov_b32 s96, 131072\n"
      "s_mov_b64 exec, -1\n"
      "s_nop 1\n"
      ".Lpoll_16_%=:\n"
      "buffer_load_dwordx2 v[24:25], v55, s[44:47], 0 offen sc1\n"
      "s_waitcnt vmcnt(0)\n"
      "v_cmp_ne_u64_e32 vcc, s[60:61], v[24:25]\n"
      "s_cbranch_vccz .Lpdone_17_%=\n"
      "s_sub_u32 s96, s96, 1\n"
      "s_cbranch_scc0 .Lpoll_16_%=\n"
      ".Lpdone_17_%=:\n"
      "s_mov_b64 exec, -1\n"
      ".Lw1ok_15_%=:\n"
      "s_add_u32 s78, s12, s4\n"
      "buffer_load_dwordx4 a[0:3], v1, s[44:47], s78 offen sc1\n"
      "s_sleep 1\n"
      "s_add_u32 s78, s12, s4\n"
      "buffer_load_dwordx4 a[12:15], v1, s[44:47], s78 offen offset:1024 sc1\n"
      "s_sleep 1\n"
      "s_add_u32 s78, s12, s4\n"
      "s_add_u32 s78, s78, 65536\n"
      "buffer_load_dwordx4 a[4:7], v1, s[44:47], s78 offen sc1\n"
      "s_sleep 1\n"
      "s_add_u32 s78, s12, s4\n"
      "s_add_u32 s78, s78, 65536\n"
      "buffer_load_dwordx4 a[16:19], v1, s[44:47], s78 offen offset:1024 sc1\n"
      "s_sleep 1\n"
      "s_add_u32 s78, s12, s4\n"
      "s_add_u32 s78, s78, 131072\n"
      "buffer_load_dwordx4 a[8:11], v48, s[44:47], s78 offen sc1\n"
      "s_sleep 1\n"
      "s_add_u32 s78, s12, s4\n"
      "s_add_u32 s78, s78, 131072\n"
      "buffer_load_dwordx4 a[20:23], v48, s[44:47], s78 offen offset:1024 sc1\n"
      "s_sleep 1\n"
      "s_add_u32 s78, s12, s5\n"
      "buffer_load_dwordx4 a[24:27], v1, s[44:47], s78 offen sc1\n"
      "s_sleep 1\n"
      "s_add_u32 s78, s12, s5\n"
      "buffer_load_dwordx4 a[36:39], v1, s[44:47], s78 offen offset:1024 sc1\n"
      "s_sleep 1\n"
      "s_add_u32 s78, s12, s5\n"
      "s_add_u32 s78, s78, 65536\n"
      "buffer_load_dwordx4 a[28:31], v1, s[44:47], s78 offen sc1\n"
      "s_sleep 1\n"
      "s_add_u32 s78, s12, s5\n"
      "s_add_u32 s78, s78, 65536\n"
      "buffer_load_dwordx4 a[40:43], v1, s[44:47], s78 offen offset:1024 sc1\n"
      "s_sleep 1\n"
      "s_waitcnt lgkmcnt(0)\n"
      "ds_write_b128 v49, a[192:195]\n"
      "ds_write_b128 v49, a[196:199] offset:1024\n"
      "ds_write_b128 v49, a[200:203] offset:2048\n"
      "ds_write_b128 v49, a[204:207] offset:3072\n"
      "ds_write_b128 v49, a[208:211] offset:4096\n"
      "ds_write_b128 v49, a[212:215] offset:5120\n"
      "ds_write_b128 v49, a[216:219] offset:6144\n"
      "ds_write_b128 v49, a[220:223] offset:7168\n"
      "ds_write_b128 v49, a[224:227] offset:8192\n"
      "ds_write_b128 v49, a[228:231] offset:9216\n"
      "ds_write_b128 v49, a[232:235] offset:10240\n"
      "ds_write_b128 v49, a[236:239] offset:11264\n"
      "ds_write_b128 v49, a[240:243] offset:12288\n"
      "ds_write_b128 v49, a[244:247] offset:13312\n"
      "ds_write_b128 v49, a[248:251] offset:14336\n"
      "ds_write_b128 v49, a[252:255] offset:15360\n"
      "s_mul_i32 s73, s65, 12288\n"
      "s_add_u32 s73, s73, 65536\n"
      "v_add_u32 v44, s73, v1\n"
      "s_mul_i32 s73, s66, 12288\n"
      "s_add_u32 s73, s73, 69632\n"
      "v_add_u32 v45, s73, v1\n"
      "s_mul_i32 s73, s63, 12288\n"
      "s_add_u32 s73, s73, 73728\n"
      "v_add_u32 v46, s73, v1\n"
      "s_mul_i32 s67, s62, 12288\n"
      "s_add_u32 s67, s67, 65536\n"
      "v_add_u32 v7, s67, v1\n"
      "v_and_b32 v20, 15, v6\n"
      "v_lshrrev_b32 v21, 4, v6\n"
      "v_lshlrev_b32 v108, 9, v21\n"
      "v_lshl_add_u32 v108, v20, 4, v108\n"
      "v_lshlrev_b32 v22, 7, v20\n"
      "v_lshl_add_u32 v22, v21, 3, v22\n"
      "v_bfe_u32 v23, v6, 1, 2\n"
      "s_lshl_b32 s73, s62, 11\n"
      "s_add_u32 s73, s73, 106496\n"
      "v_xor_b32 v20, 0, v23\n"
      "v_lshl_add_u32 v20, v20, 5, v22\n"
      "v_add_u32 v100, s67, v20\n"
      "v_add_u32 v104, s73, v20\n"
      "v_xor_b32 v20, 1, v23\n"
      "v_lshl_add_u32 v20, v20, 5, v22\n"
      "v_add_u32 v101, s67, v20\n"
      "v_add_u32 v105, s73, v20\n"
      "v_xor_b32 v20, 2, v23\n"
      "v_lshl_add_u32 v20, v20, 5, v22\n"
      "v_add_u32 v102, s67, v20\n"
      "v_add_u32 v106, s73, v20\n"
      "v_xor_b32 v20, 3, v23\n"
      "v_lshl_add_u32 v20, v20, 5, v22\n"
      "v_add_u32 v103, s67, v20\n"
      "v_add_u32 v107, s73, v20\n"
      "v_xor_b32 v20, s62, v23\n"
      "v_lshl_add_u32 v20, v20, 5, v22\n"
      "v_add_u32 v52, 67584, v20\n"
      "v_bfe_u32 v20, v6, 4, 1\n"
      "v_bfe_u32 v21, v6, 2, 2\n"
      "v_lshl_add_u32 v20, v20, 3, v21\n"
      "v_lshrrev_b32 v21, 1, v21\n"
      "v_lshrrev_b32 v22, 5, v6\n"
      "v_xor_b32 v21, v21, v22\n"
      "v_lshlrev_b32 v20, 7, v20\n"
      "v_lshl_add_u32 v20, v21, 5, v20\n"
      "v_and_b32 v21, 3, v6\n"
      "v_lshl_add_u32 v20, v21, 3, v20\n"
      "v_add_u32 v53, s67, v20\n"
      "v_xor_b32 v54, 64, v53\n"
      "v_lshlrev_b32 v20, 16, v22\n"
      "v_and_b32 v21, 31, v6\n"
      "v_lshl_add_u32 v55, v21, 4, v20\n"
      "v_mov_b32 v96, 0\n"
      "v_mov_b32 v98, 0\n"
      "v_mov_b32 v97, s60\n"
      "v_mov_b32 v99, s61\n"
      "s_lshl_b32 s74, s82, 11\n"
      "s_add_u32 s74, s74, 13631488\n"
      "s_lshl_b32 s75, s70, 8\n"
      "s_add_u32 s75, s75, s74\n"
      "s_waitcnt lgkmcnt(0)\n"
      "s_mov_b32 s96, 131072\n"
      "s_mov_b64 exec, 255\n"
      ".Lpoll_18_%=:\n"
      "buffer_load_dwordx2 v[24:25], v51, s[44:47], 0 offen sc1\n"
      "s_waitcnt vmcnt(0)\n"
      "v_cmp_ne_u64_e32 vcc, s[60:61], v[24:25]\n"
      "s_cbranch_vccz .Lpdone_19_%=\n"
      "s_sub_u32 s96, s96, 1\n"
      "s_cbranch_scc0 .Lpoll_18_%=\n"
      ".Lpdone_19_%=:\n"
      "s_mov_b64 exec, -1\n"
      "s_barrier\n"
      "buffer_load_dwordx4 v[128:131], v1, s[44:47], s81 offen sc1\n"
      "buffer_load_dwordx4 v[132:135], v1, s[44:47], s81 offen offset:1024 sc1\n"
      "buffer_load_dwordx4 v[136:139], v1, s[44:47], s81 offen offset:2048 sc1\n"
      "buffer_load_dwordx4 v[140:143], v1, s[44:47], s81 offen offset:3072 sc1\n"
      "buffer_load_dwordx4 v[144:147], v1, s[44:47], s79 offen sc1\n"
      "buffer_load_dwordx4 v[148:151], v1, s[44:47], s79 offen offset:1024 sc1\n"
      "buffer_load_dwordx4 v[152:155], v1, s[44:47], s79 offen offset:2048 sc1\n"
      "buffer_load_dwordx4 v[156:159], v1, s[44:47], s79 offen offset:3072 sc1\n"
      "buffer_load_dwordx4 v[160:163], v1, s[44:47], s84 offen sc1\n"
      "buffer_load_dwordx4 v[164:167], v1, s[44:47], s84 offen offset:1024 sc1\n"
      "ds_read_b64 v[112:113], v104\n"
      "ds_read_b64 v[114:115], v105\n"
      "ds_read_b64 v[116:117], v106\n"
      "ds_read_b64 v[118:119], v107\n"
      "s_waitcnt lgkmcnt(0)\n"
      "v_lshlrev_b32 v72, 16, v112\n"
      "v_and_b32 v73, 4294901760, v112\n"
      "v_lshlrev_b32 v74, 16, v113\n"
      "v_and_b32 v75, 4294901760, v113\n"
      "v_lshlrev_b32 v76, 16, v114\n"
      "v_and_b32 v77, 4294901760, v114\n"
      "v_lshlrev_b32 v78, 16, v115\n"
      "v_and_b32 v79, 4294901760, v115\n"
      "v_lshlrev_b32 v80, 16, v116\n"
      "v_and_b32 v81, 4294901760, v116\n"
      "v_lshlrev_b32 v82, 16, v117\n"
      "v_and_b32 v83, 4294901760, v117\n"
      "v_lshlrev_b32 v84, 16, v118\n"
      "v_and_b32 v85, 4294901760, v118\n"
      "v_lshlrev_b32 v86, 16, v119\n"
      "v_and_b32 v87, 4294901760, v119\n"
      "ds_read_b128 v[192:195], v109\n"
      "ds_read_b128 v[196:199], v109 offset:1024\n"
      "ds_read_b128 v[200:203], v109 offset:2048\n"
      "ds_read_b128 v[204:207], v109 offset:3072\n"
      "ds_read_b128 v[208:211], v109 offset:4096\n"
      "ds_read_b128 v[212:215], v109 offset:5120\n"
      "ds_read_b128 v[216:219], v109 offset:6144\n"
      "ds_read_b128 v[220:223], v109 offset:7168\n"
      "ds_read_b128 v[224:227], v109 offset:8192\n"
      "ds_read_b128 v[228:231], v109 offset:9216\n"
      "ds_read_b128 v[232:235], v109 offset:10240\n"
      "ds_read_b128 v[236:239], v109 offset:11264\n"
      "ds_read_b128 v[240:243], v109 offset:12288\n"
      "ds_read_b128 v[244:247], v109 offset:13312\n"
      "ds_read_b128 v[248:251], v109 offset:14336\n"
      "ds_read_b128 v[252:255], v109 offset:15360\n"
      "s_waitcnt vmcnt(9)\n"
      "v_mfma_f32_16x16x32_bf16 v[56:59], a[192:195], v[128:131], v[72:75]\n"
      "ds_read_b128 a[192:195], v110\n"
      "v_mfma_f32_16x16x32_bf16 v[60:63], a[196:199], v[128:131], v[76:79]\n"
      "ds_read_b128 a[196:199], v110 offset:1024\n"
      "v_mfma_f32_16x16x32_bf16 v[64:67], a[200:203], v[128:131], v[80:83]\n"
      "ds_read_b128 a[200:203], v110 offset:2048\n"
      "v_mfma_f32_16x16x32_bf16 v[68:71], a[204:207], v[128:131], v[84:87]\n"
      "ds_read_b128 a[204:207], v110 offset:3072\n"
      "buffer_load_dwordx4 v[168:171], v1, s[44:47], s84 offen offset:2048 sc1\n"
      "s_waitcnt vmcnt(9)\n"
      "v_mfma_f32_16x16x32_bf16 v[56:59], a[208:211], v[132:135], v[56:59]\n"
      "ds_read_b128 a[208:211], v110 offset:4096\n"
      "v_mfma_f32_16x16x32_bf16 v[60:63], a[212:215], v[132:135], v[60:63]\n"
      "ds_read_b128 a[212:215], v110 offset:5120\n"
      "v_mfma_f32_16x16x32_bf16 v[64:67], a[216:219], v[132:135], v[64:67]\n"
      "ds_read_b128 a[216:219], v110 offset:6144\n"
      "v_mfma_f32_16x16x32_bf16 v[68:71], a[220:223], v[132:135], v[68:71]\n"
      "ds_read_b128 a[220:223], v110 offset:7168\n"
      "buffer_load_dwordx4 v[172:175], v1, s[44:47], s84 offen offset:3072 sc1\n"
      "s_waitcnt vmcnt(9)\n"
      "v_mfma_f32_16x16x32_bf16 v[56:59], a[224:227], v[136:139], v[56:59]\n"
      "ds_read_b128 a[224:227], v110 offset:8192\n"
      "v_mfma_f32_16x16x32_bf16 v[60:63], a[228:231], v[136:139], v[60:63]\n"
      "ds_read_b128 a[228:231], v110 offset:9216\n"
      "v_mfma_f32_16x16x32_bf16 v[64:67], a[232:235], v[136:139], v[64:67]\n"
      "ds_read_b128 a[232:235], v110 offset:10240\n"
      "v_mfma_f32_16x16x32_bf16 v[68:71], a[236:239], v[136:139], v[68:71]\n"
      "ds_read_b128 a[236:239], v110 offset:11264\n"
      "buffer_load_dwordx4 v[176:179], v1, s[44:47], s83 offen sc1\n"
      "s_waitcnt vmcnt(9)\n"
      "v_mfma_f32_16x16x32_bf16 v[56:59], a[240:243], v[140:143], v[56:59]\n"
      "ds_read_b128 a[240:243], v110 offset:12288\n"
      "v_mfma_f32_16x16x32_bf16 v[60:63], a[244:247], v[140:143], v[60:63]\n"
      "ds_read_b128 a[244:247], v110 offset:13312\n"
      "v_mfma_f32_16x16x32_bf16 v[64:67], a[248:251], v[140:143], v[64:67]\n"
      "ds_read_b128 a[248:251], v110 offset:14336\n"
      "v_mfma_f32_16x16x32_bf16 v[68:71], a[252:255], v[140:143], v[68:71]\n"
      "ds_read_b128 a[252:255], v110 offset:15360\n"
      "buffer_load_dwordx4 v[180:183], v1, s[44:47], s83 offen offset:1024 sc1\n"
      "s_waitcnt vmcnt(9)\n"
      "s_waitcnt lgkmcnt(15)\n"
      "v_mfma_f32_16x16x32_bf16 v[56:59], v[192:195], v[144:147], v[56:59]\n"
      "ds_read_b128 v[192:195], v111\n"
      "v_mfma_f32_16x16x32_bf16 v[60:63], v[196:199], v[144:147], v[60:63]\n"
      "ds_read_b128 v[196:199], v111 offset:1024\n"
      "v_mfma_f32_16x16x32_bf16 v[64:67], v[200:203], v[144:147], v[64:67]\n"
      "ds_read_b128 v[200:203], v111 offset:2048\n"
      "v_mfma_f32_16x16x32_bf16 v[68:71], v[204:207], v[144:147], v[68:71]\n"
      "ds_read_b128 v[204:207], v111 offset:3072\n"
      "buffer_load_dwordx4 v[184:187], v1, s[44:47], s83 offen offset:2048 sc1\n"
      "s_waitcnt vmcnt(9)\n"
      "v_mfma_f32_16x16x32_bf16 v[56:59], v[208:211], v[148:151], v[56:59]\n"
      "ds_read_b128 v[208:211], v111 offset:4096\n"
      "v_mfma_f32_16x16x32_bf16 v[60:63], v[212:215], v[148:151], v[60:63]\n"
      "ds_read_b128 v[212:215], v111 offset:5120\n"
      "v_mfma_f32_16x16x32_bf16 v[64:67], v[216:219], v[148:151], v[64:67]\n"
      "ds_read_b128 v[216:219], v111 offset:6144\n"
      "v_mfma_f32_16x16x32_bf16 v[68:71], v[220:223], v[148:151], v[68:71]\n"
      "ds_read_b128 v[220:223], v111 offset:7168\n"
      "buffer_load_dwordx4 v[188:191], v1, s[44:47], s83 offen offset:3072 sc1\n"
      "s_waitcnt vmcnt(9)\n"
      "v_mfma_f32_16x16x32_bf16 v[56:59], v[224:227], v[152:155], v[56:59]\n"
      "ds_read_b128 v[224:227], v111 offset:8192\n"
      "v_mfma_f32_16x16x32_bf16 v[60:63], v[228:231], v[152:155], v[60:63]\n"
      "ds_read_b128 v[228:231], v111 offset:9216\n"
      "v_mfma_f32_16x16x32_bf16 v[64:67], v[232:235], v[152:155], v[64:67]\n"
      "ds_read_b128 v[232:235], v111 offset:10240\n"
      "v_mfma_f32_16x16x32_bf16 v[68:71], v[236:239], v[152:155], v[68:71]\n"
      "ds_read_b128 v[236:239], v111 offset:11264\n"
      "s_add_u32 s78, s12, s5\n"
      "s_add_u32 s78, s78, 131072\n"
      "buffer_load_dwordx4 a[32:35], v48, s[44:47], s78 offen sc1\n"
      "s_waitcnt vmcnt(9)\n"
      "v_mfma_f32_16x16x32_bf16 v[56:59], v[240:243], v[156:159], v[56:59]\n"
      "ds_read_b128 v[240:243], v111 offset:12288\n"
      "v_mfma_f32_16x16x32_bf16 v[60:63], v[244:247], v[156:159], v[60:63]\n"
      "ds_read_b128 v[244:247], v111 offset:13312\n"
      "v_mfma_f32_16x16x32_bf16 v[64:67], v[248:251], v[156:159], v[64:67]\n"
      "ds_read_b128 v[248:251], v111 offset:14336\n"
      "v_mfma_f32_16x16x32_bf16 v[68:71], v[252:255], v[156:159], v[68:71]\n"
      "ds_read_b128 v[252:255], v111 offset:15360\n"
      "s_add_u32 s78, s12, s5\n"
      "s_add_u32 s78, s78, 131072\n"
      "buffer_load_dwordx4 a[44:47], v48, s[44:47], s78 offen offset:1024 sc1\n"
      "s_waitcnt vmcnt(9)\n"
      "v_mfma_f32_16x16x32_bf16 v[56:59], a[192:195], v[160:163], v[56:59]\n"
      "s_waitcnt lgkmcnt(15)\n"
      "v_mfma_f32_16x16x32_bf16 v[60:63], a[196:199], v[160:163], v[60:63]\n"
      "v_mfma_f32_16x16x32_bf16 v[64:67], a[200:203], v[160:163], v[64:67]\n"
      "v_mfma_f32_16x16x32_bf16 v[68:71], a[204:207], v[160:163], v[68:71]\n"
      "s_add_u32 s78, s12, s6\n"
      "buffer_load_dwordx4 a[48:51], v1, s[44:47], s78 offen sc1\n"
      "s_waitcnt vmcnt(9)\n"
      "v_mfma_f32_16x16x32_bf16 v[56:59], a[208:211], v[164:167], v[56:59]\n"
      "v_mfma_f32_16x16x32_bf16 v[60:63], a[212:215], v[164:167], v[60:63]\n"
      "v_mfma_f32_16x16x32_bf16 v[64:67], a[216:219], v[164:167], v[64:67]\n"
      "v_mfma_f32_16x16x32_bf16 v[68:71], a[220:223], v[164:167], v[68:71]\n"
      "s_add_u32 s78, s12, s6\n"
      "buffer_load_dwordx4 a[60:63], v1, s[44:47], s78 offen offset:1024 sc1\n"
      "s_waitcnt vmcnt(9)\n"
      "v_mfma_f32_16x16x32_bf16 v[56:59], a[224:227], v[168:171], v[56:59]\n"
      "v_mfma_f32_16x16x32_bf16 v[60:63], a[228:231], v[168:171], v[60:63]\n"
      "v_mfma_f32_16x16x32_bf16 v[64:67], a[232:235], v[168:171], v[64:67]\n"
      "v_mfma_f32_16x16x32_bf16 v[68:71], a[236:239], v[168:171], v[68:71]\n"
      "s_add_u32 s78, s12, s6\n"
      "s_add_u32 s78, s78, 65536\n"
      "buffer_load_dwordx4 a[52:55], v1, s[44:47], s78 offen sc1\n"
      "s_waitcnt vmcnt(9)\n"
      "v_mfma_f32_16x16x32_bf16 v[56:59], a[240:243], v[172:175], v[56:59]\n"
      "v_mfma_f32_16x16x32_bf16 v[60:63], a[244:247], v[172:175], v[60:63]\n"
      "v_mfma_f32_16x16x32_bf16 v[64:67], a[248:251], v[172:175], v[64:67]\n"
      "v_mfma_f32_16x16x32_bf16 v[68:71], a[252:255], v[172:175], v[68:71]\n"
      "s_add_u32 s78, s12, s6\n"
      "s_add_u32 s78, s78, 65536\n"
      "buffer_load_dwordx4 a[64:67], v1, s[44:47], s78 offen offset:1024 sc1\n"
      "s_waitcnt vmcnt(9)\n"
      "v_mfma_f32_16x16x32_bf16 v[56:59], v[192:195], v[176:179], v[56:59]\n"
      "s_waitcnt lgkmcnt(14)\n"
      "v_mfma_f32_16x16x32_bf16 v[60:63], v[196:199], v[176:179], v[60:63]\n"
      "s_waitcnt lgkmcnt(13)\n"
      "v_mfma_f32_16x16x32_bf16 v[64:67], v[200:203], v[176:179], v[64:67]\n"
      "s_waitcnt lgkmcnt(12)\n"
      "v_mfma_f32_16x16x32_bf16 v[68:71], v[204:207], v[176:179], v[68:71]\n"
      "s_waitcnt vmcnt(8)\n"
      "s_waitcnt lgkmcnt(11)\n"
      "v_mfma_f32_16x16x32_bf16 v[56:59], v[208:211], v[180:183], v[56:59]\n"
      "s_waitcnt lgkmcnt(10)\n"
      "v_mfma_f32_16x16x32_bf16 v[60:63], v[212:215], v[180:183], v[60:63]\n"
      "s_waitcnt lgkmcnt(9)\n"
      "v_mfma_f32_16x16x32_bf16 v[64:67], v[216:219], v[180:183], v[64:67]\n"
      "s_waitcnt lgkmcnt(8)\n"
      "v_mfma_f32_16x16x32_bf16 v[68:71], v[220:223], v[180:183], v[68:71]\n"
      "s_waitcnt vmcnt(7)\n"
      "s_waitcnt lgkmcnt(7)\n"
      "v_mfma_f32_16x16x32_bf16 v[56:59], v[224:227], v[184:187], v[56:59]\n"
      "s_waitcnt lgkmcnt(6)\n"
      "v_mfma_f32_16x16x32_bf16 v[60:63], v[228:231], v[184:187], v[60:63]\n"
      "s_waitcnt lgkmcnt(5)\n"
      "v_mfma_f32_16x16x32_bf16 v[64:67], v[232:235], v[184:187], v[64:67]\n"
      "s_waitcnt lgkmcnt(4)\n"
      "v_mfma_f32_16x16x32_bf16 v[68:71], v[236:239], v[184:187], v[68:71]\n"
      "s_waitcnt vmcnt(6)\n"
      "s_waitcnt lgkmcnt(3)\n"
      "v_mfma_f32_16x16x32_bf16 v[56:59], v[240:243], v[188:191], v[56:59]\n"
      "s_waitcnt lgkmcnt(2)\n"
      "v_mfma_f32_16x16x32_bf16 v[60:63], v[244:247], v[188:191], v[60:63]\n"
      "s_waitcnt lgkmcnt(1)\n"
      "v_mfma_f32_16x16x32_bf16 v[64:67], v[248:251], v[188:191], v[64:67]\n"
      "s_waitcnt lgkmcnt(0)\n"
      "v_mfma_f32_16x16x32_bf16 v[68:71], v[252:255], v[188:191], v[68:71]\n"
      "s_nop 1\n"
      "v_cvt_pk_bf16_f32 v88, v56, v57\n"
      "v_cvt_pk_bf16_f32 v89, v58, v59\n"
      "v_cvt_pk_bf16_f32 v90, v60, v61\n"
      "v_cvt_pk_bf16_f32 v91, v62, v63\n"
      "v_cvt_pk_bf16_f32 v92, v64, v65\n"
      "v_cvt_pk_bf16_f32 v93, v66, v67\n"
      "v_cvt_pk_bf16_f32 v94, v68, v69\n"
      "v_cvt_pk_bf16_f32 v95, v70, v71\n"
      "v_dot2c_f32_bf16 v96, s86, v88\n"
      "v_dot2c_f32_bf16 v98, v88, v88\n"
      "v_dot2c_f32_bf16 v96, s86, v89\n"
      "v_dot2c_f32_bf16 v98, v89, v89\n"
      "v_dot2c_f32_bf16 v96, s86, v90\n"
      "v_dot2c_f32_bf16 v98, v90, v90\n"
      "v_dot2c_f32_bf16 v96, s86, v91\n"
      "v_dot2c_f32_bf16 v98, v91, v91\n"
      "v_dot2c_f32_bf16 v96, s86, v92\n"
      "v_dot2c_f32_bf16 v98, v92, v92\n"
      "v_dot2c_f32_bf16 v96, s86, v93\n"
      "v_dot2c_f32_bf16 v98, v93, v93\n"
      "v_dot2c_f32_bf16 v96, s86, v94\n"
      "v_dot2c_f32_bf16 v98, v94, v94\n"
      "v_dot2c_f32_bf16 v96, s86, v95\n"
      "v_dot2c_f32_bf16 v98, v95, v95\n"
      "s_nop 1\n"
      "v_mov_b32 v20, v96\n"
      "v_mov_b32 v21, v98\n"
      "s_nop 0\n"
      "v_permlane16_swap_b32 v96, v20\n"
      "v_permlane16_swap_b32 v98, v21\n"
      "v_add_f32 v96, v96, v20\n"
      "v_add_f32 v98, v98, v21\n"
      "v_mov_b32 v20, v96\n"
      "v_mov_b32 v21, v98\n"
      "s_nop 0\n"
      "v_permlane32_swap_b32 v96, v20\n"
      "v_permlane32_swap_b32 v98, v21\n"
      "v_add_f32 v96, v96, v20\n"
      "v_add_f32 v98, v98, v21\n"
      "s_mov_b64 exec, 65535\n"
      "buffer_store_dwordx4 v[96:99], v1, s[44:47], s75 offen sc1\n"
      "s_mov_b64 exec, -1\n"
      "s_add_u32 s78, s12, s6\n"
      "s_add_u32 s78, s78, 131072\n"
      "buffer_load_dwordx4 a[56:59], v48, s[44:47], s78 offen sc1\n"
      "s_sleep 1\n"
      "s_add_u32 s78, s12, s6\n"
      "s_add_u32 s78, s78, 131072\n"
      "buffer_load_dwordx4 a[68:71], v48, s[44:47], s78 offen offset:1024 sc1\n"
      "s_sleep 1\n"
      "s_add_u32 s78, s12, s7\n"
      "buffer_load_dwordx4 a[72:75], v1, s[44:47], s78 offen sc1\n"
      "s_sleep 1\n"
      "s_add_u32 s78, s12, s7\n"
      "buffer_load_dwordx4 a[84:87], v1, s[44:47], s78 offen offset:1024 sc1\n"
      "s_sleep 1\n"
      "s_add_u32 s78, s12, s7\n"
      "s_add_u32 s78, s78, 65536\n"
      "buffer_load_dwordx4 a[76:79], v1, s[44:47], s78 offen sc1\n"
      "s_sleep 1\n"
      "s_add_u32 s78, s12, s7\n"
      "s_add_u32 s78, s78, 65536\n"
      "buffer_load_dwordx4 a[88:91], v1, s[44:47], s78 offen offset:1024 sc1\n"
      "s_sleep 1\n"
      "s_add_u32 s78, s12, s7\n"
      "s_add_u32 s78, s78, 131072\n"
      "buffer_load_dwordx4 a[80:83], v48, s[44:47], s78 offen sc1\n"
      "s_sleep 1\n"
      "s_add_u32 s78, s12, s7\n"
      "s_add_u32 s78, s78, 131072\n"
      "buffer_load_dwordx4 a[92:95], v48, s[44:47], s78 offen offset:1024 sc1\n"
      "s_sleep 1\n"
      "s_add_u32 s78, s12, s8\n"
      "buffer_load_dwordx4 a[96:99], v1, s[44:47], s78 offen sc1\n"
      "s_sleep 1\n"
      "s_add_u32 s78, s12, s8\n"
      "buffer_load_dwordx4 a[108:111], v1, s[44:47], s78 offen offset:1024 sc1\n"
      "s_sleep 1\n"
      "s_add_u32 s78, s12, s8\n"
      "s_add_u32 s78, s78, 65536\n"
      "buffer_load_dwordx4 a[100:103], v1, s[44:47], s78 offen sc1\n"
      "s_sleep 1\n"
      "s_add_u32 s78, s12, s8\n"
      "s_add_u32 s78, s78, 65536\n"
      "buffer_load_dwordx4 a[112:115], v1, s[44:47], s78 offen offset:1024 sc1\n"
      "s_sleep 1\n"
      "s_add_u32 s78, s12, s8\n"
      "s_add_u32 s78, s78, 131072\n"
      "buffer_load_dwordx4 a[104:107], v48, s[44:47], s78 offen sc1\n"
      "s_sleep 1\n"
      "s_add_u32 s78, s12, s8\n"
      "s_add_u32 s78, s78, 131072\n"
      "buffer_load_dwordx4 a[116:119], v48, s[44:47], s78 offen offset:1024 sc1\n"
      "s_sleep 1\n"
      "s_add_u32 s78, s12, s9\n"
      "buffer_load_dwordx4 a[120:123], v1, s[44:47], s78 offen sc1\n"
      "s_sleep 1\n"
      "s_add_u32 s78, s12, s9\n"
      "buffer_load_dwordx4 a[132:135], v1, s[44:47], s78 offen offset:1024 sc1\n"
      "s_sleep 1\n"
      "ds_write_b64 v100, v[88:89] offset:2048\n"
      "ds_write_b64 v101, v[90:91] offset:2048\n"
      "ds_write_b64 v102, v[92:93] offset:2048\n"
      "ds_write_b64 v103, v[94:95] offset:2048\n"
      "v_lshlrev_b32 v240, 16, v88\n"
      "v_and_b32 v241, 4294901760, v88\n"
      "v_lshlrev_b32 v242, 16, v89\n"
      "v_and_b32 v243, 4294901760, v89\n"
      "v_lshlrev_b32 v244, 16, v90\n"
      "v_and_b32 v245, 4294901760, v90\n"
      "v_lshlrev_b32 v246, 16, v91\n"
      "v_and_b32 v247, 4294901760, v91\n"
      "v_lshlrev_b32 v248, 16, v92\n"
      "v_and_b32 v249, 4294901760, v92\n"
      "v_lshlrev_b32 v250, 16, v93\n"
      "v_and_b32 v251, 4294901760, v93\n"
      "v_lshlrev_b32 v252, 16, v94\n"
      "v_and_b32 v253, 4294901760, v94\n"
      "v_lshlrev_b32 v254, 16, v95\n"
      "v_and_b32 v255, 4294901760, v95\n"
      "s_lshl_b32 s73, s70, 18\n"
      "s_lshl_b32 s76, s82, 9\n"
      "s_add_u32 s73, s73, s76\n"
      "s_add_u32 s73, s73, 9437184\n"
      "s_add_u32 s75, s73, 131072\n"
      "s_add_u32 s80, s94, 262144\n"
      "s_sleep 2\n"
      "s_mov_b32 s96, 131072\n"
      ".Lspoll_20_%=:\n"
      "buffer_load_dwordx4 v[112:115], v108, s[44:47], s74 offen sc1\n"
      "buffer_load_dwordx4 v[116:119], v108, s[44:47], s74 offen offset:256 sc1\n"
      "s_waitcnt vmcnt(0)\n"
      "v_xor_b32 v20, s60, v113\n"
      "v_xor_b32 v21, s61, v115\n"
      "v_xor_b32 v22, s60, v117\n"
      "v_xor_b32 v23, s61, v119\n"
      "v_or3_b32 v20, v20, v21, v22\n"
      "v_or_b32 v20, v20, v23\n"
      "v_cmp_ne_u32_e32 vcc, 0, v20\n"
      "s_cbranch_vccz .Lsdone_21_%=\n"
      "s_sub_u32 s96, s96, 1\n"
      "s_cbranch_scc0 .Lspoll_20_%=\n"
      ".Lsdone_21_%=:\n"
      "v_add_f32 v112, v112, v116\n"
      "v_add_f32 v114, v114, v118\n"
      "v_mov_b32 v20, v112\n"
      "v_mov_b32 v21, v114\n"
      "s_nop 0\n"
      "v_permlane16_swap_b32 v112, v20\n"
      "v_permlane16_swap_b32 v114, v21\n"
      "v_add_f32 v112, v112, v20\n"
      "v_add_f32 v114, v114, v21\n"
      "v_mov_b32 v20, v112\n"
      "v_mov_b32 v21, v114\n"
      "s_nop 0\n"
      "v_permlane32_swap_b32 v112, v20\n"
      "v_permlane32_swap_b32 v114, v21\n"
      "v_add_f32 v112, v112, v20\n"
      "v_add_f32 v114, v114, v21\n"
      "v_mul_f32 v112, 3137339392, v112\n"
      "v_mul_f32 v114, 989855744, v114\n"
      "v_fma_f32 v114, -v112, v112, v114\n"
      "v_max_f32 v114, 0, v114\n"
      "v_add_f32 v20, 925353388, v114\n"
      "v_rsq_f32 v21, v20\n"
      "s_nop 0\n"
      "v_mul_f32 v20, v114, v21\n"
      "v_mul_f32 v20, v20, v21\n"
      "v_add_f32 v20, 925353388, v20\n"
      "v_rsq_f32 v20, v20\n"
      "s_nop 0\n"
      "v_mul_f32 v114, v20, v21\n"
      "v_mul_f32 v112, v112, v114\n"
      "v_pk_fma_f32 v[240:241], v[240:241], v[114:115], v[112:113] op_sel_hi:[1,0,0]\n"
      "v_pk_fma_f32 v[242:243], v[242:243], v[114:115], v[112:113] op_sel_hi:[1,0,0]\n"
      "v_cvt_pk_bf16_f32 v120, v240, v241\n"
      "v_cvt_pk_bf16_f32 v121, v242, v243\n"
      "ds_write_b64 v100, v[120:121]\n"
      "v_pk_fma_f32 v[244:245], v[244:245], v[114:115], v[112:113] op_sel_hi:[1,0,0]\n"
      "v_pk_fma_f32 v[246:247], v[246:247], v[114:115], v[112:113] op_sel_hi:[1,0,0]\n"
      "v_cvt_pk_bf16_f32 v122, v244, v245\n"
      "v_cvt_pk_bf16_f32 v123, v246, v247\n"
      "ds_write_b64 v101, v[122:123]\n"
      "v_pk_fma_f32 v[248:249], v[248:249], v[114:115], v[112:113] op_sel_hi:[1,0,0]\n"
      "v_pk_fma_f32 v[250:251], v[250:251], v[114:115], v[112:113] op_sel_hi:[1,0,0]\n"
      "v_cvt_pk_bf16_f32 v124, v248, v249\n"
      "v_cvt_pk_bf16_f32 v125, v250, v251\n"
      "ds_write_b64 v102, v[124:125]\n"
      "v_pk_fma_f32 v[252:253], v[252:253], v[114:115], v[112:113] op_sel_hi:[1,0,0]\n"
      "v_pk_fma_f32 v[254:255], v[254:255], v[114:115], v[112:113] op_sel_hi:[1,0,0]\n"
      "v_cvt_pk_bf16_f32 v126, v252, v253\n"
      "v_cvt_pk_bf16_f32 v127, v254, v255\n"
      "ds_write_b64 v103, v[126:127]\n"
      "s_waitcnt lgkmcnt(0)\n"
      "ds_read_b64_tr_b16 v[128:129], v53\n"
      "ds_read_b64_tr_b16 v[130:131], v54 offset:512\n"
      "ds_read_b64_tr_b16 v[132:133], v54\n"
      "ds_read_b64_tr_b16 v[134:135], v53 offset:512\n"
      "s_waitcnt lgkmcnt(2)\n"
      "buffer_store_dwordx4 v[128:131], v55, s[44:47], s73 offen sc1\n"
      "s_waitcnt lgkmcnt(0)\n"
      "buffer_store_dwordx4 v[132:135], v55, s[44:47], s75 offen sc1\n"
      "s_add_u32 s78, s12, s9\n"
      "s_add_u32 s78, s78, 65536\n"
      "buffer_load_dwordx4 a[124:127], v1, s[44:47], s78 offen sc1\n"
      "s_sleep 1\n"
      "s_add_u32 s78, s12, s9\n"
      "s_add_u32 s78, s78, 65536\n"
      "buffer_load_dwordx4 a[136:139], v1, s[44:47], s78 offen offset:1024 sc1\n"
      "s_sleep 1\n"
      "s_add_u32 s78, s12, s9\n"
      "s_add_u32 s78, s78, 131072\n"
      "buffer_load_dwordx4 a[128:131], v48, s[44:47], s78 offen sc1\n"
      "s_sleep 1\n"
      "s_add_u32 s78, s12, s9\n"
      "s_add_u32 s78, s78, 131072\n"
      "buffer_load_dwordx4 a[140:143], v48, s[44:47], s78 offen offset:1024 sc1\n"
      "s_sleep 1\n"
      "s_add_u32 s78, s12, s10\n"
      "buffer_load_dwordx4 a[144:147], v1, s[44:47], s78 offen sc1\n"
      "s_sleep 1\n"
      "s_add_u32 s78, s12, s10\n"
      "buffer_load_dwordx4 a[156:159], v1, s[44:47], s78 offen offset:1024 sc1\n"
      "s_sleep 1\n"
      "s_add_u32 s78, s12, s10\n"
      "s_add_u32 s78, s78, 65536\n"
      "buffer_load_dwordx4 a[148:151], v1, s[44:47], s78 offen sc1\n"
      "s_sleep 1\n"
      "s_add_u32 s78, s12, s10\n"
      "s_add_u32 s78, s78, 65536\n"
      "buffer_load_dwordx4 a[160:163], v1, s[44:47], s78 offen offset:1024 sc1\n"
      "s_sleep 1\n"
      "s_add_u32 s78, s12, s10\n"
      "s_add_u32 s78, s78, 131072\n"
      "buffer_load_dwordx4 a[152:155], v48, s[44:47], s78 offen sc1\n"
      "s_sleep 1\n"
      "s_add_u32 s78, s12, s10\n"
      "s_add_u32 s78, s78, 131072\n"
      "buffer_load_dwordx4 a[164:167], v48, s[44:47], s78 offen offset:1024 sc1\n"
      "s_sleep 1\n"
      "s_add_u32 s78, s12, s11\n"
      "buffer_load_dwordx4 a[168:171], v1, s[44:47], s78 offen sc1\n"
      "s_sleep 1\n"
      "s_add_u32 s78, s12, s11\n"
      "buffer_load_dwordx4 a[180:183], v1, s[44:47], s78 offen offset:1024 sc1\n"
      "s_sleep 1\n"
      "s_lshl_b32 s73, s62, 13\n"
      "s_lshl_b32 s76, s70, 7\n"
      "s_add_u32 s73, s73, s76\n"
      "v_bfe_u32 v20, v6, 2, 3\n"
      "v_lshlrev_b32 v20, 10, v20\n"
      "v_and_b32 v21, 3, v6\n"
      "v_lshl_add_u32 v20, v21, 5, v20\n"
      "v_add_u32 v47, s73, v20\n"
      "v_add_u32 v51, 262144, v47\n"
      "s_lshl_b32 s81, s70, 18\n"
      "s_lshl_b32 s76, s65, 16\n"
      "s_add_u32 s81, s81, s76\n"
      "s_lshl_b32 s76, s62, 14\n"
      "s_add_u32 s81, s81, s76\n"
      "s_add_u32 s81, s81, 9437184\n"
      "s_lshl_b32 s79, s70, 18\n"
      "s_lshl_b32 s76, s66, 16\n"
      "s_add_u32 s79, s79, s76\n"
      "s_lshl_b32 s76, s62, 14\n"
      "s_add_u32 s79, s79, s76\n"
      "s_add_u32 s79, s79, 9437184\n"
      "s_lshl_b32 s84, s70, 18\n"
      "s_lshl_b32 s76, s63, 16\n"
      "s_add_u32 s84, s84, s76\n"
      "s_lshl_b32 s76, s62, 14\n"
      "s_add_u32 s84, s84, s76\n"
      "s_add_u32 s84, s84, 9437184\n"
      "s_lshl_b32 s83, s70, 18\n"
      "s_lshl_b32 s76, s64, 16\n"
      "s_add_u32 s83, s83, s76\n"
      "s_lshl_b32 s76, s62, 14\n"
      "s_add_u32 s83, s83, s76\n"
      "s_add_u32 s83, s83, 9437184\n"
      "v_mov_b32 v248, 0xc0135761\n"
      "v_mov_b32 v250, 0xbdd2d3e8\n"
      "v_mov_b32 v252, 1.0\n"
      "s_mul_i32 s68, s70, 176128\n"
      "s_mul_i32 s76, s62, 44032\n"
      "s_add_u32 s68, s68, s76\n"
      "s_lshl_b32 s76, s71, 9\n"
      "s_add_u32 s68, s68, s76\n"
      "s_add_u32 s68, s68, 11534336\n"
      "v_lshlrev_b32 v116, 16, v34\n"
      "v_and_b32 v117, 4294901760, v34\n"
      "v_lshlrev_b32 v118, 16, v35\n"
      "v_and_b32 v119, 4294901760, v35\n"
      "v_lshlrev_b32 v120, 16, v36\n"
      "v_and_b32 v121, 4294901760, v36\n"
      "v_lshlrev_b32 v122, 16, v37\n"
      "v_and_b32 v123, 4294901760, v37\n"
      "v_lshlrev_b32 v124, 16, v38\n"
      "v_and_b32 v125, 4294901760, v38\n"
      "v_lshlrev_b32 v126, 16, v39\n"
      "v_and_b32 v127, 4294901760, v39\n"
      "s_waitcnt vmcnt(12)\n"
      "s_mov_b64 exec, 1\n"
      "buffer_store_dwordx2 v[4:5], v2, s[44:47], s80 offen sc1\n"
      "s_mov_b64 exec, -1\n"
      "s_barrier\n"
      "ds_read_b64 v[26:27], v52\n"
      "ds_read_b64 v[28:29], v52 offset:12288\n"
      "ds_read_b64 v[30:31], v52 offset:24576\n"
      "ds_read_b64 v[32:33], v52 offset:36864\n"
      "s_add_u32 s78, s12, s11\n"
      "s_add_u32 s78, s78, 65536\n"
      "buffer_load_dwordx4 a[172:175], v1, s[44:47], s78 offen sc1\n"
      "s_sleep 1\n"
      "s_add_u32 s78, s12, s11\n"
      "s_add_u32 s78, s78, 65536\n"
      "buffer_load_dwordx4 a[184:187], v1, s[44:47], s78 offen offset:1024 sc1\n"
      "s_sleep 1\n"
      "s_add_u32 s78, s12, s11\n"
      "s_add_u32 s78, s78, 131072\n"
      "buffer_load_dwordx4 a[176:179], v48, s[44:47], s78 offen sc1\n"
      "s_sleep 1\n"
      "s_add_u32 s78, s12, s11\n"
      "s_add_u32 s78, s78, 131072\n"
      "buffer_load_dwordx4 a[188:191], v48, s[44:47], s78 offen offset:1024 sc1\n"
      "s_sleep 1\n"
      "s_lshl_b32 s78, s16, 7\n"
      "s_add_u32 s78, s78, s93\n"
      "s_add_u32 m0, s92, 0\n"
      "s_nop 0\n"
      "buffer_load_dwordx4 v0, s[40:43], s78 offen lds\n"
      "s_sleep 1\n"
      "s_lshl_b32 s78, s16, 7\n"
      "s_add_u32 s78, s78, s93\n"
      "s_add_u32 s78, s78, 32768\n"
      "s_add_u32 m0, s92, 1024\n"
      "s_nop 0\n"
      "buffer_load_dwordx4 v0, s[40:43], s78 offen lds\n"
      "s_sleep 1\n"
      "s_lshl_b32 s78, s16, 7\n"
      "s_add_u32 s78, s78, s93\n"
      "s_add_u32 s78, s78, 65536\n"
      "s_add_u32 m0, s92, 2048\n"
      "s_nop 0\n"
      "buffer_load_dwordx4 v0, s[40:43], s78 offen lds\n"
      "s_sleep 1\n"
      "s_lshl_b32 s78, s16, 7\n"
      "s_add_u32 s78, s78, s93\n"
      "s_add_u32 s78, s78, 98304\n"
      "s_add_u32 m0, s92, 3072\n"
      "s_nop 0\n"
      "buffer_load_dwordx4 v0, s[40:43], s78 offen lds\n"
      "s_sleep 1\n"
      "s_lshl_b32 s78, s17, 7\n"
      "s_add_u32 s78, s78, s93\n"
      "s_add_u32 m0, s92, 4096\n"
      "s_nop 0\n"
      "buffer_load_dwordx4 v0, s[40:43], s78 offen lds\n"
      "s_sleep 1\n"
      "s_lshl_b32 s78, s17, 7\n"
      "s_add_u32 s78, s78, s93\n"
      "s_add_u32 s78, s78, 32768\n"
      "s_add_u32 m0, s92, 5120\n"
      "s_nop 0\n"
      "buffer_load_dwordx4 v0, s[40:43], s78 offen lds\n"
      "s_sleep 1\n"
      "s_lshl_b32 s78, s17, 7\n"
      "s_add_u32 s78, s78, s93\n"
      "s_add_u32 s78, s78, 65536\n"
      "s_add_u32 m0, s92, 6144\n"
      "s_nop 0\n"
      "buffer_load_dwordx4 v0, s[40:43], s78 offen lds\n"
      "s_sleep 1\n"
      "s_lshl_b32 s78, s17, 7\n"
      "s_add_u32 s78, s78, s93\n"
      "s_add_u32 s78, s78, 98304\n"
      "s_add_u32 m0, s92, 7168\n"
      "s_nop 0\n"
      "buffer_load_dwordx4 v0, s[40:43], s78 offen lds\n"
      "s_sleep 1\n"
      "s_lshl_b32 s78, s18, 7\n"
      "s_add_u32 s78, s78, s93\n"
      "s_add_u32 m0, s92, 8192\n"
      "s_nop 0\n"
      "buffer_load_dwordx4 v0, s[40:43], s78 offen lds\n"
      "s_sleep 1\n"
      "s_lshl_b32 s78, s18, 7\n"
      "s_add_u32 s78, s78, s93\n"
      "s_add_u32 s78, s78, 32768\n"
      "s_add_u32 m0, s92, 9216\n"
      "s_nop 0\n"
      "buffer_load_dwordx4 v0, s[40:43], s78 offen lds\n"
      "s_sleep 1\n"
      "s_lshl_b32 s78, s18, 7\n"
      "s_add_u32 s78, s78, s93\n"
      "s_add_u32 s78, s78, 65536\n"
      "s_add_u32 m0, s92, 10240\n"
      "s_nop 0\n"
      "buffer_load_dwordx4 v0, s[40:43], s78 offen lds\n"
      "s_sleep 1\n"
      "s_lshl_b32 s78, s18, 7\n"
      "s_add_u32 s78, s78, s93\n"
      "s_add_u32 s78, s78, 98304\n"
      "s_add_u32 m0, s92, 11264\n"
      "s_nop 0\n"
      "buffer_load_dwordx4 v0, s[40:43], s78 offen lds\n"
      "s_sleep 1\n"
      "s_lshl_b32 s78, s19, 7\n"
      "s_add_u32 s78, s78, s93\n"
      "s_add_u32 m0, s92, 12288\n"
      "s_nop 0\n"
      "buffer_load_dwordx4 v0, s[40:43], s78 offen lds\n"
      "s_sleep 1\n"
      "s_lshl_b32 s78, s19, 7\n"
      "s_add_u32 s78, s78, s93\n"
      "s_add_u32 s78, s78, 32768\n"
      "s_add_u32 m0, s92, 13312\n"
      "s_nop 0\n"
      "buffer_load_dwordx4 v0, s[40:43], s78 offen lds\n"
      "s_sleep 1\n"
      "s_lshl_b32 s78, s19, 7\n"
      "s_add_u32 s78, s78, s93\n"
      "s_add_u32 s78, s78, 65536\n"
      "s_add_u32 m0, s92, 14336\n"
      "s_nop 0\n"
      "buffer_load_dwordx4 v0, s[40:43], s78 offen lds\n"
      "s_lshl_b32 s78, s19, 7\n"
      "s_add_u32 s78, s78, s93\n"
      "s_add_u32 s78, s78, 98304\n"
      "s_add_u32 m0, s92, 15360\n"
      "s_nop 0\n"
      "buffer_load_dwordx4 v0, s[40:43], s78 offen lds\n"
      "s_waitcnt lgkmcnt(0)\n"
      "s_barrier\n"
      "s_sleep 4\n"
      "s_mov_b32 s96, 131072\n"
      "s_mov_b32 exec_hi, 0\n"
      ".Lpoll_22_%=:\n"
      "buffer_load_dwordx2 v[24:25], v51, s[44:47], 0 offen sc1\n"
      "s_waitcnt vmcnt(0)\n"
      "v_cmp_ne_u64_e32 vcc, s[60:61], v[24:25]\n"
      "s_cbranch_vccz .Lpdone_23_%=\n"
      "s_sub_u32 s96, s96, 1\n"
      "s_cbranch_scc0 .Lpoll_22_%=\n"
      ".Lpdone_23_%=:\n"
      "s_mov_b64 exec, -1\n"
      "s_add_u32 s76, s81, s4\n"
      "buffer_load_dwordx4 v[128:131], v1, s[44:47], s76 offen sc1\n"
      "buffer_load_dwordx4 v[132:135], v1, s[44:47], s76 offen offset:1024 sc1\n"
      "s_add_u32 s76, s81, s5\n"
      "buffer_load_dwordx4 v[136:139], v1, s[44:47], s76 offen sc1\n"
      "buffer_load_dwordx4 v[140:143], v1, s[44:47], s76 offen offset:1024 sc1\n"
      "s_add_u32 s76, s81, s6\n"
      "buffer_load_dwordx4 v[144:147], v1, s[44:47], s76 offen sc1\n"
      "buffer_load_dwordx4 v[148:151], v1, s[44:47], s76 offen offset:1024 sc1\n"
      "s_add_u32 s76, s81, s7\n"
      "buffer_load_dwordx4 v[152:155], v1, s[44:47], s76 offen sc1\n"
      "buffer_load_dwordx4 v[156:159], v1, s[44:47], s76 offen offset:1024 sc1\n"
      "s_add_u32 s76, s81, s8\n"
      "buffer_load_dwordx4 v[160:163], v1, s[44:47], s76 offen sc1\n"
      "buffer_load_dwordx4 v[164:167], v1, s[44:47], s76 offen offset:1024 sc1\n"
      "s_waitcnt vmcnt(9)\n"
      "v_mfma_f32_16x16x32_bf16 v[56:59], a[0:3], v[128:131], 0\n"
      "v_mfma_f32_16x16x32_bf16 v[60:63], a[4:7], v[128:131], 0\n"
      "v_mfma_f32_16x16x32_bf16 v[64:67], a[8:11], v[128:131], 0\n"
      "s_add_u32 s76, s81, s9\n"
      "buffer_load_dwordx4 v[128:131], v1, s[44:47], s76 offen sc1\n"
      "s_waitcnt vmcnt(9)\n"
      "v_mfma_f32_16x16x32_bf16 v[56:59], a[12:15], v[132:135], v[56:59]\n"
      "ds_read_b64_tr_b16 a[192:193], v8\n"
      "ds_read_b64_tr_b16 a[194:195], v8 offset:1024\n"
      "v_mfma_f32_16x16x32_bf16 v[60:63], a[16:19], v[132:135], v[60:63]\n"
      "v_mfma_f32_16x16x32_bf16 v[64:67], a[20:23], v[132:135], v[64:67]\n"
      "buffer_load_dwordx4 v[132:135], v1, s[44:47], s76 offen offset:1024 sc1\n"
      "s_waitcnt vmcnt(9)\n"
      "v_mfma_f32_16x16x32_bf16 v[56:59], a[24:27], v[136:139], v[56:59]\n"
      "ds_read_b64_tr_b16 a[196:197], v9\n"
      "ds_read_b64_tr_b16 a[198:199], v9 offset:1024\n"
      "v_mfma_f32_16x16x32_bf16 v[60:63], a[28:31], v[136:139], v[60:63]\n"
      "v_mfma_f32_16x16x32_bf16 v[64:67], a[32:35], v[136:139], v[64:67]\n"
      "s_add_u32 s76, s81, s10\n"
      "buffer_load_dwordx4 v[136:139], v1, s[44:47], s76 offen sc1\n"
      "s_waitcnt vmcnt(9)\n"
      "v_mfma_f32_16x16x32_bf16 v[56:59], a[36:39], v[140:143], v[56:59]\n"
      "ds_read_b64_tr_b16 a[200:201], v10\n"
      "ds_read_b64_tr_b16 a[202:203], v10 offset:1024\n"
      "v_mfma_f32_16x16x32_bf16 v[60:63], a[40:43], v[140:143], v[60:63]\n"
      "v_mfma_f32_16x16x32_bf16 v[64:67], a[44:47], v[140:143], v[64:67]\n"
      "buffer_load_dwordx4 v[140:143], v1, s[44:47], s76 offen offset:1024 sc1\n"
      "s_waitcnt vmcnt(9)\n"
      "v_mfma_f32_16x16x32_bf16 v[56:59], a[48:51], v[144:147], v[56:59]\n"
      "ds_read_b64_tr_b16 a[204:205], v11\n"
      "ds_read_b64_tr_b16 a[206:207], v11 offset:1024\n"
      "v_mfma_f32_16x16x32_bf16 v[60:63], a[52:55], v[144:147], v[60:63]\n"
      "v_mfma_f32_16x16x32_bf16 v[64:67], a[56:59], v[144:147], v[64:67]\n"
      "s_add_u32 s76, s81, s11\n"
      "buffer_load_dwordx4 v[144:147], v1, s[44:47], s76 offen sc1\n"
      "s_waitcnt vmcnt(9)\n"
      "v_mfma_f32_16x16x32_bf16 v[56:59], a[60:63], v[148:151], v[56:59]\n"
      "ds_read_b64_tr_b16 a[208:209], v8 offset:4096\n"
      "ds_read_b64_tr_b16 a[210:211], v8 offset:5120\n"
      "v_mfma_f32_16x16x32_bf16 v[60:63], a[64:67], v[148:151], v[60:63]\n"
      "v_mfma_f32_16x16x32_bf16 v[64:67], a[68:71], v[148:151], v[64:67]\n"
      "buffer_load_dwordx4 v[148:151], v1, s[44:47], s76 offen offset:1024 sc1\n"
      "s_waitcnt vmcnt(9)\n"
      "v_mfma_f32_16x16x32_bf16 v[56:59], a[72:75], v[152:155], v[56:59]\n"
      "ds_read_b64_tr_b16 a[212:213], v9 offset:4096\n"
      "ds_read_b64_tr_b16 a[214:215], v9 offset:5120\n"
      "v_mfma_f32_16x16x32_bf16 v[60:63], a[76:79], v[152:155], v[60:63]\n"
      "v_mfma_f32_16x16x32_bf16 v[64:67], a[80:83], v[152:155], v[64:67]\n"
      "s_add_u32 s76, s79, s4\n"
      "buffer_load_dwordx4 v[152:155], v1, s[44:47], s76 offen sc1\n"
      "s_waitcnt vmcnt(9)\n"
      "v_mfma_f32_16x16x32_bf16 v[56:59], a[84:87], v[156:159], v[56:59]\n"
      "ds_read_b64_tr_b16 a[216:217], v10 offset:4096\n"
      "ds_read_b64_tr_b16 a[218:219], v10 offset:5120\n"
      "v_mfma_f32_16x16x32_bf16 v[60:63], a[88:91], v[156:159], v[60:63]\n"
      "v_mfma_f32_16x16x32_bf16 v[64:67], a[92:95], v[156:159], v[64:67]\n"
      "buffer_load_dwordx4 v[156:159], v1, s[44:47], s76 offen offset:1024 sc1\n"
      "s_waitcnt vmcnt(9)\n"
      "v_mfma_f32_16x16x32_bf16 v[56:59], a[96:99], v[160:163], v[56:59]\n"
      "ds_read_b64_tr_b16 a[220:221], v11 offset:4096\n"
      "ds_read_b64_tr_b16 a[222:223], v11 offset:5120\n"
      "v_mfma_f32_16x16x32_bf16 v[60:63], a[100:103], v[160:163], v[60:63]\n"
      "v_mfma_f32_16x16x32_bf16 v[64:67], a[104:107], v[160:163], v[64:67]\n"
      "s_add_u32 s76, s79, s5\n"
      "buffer_load_dwordx4 v[160:163], v1, s[44:47], s76 offen sc1\n"
      "s_waitcnt vmcnt(9)\n"
      "v_mfma_f32_16x16x32_bf16 v[56:59], a[108:111], v[164:167], v[56:59]\n"
      "ds_read_b64_tr_b16 a[224:225], v8 offset:8192\n"
      "ds_read_b64_tr_b16 a[226:227], v8 offset:9216\n"
      "v_mfma_f32_16x16x32_bf16 v[60:63], a[112:115], v[164:167], v[60:63]\n"
      "v_mfma_f32_16x16x32_bf16 v[64:67], a[116:119], v[164:167], v[64:67]\n"
      "buffer_load_dwordx4 v[164:167], v1, s[44:47], s76 offen offset:1024 sc1\n"
      "s_waitcnt vmcnt(9)\n"
      "v_mfma_f32_16x16x32_bf16 v[56:59], a[120:123], v[128:131], v[56:59]\n"
      "ds_read_b64_tr_b16 a[228:229], v9 offset:8192\n"
      "ds_read_b64_tr_b16 a[230:231], v9 offset:9216\n"
      "v_mfma_f32_16x16x32_bf16 v[60:63], a[124:127], v[128:131], v[60:63]\n"
      "v_mfma_f32_16x16x32_bf16 v[64:67], a[128:131], v[128:131], v[64:67]\n"
      "s_add_u32 s76, s79, s6\n"
      "buffer_load_dwordx4 v[128:131], v1, s[44:47], s76 offen sc1\n"
      "s_waitcnt vmcnt(9)\n"
      "v_mfma_f32_16x16x32_bf16 v[56:59], a[132:135], v[132:135], v[56:59]\n"
      "ds_read_b64_tr_b16 a[232:233], v10 offset:8192\n"
      "ds_read_b64_tr_b16 a[234:235], v10 offset:9216\n"
      "v_mfma_f32_16x16x32_bf16 v[60:63], a[136:139], v[132:135], v[60:63]\n"
      "v_mfma_f32_16x16x32_bf16 v[64:67], a[140:143], v[132:135], v[64:67]\n"
      "buffer_load_dwordx4 v[132:135], v1, s[44:47], s76 offen offset:1024 sc1\n"
      "s_waitcnt vmcnt(9)\n"
      "v_mfma_f32_16x16x32_bf16 v[56:59], a[144:147], v[136:139], v[56:59]\n"
      "ds_read_b64_tr_b16 a[236:237], v11 offset:8192\n"
      "ds_read_b64_tr_b16 a[238:239], v11 offset:9216\n"
      "v_mfma_f32_16x16x32_bf16 v[60:63], a[148:151], v[136:139], v[60:63]\n"
      "v_mfma_f32_16x16x32_bf16 v[64:67], a[152:155], v[136:139], v[64:67]\n"
      "s_add_u32 s76, s79, s7\n"
      "buffer_load_dwordx4 v[136:139], v1, s[44:47], s76 offen sc1\n"
      "s_waitcnt vmcnt(9)\n"
      "v_mfma_f32_16x16x32_bf16 v[56:59], a[156:159], v[140:143], v[56:59]\n"
      "ds_read_b64_tr_b16 a[240:241], v8 offset:12288\n"
      "ds_read_b64_tr_b16 a[242:243], v8 offset:13312\n"
      "v_mfma_f32_16x16x32_bf16 v[60:63], a[160:163], v[140:143], v[60:63]\n"
      "v_mfma_f32_16x16x32_bf16 v[64:67], a[164:167], v[140:143], v[64:67]\n"
      "buffer_load_dwordx4 v[140:143], v1, s[44:47], s76 offen offset:1024 sc1\n"
      "s_waitcnt vmcnt(9)\n"
      "v_mfma_f32_16x16x32_bf16 v[56:59], a[168:171], v[144:147], v[56:59]\n"
      "ds_read_b64_tr_b16 a[244:245], v9 offset:12288\n"
      "ds_read_b64_tr_b16 a[246:247], v9 offset:13312\n"
      "v_mfma_f32_16x16x32_bf16 v[60:63], a[172:175], v[144:147], v[60:63]\n"
      "v_mfma_f32_16x16x32_bf16 v[64:67], a[176:179], v[144:147], v[64:67]\n"
      "s_add_u32 s76, s79, s8\n"
      "buffer_load_dwordx4 v[144:147], v1, s[44:47], s76 offen sc1\n"
      "s_waitcnt vmcnt(9)\n"
      "v_mfma_f32_16x16x32_bf16 v[56:59], a[180:183], v[148:151], v[56:59]\n"
      "ds_read_b64_tr_b16 a[248:249], v10 offset:12288\n"
      "ds_read_b64_tr_b16 a[250:251], v10 offset:13312\n"
      "v_mfma_f32_16x16x32_bf16 v[60:63], a[184:187], v[148:151], v[60:63]\n"
      "v_mfma_f32_16x16x32_bf16 v[64:67], a[188:191], v[148:151], v[64:67]\n"
      "buffer_load_dwordx4 v[148:151], v1, s[44:47], s76 offen offset:1024 sc1\n"
      "s_waitcnt vmcnt(9)\n"
      "v_mfma_f32_16x16x32_bf16 v[68:71], a[0:3], v[152:155], 0\n"
      "ds_read_b64_tr_b16 a[252:253], v11 offset:12288\n"
      "ds_read_b64_tr_b16 a[254:255], v11 offset:13312\n"
      "v_mfma_f32_16x16x32_bf16 v[72:75], a[4:7], v[152:155], 0\n"
      "v_mfma_f32_16x16x32_bf16 v[76:79], a[8:11], v[152:155], 0\n"
      "s_add_u32 s76, s79, s9\n"
      "buffer_load_dwordx4 v[152:155], v1, s[44:47], s76 offen sc1\n"
      "s_waitcnt vmcnt(9)\n"
      "v_mfma_f32_16x16x32_bf16 v[68:71], a[12:15], v[156:159], v[68:71]\n"
      "ds_write_b128 v44, v[56:59]\n"
      "v_mfma_f32_16x16x32_bf16 v[72:75], a[16:19], v[156:159], v[72:75]\n"
      "ds_write_b128 v44, v[60:63] offset:1024\n"
      "v_mfma_f32_16x16x32_bf16 v[76:79], a[20:23], v[156:159], v[76:79]\n"
      "ds_write_b128 v44, v[64:67] offset:2048\n"
      "buffer_load_dwordx4 v[156:159], v1, s[44:47], s76 offen offset:1024 sc1\n"
      "s_waitcnt vmcnt(9)\n"
      "v_mfma_f32_16x16x32_bf16 v[68:71], a[24:27], v[160:163], v[68:71]\n"
      "v_mfma_f32_16x16x32_bf16 v[72:75], a[28:31], v[160:163], v[72:75]\n"
      "v_mfma_f32_16x16x32_bf16 v[76:79], a[32:35], v[160:163], v[76:79]\n"
      "s_add_u32 s76, s79, s10\n"
      "buffer_load_dwordx4 v[160:163], v1, s[44:47], s76 offen sc1\n"
      "s_waitcnt vmcnt(9)\n"
      "v_mfma_f32_16x16x32_bf16 v[68:71], a[36:39], v[164:167], v[68:71]\n"
      "v_mfma_f32_16x16x32_bf16 v[72:75], a[40:43], v[164:167], v[72:75]\n"
      "v_mfma_f32_16x16x32_bf16 v[76:79], a[44:47], v[164:167], v[76:79]\n"
      "buffer_load_dwordx4 v[164:167], v1, s[44:47], s76 offen offset:1024 sc1\n"
      "s_waitcnt vmcnt(9)\n"
      "v_mfma_f32_16x16x32_bf16 v[68:71], a[48:51], v[128:131], v[68:71]\n"
      "v_mfma_f32_16x16x32_bf16 v[72:75], a[52:55], v[128:131], v[72:75]\n"
      "v_mfma_f32_16x16x32_bf16 v[76:79], a[56:59], v[128:131], v[76:79]\n"
      "s_add_u32 s76, s79, s11\n"
      "buffer_load_dwordx4 v[128:131], v1, s[44:47], s76 offen sc1\n"
      "s_waitcnt vmcnt(9)\n"
      "v_mfma_f32_16x16x32_bf16 v[68:71], a[60:63], v[132:135], v[68:71]\n"
      "v_mfma_f32_16x16x32_bf16 v[72:75], a[64:67], v[132:135], v[72:75]\n"
      "v_mfma_f32_16x16x32_bf16 v[76:79], a[68:71], v[132:135], v[76:79]\n"
      "buffer_load_dwordx4 v[132:135], v1, s[44:47], s76 offen offset:1024 sc1\n"
      "s_waitcnt vmcnt(9)\n"
      "v_mfma_f32_16x16x32_bf16 v[68:71], a[72:75], v[136:139], v[68:71]\n"
      "v_mfma_f32_16x16x32_bf16 v[72:75], a[76:79], v[136:139], v[72:75]\n"
      "v_mfma_f32_16x16x32_bf16 v[76:79], a[80:83], v[136:139], v[76:79]\n"
      "s_add_u32 s76, s84, s4\n"
      "buffer_load_dwordx4 v[136:139], v1, s[44:47], s76 offen sc1\n"
      "s_waitcnt vmcnt(9)\n"
      "v_mfma_f32_16x16x32_bf16 v[68:71], a[84:87], v[140:143], v[68:71]\n"
      "v_mfma_f32_16x16x32_bf16 v[72:75], a[88:91], v[140:143], v[72:75]\n"
      "v_mfma_f32_16x16x32_bf16 v[76:79], a[92:95], v[140:143], v[76:79]\n"
      "buffer_load_dwordx4 v[140:143], v1, s[44:47], s76 offen offset:1024 sc1\n"
      "s_waitcnt vmcnt(9)\n"
      "v_mfma_f32_16x16x32_bf16 v[68:71], a[96:99], v[144:147], v[68:71]\n"
      "v_mfma_f32_16x16x32_bf16 v[72:75], a[100:103], v[144:147], v[72:75]\n"
      "v_mfma_f32_16x16x32_bf16 v[76:79], a[104:107], v[144:147], v[76:79]\n"
      "s_add_u32 s76, s84, s5\n"
      "buffer_load_dwordx4 v[144:147], v1, s[44:47], s76 offen sc1\n"
      "s_waitcnt vmcnt(9)\n"
      "v_mfma_f32_16x16x32_bf16 v[68:71], a[108:111], v[148:151], v[68:71]\n"
      "v_mfma_f32_16x16x32_bf16 v[72:75], a[112:115], v[148:151], v[72:75]\n"
      "v_mfma_f32_16x16x32_bf16 v[76:79], a[116:119], v[148:151], v[76:79]\n"
      "buffer_load_dwordx4 v[148:151], v1, s[44:47], s76 offen offset:1024 sc1\n"
      "s_waitcnt vmcnt(9)\n"
      "v_mfma_f32_16x16x32_bf16 v[68:71], a[120:123], v[152:155], v[68:71]\n"
      "v_mfma_f32_16x16x32_bf16 v[72:75], a[124:127], v[152:155], v[72:75]\n"
      "v_mfma_f32_16x16x32_bf16 v[76:79], a[128:131], v[152:155], v[76:79]\n"
      "s_add_u32 s76, s84, s6\n"
      "buffer_load_dwordx4 v[152:155], v1, s[44:47], s76 offen sc1\n"
      "s_waitcnt vmcnt(9)\n"
      "v_mfma_f32_16x16x32_bf16 v[68:71], a[132:135], v[156:159], v[68:71]\n"
      "v_mfma_f32_16x16x32_bf16 v[72:75], a[136:139], v[156:159], v[72:75]\n"
      "v_mfma_f32_16x16x32_bf16 v[76:79], a[140:143], v[156:159], v[76:79]\n"
      "buffer_load_dwordx4 v[156:159], v1, s[44:47], s76 offen offset:1024 sc1\n"
      "s_waitcnt vmcnt(9)\n"
      "v_mfma_f32_16x16x32_bf16 v[68:71], a[144:147], v[160:163], v[68:71]\n"
      "v_mfma_f32_16x16x32_bf16 v[72:75], a[148:151], v[160:163], v[72:75]\n"
      "v_mfma_f32_16x16x32_bf16 v[76:79], a[152:155], v[160:163], v[76:79]\n"
      "s_add_u32 s76, s84, s7\n"
      "buffer_load_dwordx4 v[160:163], v1, s[44:47], s76 offen sc1\n"
      "s_waitcnt vmcnt(9)\n"
      "v_mfma_f32_16x16x32_bf16 v[68:71], a[156:159], v[164:167], v[68:71]\n"
      "v_mfma_f32_16x16x32_bf16 v[72:75], a[160:163], v[164:167], v[72:75]\n"
      "v_mfma_f32_16x16x32_bf16 v[76:79], a[164:167], v[164:167], v[76:79]\n"
      "buffer_load_dwordx4 v[164:167], v1, s[44:47], s76 offen offset:1024 sc1\n"
      "s_waitcnt vmcnt(9)\n"
      "v_mfma_f32_16x16x32_bf16 v[68:71], a[168:171], v[128:131], v[68:71]\n"
      "s_waitcnt lgkmcnt(3)\n"
      "v_mfma_f32_16x16x32_bf16 v[72:75], a[172:175], v[128:131], v[72:75]\n"
      "v_mfma_f32_16x16x32_bf16 v[76:79], a[176:179], v[128:131], v[76:79]\n"
      "s_add_u32 s76, s84, s8\n"
      "buffer_load_dwordx4 v[128:131], v1, s[44:47], s76 offen sc1\n"
      "s_waitcnt vmcnt(9)\n"
      "v_mfma_f32_16x16x32_bf16 v[68:71], a[180:183], v[132:135], v[68:71]\n"
      "v_mfma_f32_16x16x32_bf16 v[72:75], a[184:187], v[132:135], v[72:75]\n"
      "v_mfma_f32_16x16x32_bf16 v[76:79], a[188:191], v[132:135], v[76:79]\n"
      "buffer_load_dwordx4 v[132:135], v1, s[44:47], s76 offen offset:1024 sc1\n"
      "s_waitcnt vmcnt(9)\n"
      "v_mfma_f32_16x16x32_bf16 v[56:59], a[0:3], v[136:139], 0\n"
      "v_mfma_f32_16x16x32_bf16 v[60:63], a[4:7], v[136:139], 0\n"
      "v_mfma_f32_16x16x32_bf16 v[64:67], a[8:11], v[136:139], 0\n"
      "s_add_u32 s76, s84, s9\n"
      "buffer_load_dwordx4 v[136:139], v1, s[44:47], s76 offen sc1\n"
      "s_waitcnt vmcnt(9)\n"
      "v_mfma_f32_16x16x32_bf16 v[56:59], a[12:15], v[140:143], v[56:59]\n"
      "ds_write_b128 v45, v[68:71]\n"
      "v_mfma_f32_16x16x32_bf16 v[60:63], a[16:19], v[140:143], v[60:63]\n"
      "ds_write_b128 v45, v[72:75] offset:1024\n"
      "v_mfma_f32_16x16x32_bf16 v[64:67], a[20:23], v[140:143], v[64:67]\n"
      "ds_write_b128 v45, v[76:79] offset:2048\n"
      "buffer_load_dwordx4 v[140:143], v1, s[44:47], s76 offen offset:1024 sc1\n"
      "s_waitcnt vmcnt(9)\n"
      "v_mfma_f32_16x16x32_bf16 v[56:59], a[24:27], v[144:147], v[56:59]\n"
      "v_mfma_f32_16x16x32_bf16 v[60:63], a[28:31], v[144:147], v[60:63]\n"
      "v_mfma_f32_16x16x32_bf16 v[64:67], a[32:35], v[144:147], v[64:67]\n"
      "s_add_u32 s76, s84, s10\n"
      "buffer_load_dwordx4 v[144:147], v1, s[44:47], s76 offen sc1\n"
      "s_waitcnt vmcnt(9)\n"
      "v_mfma_f32_16x16x32_bf16 v[56:59], a[36:39], v[148:151], v[56:59]\n"
      "v_mfma_f32_16x16x32_bf16 v[60:63], a[40:43], v[148:151], v[60:63]\n"
      "v_mfma_f32_16x16x32_bf16 v[64:67], a[44:47], v[148:151], v[64:67]\n"
      "buffer_load_dwordx4 v[148:151], v1, s[44:47], s76 offen offset:1024 sc1\n"
      "s_waitcnt vmcnt(9)\n"
      "v_mfma_f32_16x16x32_bf16 v[56:59], a[48:51], v[152:155], v[56:59]\n"
      "v_mfma_f32_16x16x32_bf16 v[60:63], a[52:55], v[152:155], v[60:63]\n"
      "v_mfma_f32_16x16x32_bf16 v[64:67], a[56:59], v[152:155], v[64:67]\n"
      "s_add_u32 s76, s84, s11\n"
      "buffer_load_dwordx4 v[152:155], v1, s[44:47], s76 offen sc1\n"
      "s_waitcnt vmcnt(9)\n"
      "v_mfma_f32_16x16x32_bf16 v[56:59], a[60:63], v[156:159], v[56:59]\n"
      "v_mfma_f32_16x16x32_bf16 v[60:63], a[64:67], v[156:159], v[60:63]\n"
      "v_mfma_f32_16x16x32_bf16 v[64:67], a[68:71], v[156:159], v[64:67]\n"
      "buffer_load_dwordx4 v[156:159], v1, s[44:47], s76 offen offset:1024 sc1\n"
      "s_waitcnt vmcnt(9)\n"
      "v_mfma_f32_16x16x32_bf16 v[56:59], a[72:75], v[160:163], v[56:59]\n"
      "v_mfma_f32_16x16x32_bf16 v[60:63], a[76:79], v[160:163], v[60:63]\n"
      "v_mfma_f32_16x16x32_bf16 v[64:67], a[80:83], v[160:163], v[64:67]\n"
      "s_add_u32 s76, s83, s4\n"
      "buffer_load_dwordx4 v[160:163], v1, s[44:47], s76 offen sc1\n"
      "s_waitcnt vmcnt(9)\n"
      "v_mfma_f32_16x16x32_bf16 v[56:59], a[84:87], v[164:167], v[56:59]\n"
      "v_mfma_f32_16x16x32_bf16 v[60:63], a[88:91], v[164:167], v[60:63]\n"
      "v_mfma_f32_16x16x32_bf16 v[64:67], a[92:95], v[164:167], v[64:67]\n"
      "buffer_load_dwordx4 v[164:167], v1, s[44:47], s76 offen offset:1024 sc1\n"
      "s_waitcnt vmcnt(9)\n"
      "v_mfma_f32_16x16x32_bf16 v[56:59], a[96:99], v[128:131], v[56:59]\n"
      "v_mfma_f32_16x16x32_bf16 v[60:63], a[100:103], v[128:131], v[60:63]\n"
      "v_mfma_f32_16x16x32_bf16 v[64:67], a[104:107], v[128:131], v[64:67]\n"
      "s_add_u32 s76, s83, s5\n"
      "buffer_load_dwordx4 v[128:131], v1, s[44:47], s76 offen sc1\n"
      "s_waitcnt vmcnt(9)\n"
      "v_mfma_f32_16x16x32_bf16 v[56:59], a[108:111], v[132:135], v[56:59]\n"
      "v_mfma_f32_16x16x32_bf16 v[60:63], a[112:115], v[132:135], v[60:63]\n"
      "v_mfma_f32_16x16x32_bf16 v[64:67], a[116:119], v[132:135], v[64:67]\n"
      "buffer_load_dwordx4 v[132:135], v1, s[44:47], s76 offen offset:1024 sc1\n"
      "s_waitcnt vmcnt(9)\n"
      "v_mfma_f32_16x16x32_bf16 v[56:59], a[120:123], v[136:139], v[56:59]\n"
      "v_mfma_f32_16x16x32_bf16 v[60:63], a[124:127], v[136:139], v[60:63]\n"
      "v_mfma_f32_16x16x32_bf16 v[64:67], a[128:131], v[136:139], v[64:67]\n"
      "s_add_u32 s76, s83, s6\n"
      "buffer_load_dwordx4 v[136:139], v1, s[44:47], s76 offen sc1\n"
      "s_waitcnt vmcnt(9)\n"
      "v_mfma_f32_16x16x32_bf16 v[56:59], a[132:135], v[140:143], v[56:59]\n"
      "v_mfma_f32_16x16x32_bf16 v[60:63], a[136:139], v[140:143], v[60:63]\n"
      "v_mfma_f32_16x16x32_bf16 v[64:67], a[140:143], v[140:143], v[64:67]\n"
      "buffer_load_dwordx4 v[140:143], v1, s[44:47], s76 offen offset:1024 sc1\n"
      "s_waitcnt vmcnt(9)\n"
      "v_mfma_f32_16x16x32_bf16 v[56:59], a[144:147], v[144:147], v[56:59]\n"
      "v_mfma_f32_16x16x32_bf16 v[60:63], a[148:151], v[144:147], v[60:63]\n"
      "v_mfma_f32_16x16x32_bf16 v[64:67], a[152:155], v[144:147], v[64:67]\n"
      "s_add_u32 s76, s83, s7\n"
      "buffer_load_dwordx4 v[144:147], v1, s[44:47], s76 offen sc1\n"
      "s_waitcnt vmcnt(9)\n"
      "v_mfma_f32_16x16x32_bf16 v[56:59], a[156:159], v[148:151], v[56:59]\n"
      "v_mfma_f32_16x16x32_bf16 v[60:63], a[160:163], v[148:151], v[60:63]\n"
      "v_mfma_f32_16x16x32_bf16 v[64:67], a[164:167], v[148:151], v[64:67]\n"
      "buffer_load_dwordx4 v[148:151], v1, s[44:47], s76 offen offset:1024 sc1\n"
      "s_waitcnt vmcnt(9)\n"
      "v_mfma_f32_16x16x32_bf16 v[56:59], a[168:171], v[152:155], v[56:59]\n"
      "v_mfma_f32_16x16x32_bf16 v[60:63], a[172:175], v[152:155], v[60:63]\n"
      "v_mfma_f32_16x16x32_bf16 v[64:67], a[176:179], v[152:155], v[64:67]\n"
      "s_add_u32 s76, s83, s8\n"
      "buffer_load_dwordx4 v[152:155], v1, s[44:47], s76 offen sc1\n"
      "s_waitcnt vmcnt(9)\n"
      "v_mfma_f32_16x16x32_bf16 v[56:59], a[180:183], v[156:159], v[56:59]\n"
      "v_mfma_f32_16x16x32_bf16 v[60:63], a[184:187], v[156:159], v[60:63]\n"
      "v_mfma_f32_16x16x32_bf16 v[64:67], a[188:191], v[156:159], v[64:67]\n"
      "buffer_load_dwordx4 v[156:159], v1, s[44:47], s76 offen offset:1024 sc1\n"
      "s_waitcnt vmcnt(9)\n"
      "v_mfma_f32_16x16x32_bf16 v[68:71], a[0:3], v[160:163], 0\n"
      "v_mfma_f32_16x16x32_bf16 v[72:75], a[4:7], v[160:163], 0\n"
      "v_mfma_f32_16x16x32_bf16 v[76:79], a[8:11], v[160:163], 0\n"
      "s_add_u32 s76, s83, s9\n"
      "buffer_load_dwordx4 v[160:163], v1, s[44:47], s76 offen sc1\n"
      "s_waitcnt vmcnt(9)\n"
      "v_mfma_f32_16x16x32_bf16 v[68:71], a[12:15], v[164:167], v[68:71]\n"
      "ds_write_b128 v46, v[56:59]\n"
      "v_mfma_f32_16x16x32_bf16 v[72:75], a[16:19], v[164:167], v[72:75]\n"
      "ds_write_b128 v46, v[60:63] offset:1024\n"
      "v_mfma_f32_16x16x32_bf16 v[76:79], a[20:23], v[164:167], v[76:79]\n"
      "ds_write_b128 v46, v[64:67] offset:2048\n"
      "buffer_load_dwordx4 v[164:167], v1, s[44:47], s76 offen offset:1024 sc1\n"
      "s_waitcnt vmcnt(9)\n"
      "v_mfma_f32_16x16x32_bf16 v[68:71], a[24:27], v[128:131], v[68:71]\n"
      "v_mfma_f32_16x16x32_bf16 v[72:75], a[28:31], v[128:131], v[72:75]\n"
      "v_mfma_f32_16x16x32_bf16 v[76:79], a[32:35], v[128:131], v[76:79]\n"
      "s_add_u32 s76, s83, s10\n"
      "buffer_load_dwordx4 v[128:131], v1, s[44:47], s76 offen sc1\n"
      "s_waitcnt vmcnt(9)\n"
      "v_mfma_f32_16x16x32_bf16 v[68:71], a[36:39], v[132:135], v[68:71]\n"
      "v_mfma_f32_16x16x32_bf16 v[72:75], a[40:43], v[132:135], v[72:75]\n"
      "v_mfma_f32_16x16x32_bf16 v[76:79], a[44:47], v[132:135], v[76:79]\n"
      "buffer_load_dwordx4 v[132:135], v1, s[44:47], s76 offen offset:1024 sc1\n"
      "s_waitcnt lgkmcnt(0)\n"
      "s_barrier\n"
      "ds_read_b128 v[80:83], v7\n"
      "ds_read_b128 v[84:87], v7 offset:1024\n"
      "ds_read_b128 v[88:91], v7 offset:2048\n"
      "ds_read_b128 v[92:95], v7 offset:4096\n"
      "ds_read_b128 v[96:99], v7 offset:5120\n"
      "ds_read_b128 v[100:103], v7 offset:6144\n"
      "ds_read_b128 v[104:107], v7 offset:8192\n"
      "ds_read_b128 v[108:111], v7 offset:9216\n"
      "ds_read_b128 v[112:115], v7 offset:10240\n"
      "s_waitcnt vmcnt(9)\n"
      "v_mfma_f32_16x16x32_bf16 v[68:71], a[48:51], v[136:139], v[68:71]\n"
      "v_mfma_f32_16x16x32_bf16 v[72:75], a[52:55], v[136:139], v[72:75]\n"
      "v_mfma_f32_16x16x32_bf16 v[76:79], a[56:59], v[136:139], v[76:79]\n"
      "s_add_u32 s76, s83, s11\n"
      "buffer_load_dwordx4 v[136:139], v1, s[44:47], s76 offen sc1\n"
      "s_waitcnt vmcnt(9)\n"
      "v_mfma_f32_16x16x32_bf16 v[68:71], a[60:63], v[140:143], v[68:71]\n"
      "v_mfma_f32_16x16x32_bf16 v[72:75], a[64:67], v[140:143], v[72:75]\n"
      "v_mfma_f32_16x16x32_bf16 v[76:79], a[68:71], v[140:143], v[76:79]\n"
      "buffer_load_dwordx4 v[140:143], v1, s[44:47], s76 offen offset:1024 sc1\n"
      "s_waitcnt vmcnt(9)\n"
      "v_mfma_f32_16x16x32_bf16 v[68:71], a[72:75], v[144:147], v[68:71]\n"
      "v_mfma_f32_16x16x32_bf16 v[72:75], a[76:79], v[144:147], v[72:75]\n"
      "v_mfma_f32_16x16x32_bf16 v[76:79], a[80:83], v[144:147], v[76:79]\n"
      "s_lshl_b32 s78, s88, 7\n"
      "s_add_u32 s78, s78, s93\n"
      "s_add_u32 m0, s91, 0\n"
      "s_nop 0\n"
      "buffer_load_dwordx4 v0, s[40:43], s78 offen lds\n"
      "s_waitcnt vmcnt(9)\n"
      "v_mfma_f32_16x16x32_bf16 v[68:71], a[84:87], v[148:151], v[68:71]\n"
      "v_mfma_f32_16x16x32_bf16 v[72:75], a[88:91], v[148:151], v[72:75]\n"
      "v_mfma_f32_16x16x32_bf16 v[76:79], a[92:95], v[148:151], v[76:79]\n"
      "s_lshl_b32 s78, s88, 7\n"
      "s_add_u32 s78, s78, s93\n"
      "s_add_u32 s78, s78, 32768\n"
      "s_add_u32 m0, s91, 1024\n"
      "s_nop 0\n"
      "buffer_load_dwordx4 v0, s[40:43], s78 offen lds\n"
      "s_waitcnt vmcnt(9)\n"
      "v_mfma_f32_16x16x32_bf16 v[68:71], a[96:99], v[152:155], v[68:71]\n"
      "v_mfma_f32_16x16x32_bf16 v[72:75], a[100:103], v[152:155], v[72:75]\n"
      "v_mfma_f32_16x16x32_bf16 v[76:79], a[104:107], v[152:155], v[76:79]\n"
      "s_lshl_b32 s78, s88, 7\n"
      "s_add_u32 s78, s78, s93\n"
      "s_add_u32 s78, s78, 65536\n"
      "s_add_u32 m0, s91, 2048\n"
      "s_nop 0\n"
      "buffer_load_dwordx4 v0, s[40:43], s78 offen lds\n"
      "s_waitcnt vmcnt(9)\n"
      "v_mfma_f32_16x16x32_bf16 v[68:71], a[108:111], v[156:159], v[68:71]\n"
      "v_mfma_f32_16x16x32_bf16 v[72:75], a[112:115], v[156:159], v[72:75]\n"
      "v_mfma_f32_16x16x32_bf16 v[76:79], a[116:119], v[156:159], v[76:79]\n"
      "s_lshl_b32 s78, s88, 7\n"
      "s_add_u32 s78, s78, s93\n"
      "s_add_u32 s78, s78, 98304\n"
      "s_add_u32 m0, s91, 3072\n"
      "s_nop 0\n"
      "buffer_load_dwordx4 v0, s[40:43], s78 offen lds\n"
      "s_waitcnt vmcnt(9)\n"
      "v_mfma_f32_16x16x32_bf16 v[68:71], a[120:123], v[160:163], v[68:71]\n"
      "v_mfma_f32_16x16x32_bf16 v[72:75], a[124:127], v[160:163], v[72:75]\n"
      "v_mfma_f32_16x16x32_bf16 v[76:79], a[128:131], v[160:163], v[76:79]\n"
      "s_lshl_b32 s78, s87, 7\n"
      "s_add_u32 s78, s78, s93\n"
      "s_add_u32 m0, s91, 4096\n"
      "s_nop 0\n"
      "buffer_load_dwordx4 v0, s[40:43], s78 offen lds\n"
      "s_waitcnt vmcnt(9)\n"
      "v_mfma_f32_16x16x32_bf16 v[68:71], a[132:135], v[164:167], v[68:71]\n"
      "v_mfma_f32_16x16x32_bf16 v[72:75], a[136:139], v[164:167], v[72:75]\n"
      "v_mfma_f32_16x16x32_bf16 v[76:79], a[140:143], v[164:167], v[76:79]\n"
      "s_lshl_b32 s78, s87, 7\n"
      "s_add_u32 s78, s78, s93\n"
      "s_add_u32 s78, s78, 32768\n"
      "s_add_u32 m0, s91, 5120\n"
      "s_nop 0\n"
      "buffer_load_dwordx4 v0, s[40:43], s78 offen lds\n"
      "s_waitcnt vmcnt(9)\n"
      "v_mfma_f32_16x16x32_bf16 v[68:71], a[144:147], v[128:131], v[68:71]\n"
      "v_mfma_f32_16x16x32_bf16 v[72:75], a[148:151], v[128:131], v[72:75]\n"
      "v_mfma_f32_16x16x32_bf16 v[76:79], a[152:155], v[128:131], v[76:79]\n"
      "s_lshl_b32 s78, s87, 7\n"
      "s_add_u32 s78, s78, s93\n"
      "s_add_u32 s78, s78, 65536\n"
      "s_add_u32 m0, s91, 6144\n"
      "s_nop 0\n"
      "buffer_load_dwordx4 v0, s[40:43], s78 offen lds\n"
      "s_waitcnt vmcnt(9)\n"
      "v_mfma_f32_16x16x32_bf16 v[68:71], a[156:159], v[132:135], v[68:71]\n"
      "v_mfma_f32_16x16x32_bf16 v[72:75], a[160:163], v[132:135], v[72:75]\n"
      "v_mfma_f32_16x16x32_bf16 v[76:79], a[164:167], v[132:135], v[76:79]\n"
      "s_lshl_b32 s78, s87, 7\n"
      "s_add_u32 s78, s78, s93\n"
      "s_add_u32 s78, s78, 98304\n"
      "s_add_u32 m0, s91, 7168\n"
      "s_nop 0\n"
      "buffer_load_dwordx4 v0, s[40:43], s78 offen lds\n"
      "s_waitcnt vmcnt(9)\n"
      "v_mfma_f32_16x16x32_bf16 v[68:71], a[168:171], v[136:139], v[68:71]\n"
      "v_mfma_f32_16x16x32_bf16 v[72:75], a[172:175], v[136:139], v[72:75]\n"
      "v_mfma_f32_16x16x32_bf16 v[76:79], a[176:179], v[136:139], v[76:79]\n"
      "s_lshl_b32 s78, s13, 7\n"
      "s_add_u32 s78, s78, s93\n"
      "s_add_u32 m0, s91, 8192\n"
      "s_nop 0\n"
      "buffer_load_dwordx4 v0, s[40:43], s78 offen lds\n"
      "s_waitcnt vmcnt(9)\n"
      "v_mfma_f32_16x16x32_bf16 v[68:71], a[180:183], v[140:143], v[68:71]\n"
      "v_mfma_f32_16x16x32_bf16 v[72:75], a[184:187], v[140:143], v[72:75]\n"
      "v_mfma_f32_16x16x32_bf16 v[76:79], a[188:191], v[140:143], v[76:79]\n"
      "s_lshl_b32 s78, s13, 7\n"
      "s_add_u32 s78, s78, s93\n"
      "s_add_u32 s78, s78, 32768\n"
      "s_add_u32 m0, s91, 9216\n"
      "s_nop 0\n"
      "buffer_load_dwordx4 v0, s[40:43], s78 offen lds\n"
      "s_waitcnt lgkmcnt(8)\n"
      "v_pk_add_f32 v[116:117], v[116:117], v[80:81]\n"
      "v_pk_add_f32 v[118:119], v[118:119], v[82:83]\n"
      "s_waitcnt lgkmcnt(7)\n"
      "v_pk_add_f32 v[120:121], v[120:121], v[84:85]\n"
      "v_pk_add_f32 v[122:123], v[122:123], v[86:87]\n"
      "s_waitcnt lgkmcnt(6)\n"
      "v_pk_add_f32 v[124:125], v[124:125], v[88:89]\n"
      "v_pk_add_f32 v[126:127], v[126:127], v[90:91]\n"
      "s_waitcnt lgkmcnt(5)\n"
      "v_pk_add_f32 v[116:117], v[116:117], v[92:93]\n"
      "v_pk_add_f32 v[118:119], v[118:119], v[94:95]\n"
      "s_waitcnt lgkmcnt(4)\n"
      "v_pk_add_f32 v[120:121], v[120:121], v[96:97]\n"
      "v_pk_add_f32 v[122:123], v[122:123], v[98:99]\n"
      "s_waitcnt lgkmcnt(3)\n"
      "v_pk_add_f32 v[124:125], v[124:125], v[100:101]\n"
      "v_pk_add_f32 v[126:127], v[126:127], v[102:103]\n"
      "s_waitcnt lgkmcnt(2)\n"
      "v_pk_add_f32 v[116:117], v[116:117], v[104:105]\n"
      "v_pk_add_f32 v[118:119], v[118:119], v[106:107]\n"
      "s_waitcnt lgkmcnt(1)\n"
      "v_pk_add_f32 v[120:121], v[120:121], v[108:109]\n"
      "v_pk_add_f32 v[122:123], v[122:123], v[110:111]\n"
      "s_waitcnt lgkmcnt(0)\n"
      "v_pk_add_f32 v[124:125], v[124:125], v[112:113]\n"
      "v_pk_add_f32 v[126:127], v[126:127], v[114:115]\n"
      "v_pk_add_f32 v[116:117], v[116:117], v[68:69]\n"
      "v_pk_add_f32 v[118:119], v[118:119], v[70:71]\n"
      "v_pk_add_f32 v[120:121], v[120:121], v[72:73]\n"
      "v_pk_add_f32 v[122:123], v[122:123], v[74:75]\n"
      "v_pk_add_f32 v[124:125], v[124:125], v[76:77]\n"
      "v_pk_add_f32 v[126:127], v[126:127], v[78:79]\n"
      "v_pk_mul_f32 v[136:137], v[116:117], v[116:117]\n"
      "v_pk_mul_f32 v[138:139], v[118:119], v[118:119]\n"
      "v_pk_mul_f32 v[140:141], v[120:121], v[120:121]\n"
      "v_pk_mul_f32 v[142:143], v[122:123], v[122:123]\n"
      "v_pk_mul_f32 v[144:145], v[124:125], v[124:125]\n"
      "v_pk_mul_f32 v[146:147], v[126:127], v[126:127]\n"
      "v_pk_fma_f32 v[136:137], v[136:137], v[250:251], v[248:249] op_sel_hi:[1,0,0]\n"
      "v_pk_fma_f32 v[138:139], v[138:139], v[250:251], v[248:249] op_sel_hi:[1,0,0]\n"
      "v_pk_fma_f32 v[140:141], v[140:141], v[250:251], v[248:249] op_sel_hi:[1,0,0]\n"
      "v_pk_fma_f32 v[142:143], v[142:143], v[250:251], v[248:249] op_sel_hi:[1,0,0]\n"
      "v_pk_fma_f32 v[144:145], v[144:145], v[250:251], v[248:249] op_sel_hi:[1,0,0]\n"
      "v_pk_fma_f32 v[146:147], v[146:147], v[250:251], v[248:249] op_sel_hi:[1,0,0]\n"
      "v_pk_mul_f32 v[136:137], v[136:137], v[116:117]\n"
      "v_pk_mul_f32 v[138:139], v[138:139], v[118:119]\n"
      "v_pk_mul_f32 v[140:141], v[140:141], v[120:121]\n"
      "v_pk_mul_f32 v[142:143], v[142:143], v[122:123]\n"
      "v_pk_mul_f32 v[144:145], v[144:145], v[124:125]\n"
      "v_pk_mul_f32 v[146:147], v[146:147], v[126:127]\n"
      "v_exp_f32 v136, v136\n"
      "v_exp_f32 v137, v137\n"
      "v_exp_f32 v138, v138\n"
      "v_exp_f32 v139, v139\n"
      "v_exp_f32 v140, v140\n"
      "v_exp_f32 v141, v141\n"
      "v_exp_f32 v142, v142\n"
      "v_exp_f32 v143, v143\n"
      "v_exp_f32 v144, v144\n"
      "v_exp_f32 v145, v145\n"
      "v_exp_f32 v146, v146\n"
      "v_exp_f32 v147, v147\n"
      "v_pk_add_f32 v[136:137], v[136:137], v[252:253] op_sel_hi:[1,0]\n"
      "v_pk_add_f32 v[138:139], v[138:139], v[252:253] op_sel_hi:[1,0]\n"
      "v_pk_add_f32 v[140:141], v[140:141], v[252:253] op_sel_hi:[1,0]\n"
      "v_pk_add_f32 v[142:143], v[142:143], v[252:253] op_sel_hi:[1,0]\n"
      "v_pk_add_f32 v[144:145], v[144:145], v[252:253] op_sel_hi:[1,0]\n"
      "v_pk_add_f32 v[146:147], v[146:147], v[252:253] op_sel_hi:[1,0]\n"
      "v_rcp_f32 v136, v136\n"
      "v_rcp_f32 v137, v137\n"
      "v_rcp_f32 v138, v138\n"
      "v_rcp_f32 v139, v139\n"
      "v_rcp_f32 v140, v140\n"
      "v_rcp_f32 v141, v141\n"
      "v_rcp_f32 v142, v142\n"
      "v_rcp_f32 v143, v143\n"
      "v_rcp_f32 v144, v144\n"
      "v_rcp_f32 v145, v145\n"
      "v_rcp_f32 v146, v146\n"
      "v_rcp_f32 v147, v147\n"
      "v_pk_mul_f32 v[136:137], v[136:137], v[116:117]\n"
      "v_pk_mul_f32 v[138:139], v[138:139], v[118:119]\n"
      "v_pk_mul_f32 v[140:141], v[140:141], v[120:121]\n"
      "v_pk_mul_f32 v[142:143], v[142:143], v[122:123]\n"
      "v_pk_mul_f32 v[144:145], v[144:145], v[124:125]\n"
      "v_pk_mul_f32 v[146:147], v[146:147], v[126:127]\n"
      "v_cvt_pk_bf16_f32 v148, v136, v137\n"
      "v_cvt_pk_bf16_f32 v149, v138, v139\n"
      "v_cvt_pk_bf16_f32 v150, v140, v141\n"
      "v_cvt_pk_bf16_f32 v151, v142, v143\n"
      "v_cvt_pk_bf16_f32 v152, v144, v145\n"
      "v_cvt_pk_bf16_f32 v153, v146, v147\n"
      "v_mov_b32 v154, v152\n"
      "v_mov_b32 v155, v153\n"
      "v_permlane32_swap_b32 v148, v150\n"
      "v_permlane32_swap_b32 v149, v151\n"
      "buffer_store_dwordx4 v[148:151], v1, s[44:47], s68 offen sc1\n"
      "v_permlane32_swap_b32 v152, v154\n"
      "v_permlane32_swap_b32 v153, v155\n"
      "s_mov_b32 exec_hi, 0\n"
      "buffer_store_dwordx4 v[152:155], v48, s[44:47], s68 offen offset:1024 sc1\n"
      "s_mov_b64 exec, -1\n"
      "s_lshl_b32 s78, s13, 7\n"
      "s_add_u32 s78, s78, s93\n"
      "s_add_u32 s78, s78, 65536\n"
      "s_add_u32 m0, s91, 10240\n"
      "s_nop 0\n"
      "buffer_load_dwordx4 v0, s[40:43], s78 offen lds\n"
      "s_sleep 1\n"
      "s_lshl_b32 s78, s13, 7\n"
      "s_add_u32 s78, s78, s93\n"
      "s_add_u32 s78, s78, 98304\n"
      "s_add_u32 m0, s91, 11264\n"
      "s_nop 0\n"
      "buffer_load_dwordx4 v0, s[40:43], s78 offen lds\n"
      "s_sleep 1\n"
      "s_lshl_b32 s78, s95, 7\n"
      "s_add_u32 s78, s78, s93\n"
      "s_add_u32 m0, s92, 0\n"
      "s_nop 0\n"
      "buffer_load_dwordx4 v0, s[40:43], s78 offen lds\n"
      "s_sleep 1\n"
      "s_lshl_b32 s78, s95, 7\n"
      "s_add_u32 s78, s78, s93\n"
      "s_add_u32 s78, s78, 32768\n"
      "s_add_u32 m0, s92, 1024\n"
      "s_nop 0\n"
      "buffer_load_dwordx4 v0, s[40:43], s78 offen lds\n"
      "s_sleep 1\n"
      "s_lshl_b32 s78, s95, 7\n"
      "s_add_u32 s78, s78, s93\n"
      "s_add_u32 s78, s78, 65536\n"
      "s_add_u32 m0, s92, 2048\n"
      "s_nop 0\n"
      "buffer_load_dwordx4 v0, s[40:43], s78 offen lds\n"
      "s_sleep 1\n"
      "s_lshl_b32 s78, s95, 7\n"
      "s_add_u32 s78, s78, s93\n"
      "s_add_u32 s78, s78, 98304\n"
      "s_add_u32 m0, s92, 3072\n"
      "s_nop 0\n"
      "buffer_load_dwordx4 v0, s[40:43], s78 offen lds\n"
      "s_sleep 1\n"
      "s_add_u32 s73, s94, 393216\n"
      "v_add_u32 v51, 393216, v47\n"
      "s_mul_i32 s81, s70, 176128\n"
      "s_mul_i32 s76, s65, 44032\n"
      "s_add_u32 s81, s81, s76\n"
      "s_mul_i32 s76, s62, 11264\n"
      "s_add_u32 s81, s81, s76\n"
      "s_add_u32 s81, s81, 11534336\n"
      "s_mul_i32 s79, s70, 176128\n"
      "s_mul_i32 s76, s66, 44032\n"
      "s_add_u32 s79, s79, s76\n"
      "s_mul_i32 s76, s62, 11264\n"
      "s_add_u32 s79, s79, s76\n"
      "s_add_u32 s79, s79, 11534336\n"
      "s_mul_i32 s84, s70, 176128\n"
      "s_mul_i32 s76, s63, 44032\n"
      "s_add_u32 s84, s84, s76\n"
      "s_mul_i32 s76, s62, 11264\n"
      "s_add_u32 s84, s84, s76\n"
      "s_add_u32 s84, s84, 11534336\n"
      "s_mul_i32 s83, s70, 176128\n"
      "s_mul_i32 s76, s64, 44032\n"
      "s_add_u32 s83, s83, s76\n"
      "s_mul_i32 s76, s62, 11264\n"
      "s_add_u32 s83, s83, s76\n"
      "s_add_u32 s83, s83, 11534336\n"
      "s_waitcnt vmcnt(6)\n"
      "s_mov_b64 exec, 1\n"
      "buffer_store_dwordx2 v[4:5], v2, s[44:47], s73 offen sc1\n"
      "s_mov_b64 exec, -1\n"
      "s_lshl_b32 s78, s90, 7\n"
      "s_add_u32 s78, s78, s93\n"
      "s_add_u32 m0, s92, 4096\n"
      "s_nop 0\n"
      "buffer_load_dwordx4 v0, s[40:43], s78 offen lds\n"
      "s_sleep 1\n"
      "s_lshl_b32 s78, s90, 7\n"
      "s_add_u32 s78, s78, s93\n"
      "s_add_u32 s78, s78, 32768\n"
      "s_add_u32 m0, s92, 5120\n"
      "s_nop 0\n"
      "buffer_load_dwordx4 v0, s[40:43], s78 offen lds\n"
      "s_sleep 1\n"
      "s_lshl_b32 s78, s90, 7\n"
      "s_add_u32 s78, s78, s93\n"
      "s_add_u32 s78, s78, 65536\n"
      "s_add_u32 m0, s92, 6144\n"
      "s_nop 0\n"
      "buffer_load_dwordx4 v0, s[40:43], s78 offen lds\n"
      "s_sleep 1\n"
      "s_lshl_b32 s78, s90, 7\n"
      "s_add_u32 s78, s78, s93\n"
      "s_add_u32 s78, s78, 98304\n"
      "s_add_u32 m0, s92, 7168\n"
      "s_nop 0\n"
      "buffer_load_dwordx4 v0, s[40:43], s78 offen lds\n"
      "s_sleep 1\n"
      "s_lshl_b32 s78, s23, 7\n"
      "s_add_u32 s78, s78, s93\n"
      "s_add_u32 m0, s92, 8192\n"
      "s_nop 0\n"
      "buffer_load_dwordx4 v0, s[40:43], s78 offen lds\n"
      "s_sleep 1\n"
      "s_lshl_b32 s78, s23, 7\n"
      "s_add_u32 s78, s78, s93\n"
      "s_add_u32 s78, s78, 32768\n"
      "s_add_u32 m0, s92, 9216\n"
      "s_nop 0\n"
      "buffer_load_dwordx4 v0, s[40:43], s78 offen lds\n"
      "s_sleep 1\n"
      "s_lshl_b32 s78, s23, 7\n"
      "s_add_u32 s78, s78, s93\n"
      "s_add_u32 s78, s78, 65536\n"
      "s_add_u32 m0, s92, 10240\n"
      "s_nop 0\n"
      "buffer_load_dwordx4 v0, s[40:43], s78 offen lds\n"
      "s_sleep 1\n"
      "s_lshl_b32 s78, s23, 7\n"
      "s_add_u32 s78, s78, s93\n"
      "s_add_u32 s78, s78, 98304\n"
      "s_add_u32 m0, s92, 11264\n"
      "s_nop 0\n"
      "buffer_load_dwordx4 v0, s[40:43], s78 offen lds\n"
      "s_sleep 1\n"
      "s_lshl_b32 s78, s22, 7\n"
      "s_add_u32 s78, s78, s93\n"
      "s_add_u32 m0, s92, 12288\n"
      "s_nop 0\n"
      "buffer_load_dwordx4 v0, s[40:43], s78 offen lds\n"
      "s_sleep 1\n"
      "s_lshl_b32 s78, s22, 7\n"
      "s_add_u32 s78, s78, s93\n"
      "s_add_u32 s78, s78, 32768\n"
      "s_add_u32 m0, s92, 13312\n"
      "s_nop 0\n"
      "buffer_load_dwordx4 v0, s[40:43], s78 offen lds\n"
      "s_sleep 1\n"
      "s_lshl_b32 s78, s22, 7\n"
      "s_add_u32 s78, s78, s93\n"
      "s_add_u32 s78, s78, 65536\n"
      "s_add_u32 m0, s92, 14336\n"
      "s_nop 0\n"
      "buffer_load_dwordx4 v0, s[40:43], s78 offen lds\n"
      "s_sleep 1\n"
      "s_lshl_b32 s78, s22, 7\n"
      "s_add_u32 s78, s78, s93\n"
      "s_add_u32 s78, s78, 98304\n"
      "s_add_u32 m0, s92, 15360\n"
      "s_nop 0\n"
      "buffer_load_dwordx4 v0, s[40:43], s78 offen lds\n"
      "s_sleep 1\n"
      "ds_read_b64_tr_b16 a[0:1], v12\n"
      "ds_read_b64_tr_b16 a[2:3], v12 offset:1024\n"
      "ds_read_b64_tr_b16 a[4:5], v13\n"
      "ds_read_b64_tr_b16 a[6:7], v13 offset:1024\n"
      "ds_read_b64_tr_b16 a[8:9], v14\n"
      "ds_read_b64_tr_b16 a[10:11], v14 offset:1024\n"
      "ds_read_b64_tr_b16 a[12:13], v15\n"
      "ds_read_b64_tr_b16 a[14:15], v15 offset:1024\n"
      "ds_read_b64_tr_b16 a[16:17], v12 offset:4096\n"
      "ds_read_b64_tr_b16 a[18:19], v12 offset:5120\n"
      "ds_read_b64_tr_b16 a[20:21], v13 offset:4096\n"
      "ds_read_b64_tr_b16 a[22:23], v13 offset:5120\n"
      "ds_read_b64_tr_b16 a[24:25], v14 offset:4096\n"
      "ds_read_b64_tr_b16 a[26:27], v14 offset:5120\n"
      "ds_read_b64_tr_b16 a[28:29], v15 offset:4096\n"
      "ds_read_b64_tr_b16 a[30:31], v15 offset:5120\n"
      "s_or_b32 s73, %[wg], s62\n"
      "s_cmp_eq_u32 s73, 0\n"
      "s_cbranch_scc0 .Lnoepo_24_%=\n"
      "s_mov_b32 s73, 786432\n"
      "s_mov_b64 exec, 1\n"
      "buffer_store_dwordx2 v[4:5], v2, s[44:47], s73 offen sc1\n"
      "s_mov_b64 exec, -1\n"
      ".Lnoepo_24_%=:\n"
      "v_lshlrev_b32 v40, 16, v40\n"
      "v_lshlrev_b32 v104, 16, v26\n"
      "v_and_b32 v105, 4294901760, v26\n"
      "v_lshlrev_b32 v106, 16, v27\n"
      "v_and_b32 v107, 4294901760, v27\n"
      "v_add_f32 v104, v104, v40\n"
      "v_add_f32 v105, v105, v40\n"
      "v_add_f32 v106, v106, v40\n"
      "v_add_f32 v107, v107, v40\n"
      "v_lshlrev_b32 v41, 16, v41\n"
      "v_lshlrev_b32 v108, 16, v28\n"
      "v_and_b32 v109, 4294901760, v28\n"
      "v_lshlrev_b32 v110, 16, v29\n"
      "v_and_b32 v111, 4294901760, v29\n"
      "v_add_f32 v108, v108, v41\n"
      "v_add_f32 v109, v109, v41\n"
      "v_add_f32 v110, v110, v41\n"
      "v_add_f32 v111, v111, v41\n"
      "v_lshlrev_b32 v42, 16, v42\n"
      "v_lshlrev_b32 v112, 16, v30\n"
      "v_and_b32 v113, 4294901760, v30\n"
      "v_lshlrev_b32 v114, 16, v31\n"
      "v_and_b32 v115, 4294901760, v31\n"
      "v_add_f32 v112, v112, v42\n"
      "v_add_f32 v113, v113, v42\n"
      "v_add_f32 v114, v114, v42\n"
      "v_add_f32 v115, v115, v42\n"
      "v_lshlrev_b32 v43, 16, v43\n"
      "v_lshlrev_b32 v116, 16, v32\n"
      "v_and_b32 v117, 4294901760, v32\n"
      "v_lshlrev_b32 v118, 16, v33\n"
      "v_and_b32 v119, 4294901760, v33\n"
      "v_add_f32 v116, v116, v43\n"
      "v_add_f32 v117, v117, v43\n"
      "v_add_f32 v118, v118, v43\n"
      "v_add_f32 v119, v119, v43\n"
      "v_and_b32 v20, 31, v6\n"
      "v_lshlrev_b32 v20, 10, v20\n"
      "v_lshrrev_b32 v21, 5, v6\n"
      "v_lshl_add_u32 v50, v21, 4, v20\n"
      "v_and_b32 v20, 15, v6\n"
      "v_lshrrev_b32 v21, 5, v6\n"
      "s_lshl_b32 s73, s62, 1\n"
      "v_add_u32 v21, s73, v21\n"
      "v_and_b32 v22, 7, v6\n"
      "v_xor_b32 v21, v21, v22\n"
      "v_lshlrev_b32 v20, 7, v20\n"
      "v_lshl_add_u32 v20, v21, 4, v20\n"
      "v_bfe_u32 v21, v6, 4, 1\n"
      "v_lshl_add_u32 v52, v21, 3, v20\n"
      "v_lshrrev_b32 v20, 3, v6\n"
      "v_xor_b32 v21, v20, v22\n"
      "v_lshlrev_b32 v21, 4, v21\n"
      "v_lshl_add_u32 v21, v20, 7, v21\n"
      "s_lshl_b32 s73, s62, 11\n"
      "v_add_u32 v53, s73, v21\n"
      "v_lshlrev_b32 v22, 4, v22\n"
      "v_lshl_add_u32 v50, v20, 10, v22\n"
      "s_lshl_b32 s74, s82, 14\n"
      "s_lshl_b32 s76, s70, 7\n"
      "s_add_u32 s74, s74, s76\n"
      "s_add_u32 s75, s74, 8192\n"
      "s_waitcnt lgkmcnt(0)\n"
      "s_barrier\n"
      "s_mov_b32 s96, 131072\n"
      "s_mov_b32 exec_hi, 0\n"
      ".Lpoll_25_%=:\n"
      "buffer_load_dwordx2 v[24:25], v51, s[44:47], 0 offen sc1\n"
      "s_waitcnt vmcnt(0)\n"
      "v_cmp_ne_u64_e32 vcc, s[60:61], v[24:25]\n"
      "s_cbranch_vccz .Lpdone_26_%=\n"
      "s_sub_u32 s96, s96, 1\n"
      "s_cbranch_scc0 .Lpoll_25_%=\n"
      ".Lpdone_26_%=:\n"
      "s_mov_b64 exec, -1\n"
      "s_add_u32 s77, s81, s16\n"
      "buffer_load_dwordx4 v[128:131], v1, s[44:47], s77 offen sc1\n"
      "s_add_u32 s77, s81, s17\n"
      "buffer_load_dwordx4 v[132:135], v1, s[44:47], s77 offen sc1\n"
      "s_add_u32 s77, s81, s18\n"
      "buffer_load_dwordx4 v[136:139], v1, s[44:47], s77 offen sc1\n"
      "s_add_u32 s77, s81, s19\n"
      "buffer_load_dwordx4 v[140:143], v1, s[44:47], s77 offen sc1\n"
      "s_add_u32 s77, s81, s88\n"
      "buffer_load_dwordx4 v[144:147], v1, s[44:47], s77 offen sc1\n"
      "s_add_u32 s77, s81, s87\n"
      "buffer_load_dwordx4 v[148:151], v1, s[44:47], s77 offen sc1\n"
      "s_add_u32 s77, s81, s13\n"
      "buffer_load_dwordx4 v[152:155], v1, s[44:47], s77 offen sc1\n"
      "s_add_u32 s77, s81, s95\n"
      "buffer_load_dwordx4 v[156:159], v1, s[44:47], s77 offen sc1\n"
      "s_add_u32 s77, s81, s90\n"
      "buffer_load_dwordx4 v[160:163], v1, s[44:47], s77 offen sc1\n"
      "s_add_u32 s77, s81, s23\n"
      "buffer_load_dwordx4 v[164:167], v1, s[44:47], s77 offen sc1\n"
      "s_add_u32 s77, s81, s22\n"
      "buffer_load_dwordx4 v[168:171], v17, s[44:47], s77 offen sc1\n"
      "s_add_u32 s77, s79, s16\n"
      "buffer_load_dwordx4 v[172:175], v1, s[44:47], s77 offen sc1\n"
      "ds_read_b64_tr_b16 a[32:33], v12 offset:8192\n"
      "ds_read_b64_tr_b16 a[34:35], v12 offset:9216\n"
      "ds_read_b64_tr_b16 a[36:37], v13 offset:8192\n"
      "ds_read_b64_tr_b16 a[38:39], v13 offset:9216\n"
      "ds_read_b64_tr_b16 a[40:41], v14 offset:8192\n"
      "ds_read_b64_tr_b16 a[42:43], v14 offset:9216\n"
      "ds_read_b64_tr_b16 a[44:45], v15 offset:8192\n"
      "ds_read_b64_tr_b16 a[46:47], v15 offset:9216\n"
      "ds_read_b64_tr_b16 a[48:49], v8\n"
      "ds_read_b64_tr_b16 a[50:51], v8 offset:1024\n"
      "ds_read_b64_tr_b16 a[52:53], v9\n"
      "ds_read_b64_tr_b16 a[54:55], v9 offset:1024\n"
      "ds_read_b64_tr_b16 a[56:57], v10\n"
      "ds_read_b64_tr_b16 a[58:59], v10 offset:1024\n"
      "ds_read_b64_tr_b16 a[60:61], v11\n"
      "ds_read_b64_tr_b16 a[62:63], v11 offset:1024\n"
      "ds_read_b64_tr_b16 a[64:65], v8 offset:4096\n"
      "ds_read_b64_tr_b16 a[66:67], v8 offset:5120\n"
      "ds_read_b64_tr_b16 a[68:69], v9 offset:4096\n"
      "ds_read_b64_tr_b16 a[70:71], v9 offset:5120\n"
      "ds_read_b64_tr_b16 a[72:73], v10 offset:4096\n"
      "ds_read_b64_tr_b16 a[74:75], v10 offset:5120\n"
      "ds_read_b64_tr_b16 a[76:77], v11 offset:4096\n"
      "ds_read_b64_tr_b16 a[78:79], v11 offset:5120\n"
      "ds_read_b64_tr_b16 a[80:81], v8 offset:8192\n"
      "ds_read_b64_tr_b16 a[82:83], v8 offset:9216\n"
      "ds_read_b64_tr_b16 a[84:85], v9 offset:8192\n"
      "ds_read_b64_tr_b16 a[86:87], v9 offset:9216\n"
      "ds_read_b64_tr_b16 a[88:89], v10 offset:8192\n"
      "ds_read_b64_tr_b16 a[90:91], v10 offset:9216\n"
      "ds_read_b64_tr_b16 a[92:93], v11 offset:8192\n"
      "ds_read_b64_tr_b16 a[94:95], v11 offset:9216\n"
      "ds_read_b64_tr_b16 a[96:97], v8 offset:12288\n"
      "ds_read_b64_tr_b16 a[98:99], v8 offset:13312\n"
      "ds_read_b64_tr_b16 a[100:101], v9 offset:12288\n"
      "ds_read_b64_tr_b16 a[102:103], v9 offset:13312\n"
      "ds_read_b64_tr_b16 a[104:105], v10 offset:12288\n"
      "ds_read_b64_tr_b16 a[106:107], v10 offset:13312\n"
      "ds_read_b64_tr_b16 a[108:109], v11 offset:12288\n"
      "ds_read_b64_tr_b16 a[110:111], v11 offset:13312\n"
      "s_waitcnt vmcnt(11)\n"
      "v_mfma_f32_16x16x32_bf16 v[56:59], v[128:131], a[192:195], 0\n"
      "v_mfma_f32_16x16x32_bf16 v[60:63], v[128:131], a[196:199], 0\n"
      "v_mfma_f32_16x16x32_bf16 v[64:67], v[128:131], a[200:203], 0\n"
      "v_mfma_f32_16x16x32_bf16 v[68:71], v[128:131], a[204:207], 0\n"
      "s_add_u32 s77, s79, s17\n"
      "buffer_load_dwordx4 v[128:131], v1, s[44:47], s77 offen sc1\n"
      "s_waitcnt vmcnt(11)\n"
      "v_mfma_f32_16x16x32_bf16 v[56:59], v[132:135], a[208:211], v[56:59]\n"
      "v_mfma_f32_16x16x32_bf16 v[60:63], v[132:135], a[212:215], v[60:63]\n"
      "v_mfma_f32_16x16x32_bf16 v[64:67], v[132:135], a[216:219], v[64:67]\n"
      "v_mfma_f32_16x16x32_bf16 v[68:71], v[132:135], a[220:223], v[68:71]\n"
      "s_add_u32 s77, s79, s18\n"
      "buffer_load_dwordx4 v[132:135], v1, s[44:47], s77 offen sc1\n"
      "s_waitcnt vmcnt(11)\n"
      "v_mfma_f32_16x16x32_bf16 v[56:59], v[136:139], a[224:227], v[56:59]\n"
      "v_mfma_f32_16x16x32_bf16 v[60:63], v[136:139], a[228:231], v[60:63]\n"
      "v_mfma_f32_16x16x32_bf16 v[64:67], v[136:139], a[232:235], v[64:67]\n"
      "v_mfma_f32_16x16x32_bf16 v[68:71], v[136:139], a[236:239], v[68:71]\n"
      "s_add_u32 s77, s79, s19\n"
      "buffer_load_dwordx4 v[136:139], v1, s[44:47], s77 offen sc1\n"
      "s_waitcnt vmcnt(11)\n"
      "v_mfma_f32_16x16x32_bf16 v[56:59], v[140:143], a[240:243], v[56:59]\n"
      "v_mfma_f32_16x16x32_bf16 v[60:63], v[140:143], a[244:247], v[60:63]\n"
      "v_mfma_f32_16x16x32_bf16 v[64:67], v[140:143], a[248:251], v[64:67]\n"
      "v_mfma_f32_16x16x32_bf16 v[68:71], v[140:143], a[252:255], v[68:71]\n"
      "s_add_u32 s77, s79, s88\n"
      "buffer_load_dwordx4 v[140:143], v1, s[44:47], s77 offen sc1\n"
      "s_waitcnt vmcnt(11)\n"
      "v_mfma_f32_16x16x32_bf16 v[56:59], v[144:147], a[0:3], v[56:59]\n"
      "v_mfma_f32_16x16x32_bf16 v[60:63], v[144:147], a[4:7], v[60:63]\n"
      "v_mfma_f32_16x16x32_bf16 v[64:67], v[144:147], a[8:11], v[64:67]\n"
      "v_mfma_f32_16x16x32_bf16 v[68:71], v[144:147], a[12:15], v[68:71]\n"
      "s_add_u32 s77, s79, s87\n"
      "buffer_load_dwordx4 v[144:147], v1, s[44:47], s77 offen sc1\n"
      "s_waitcnt vmcnt(11)\n"
      "v_mfma_f32_16x16x32_bf16 v[56:59], v[148:151], a[16:19], v[56:59]\n"
      "v_mfma_f32_16x16x32_bf16 v[60:63], v[148:151], a[20:23], v[60:63]\n"
      "v_mfma_f32_16x16x32_bf16 v[64:67], v[148:151], a[24:27], v[64:67]\n"
      "v_mfma_f32_16x16x32_bf16 v[68:71], v[148:151], a[28:31], v[68:71]\n"
      "s_add_u32 s77, s79, s13\n"
      "buffer_load_dwordx4 v[148:151], v1, s[44:47], s77 offen sc1\n"
      "s_waitcnt vmcnt(11)\n"
      "s_waitcnt lgkmcnt(15)\n"
      "v_mfma_f32_16x16x32_bf16 v[56:59], v[152:155], a[32:35], v[56:59]\n"
      "v_mfma_f32_16x16x32_bf16 v[60:63], v[152:155], a[36:39], v[60:63]\n"
      "v_mfma_f32_16x16x32_bf16 v[64:67], v[152:155], a[40:43], v[64:67]\n"
      "v_mfma_f32_16x16x32_bf16 v[68:71], v[152:155], a[44:47], v[68:71]\n"
      "s_add_u32 s77, s79, s95\n"
      "buffer_load_dwordx4 v[152:155], v1, s[44:47], s77 offen sc1\n"
      "s_waitcnt vmcnt(11)\n"
      "v_mfma_f32_16x16x32_bf16 v[56:59], v[156:159], a[48:51], v[56:59]\n"
      "v_mfma_f32_16x16x32_bf16 v[60:63], v[156:159], a[52:55], v[60:63]\n"
      "v_mfma_f32_16x16x32_bf16 v[64:67], v[156:159], a[56:59], v[64:67]\n"
      "v_mfma_f32_16x16x32_bf16 v[68:71], v[156:159], a[60:63], v[68:71]\n"
      "s_add_u32 s77, s79, s90\n"
      "buffer_load_dwordx4 v[156:159], v1, s[44:47], s77 offen sc1\n"
      "s_waitcnt vmcnt(11)\n"
      "v_mfma_f32_16x16x32_bf16 v[56:59], v[160:163], a[64:67], v[56:59]\n"
      "v_mfma_f32_16x16x32_bf16 v[60:63], v[160:163], a[68:71], v[60:63]\n"
      "v_mfma_f32_16x16x32_bf16 v[64:67], v[160:163], a[72:75], v[64:67]\n"
      "v_mfma_f32_16x16x32_bf16 v[68:71], v[160:163], a[76:79], v[68:71]\n"
      "s_add_u32 s77, s79, s23\n"
      "buffer_load_dwordx4 v[160:163], v1, s[44:47], s77 offen sc1\n"
      "s_waitcnt vmcnt(11)\n"
      "s_waitcnt lgkmcnt(8)\n"
      "v_mfma_f32_16x16x32_bf16 v[56:59], v[164:167], a[80:83], v[56:59]\n"
      "v_mfma_f32_16x16x32_bf16 v[60:63], v[164:167], a[84:87], v[60:63]\n"
      "v_mfma_f32_16x16x32_bf16 v[64:67], v[164:167], a[88:91], v[64:67]\n"
      "v_mfma_f32_16x16x32_bf16 v[68:71], v[164:167], a[92:95], v[68:71]\n"
      "s_add_u32 s77, s79, s22\n"
      "buffer_load_dwordx4 v[164:167], v17, s[44:47], s77 offen sc1\n"
      "s_waitcnt vmcnt(11)\n"
      "s_waitcnt lgkmcnt(0)\n"
      "v_mfma_f32_16x16x32_bf16 v[56:59], v[168:171], a[96:99], v[56:59]\n"
      "v_mfma_f32_16x16x32_bf16 v[60:63], v[168:171], a[100:103], v[60:63]\n"
      "v_mfma_f32_16x16x32_bf16 v[64:67], v[168:171], a[104:107], v[64:67]\n"
      "v_mfma_f32_16x16x32_bf16 v[68:71], v[168:171], a[108:111], v[68:71]\n"
      "s_add_u32 s77, s84, s16\n"
      "buffer_load_dwordx4 v[168:171], v1, s[44:47], s77 offen sc1\n"
      "s_waitcnt vmcnt(11)\n"
      "v_mfma_f32_16x16x32_bf16 v[72:75], v[172:175], a[192:195], 0\n"
      "v_mfma_f32_16x16x32_bf16 v[76:79], v[172:175], a[196:199], 0\n"
      "v_mfma_f32_16x16x32_bf16 v[80:83], v[172:175], a[200:203], 0\n"
      "v_mfma_f32_16x16x32_bf16 v[84:87], v[172:175], a[204:207], 0\n"
      "s_add_u32 s77, s84, s17\n"
      "buffer_load_dwordx4 v[172:175], v1, s[44:47], s77 offen sc1\n"
      "s_waitcnt vmcnt(11)\n"
      "v_mfma_f32_16x16x32_bf16 v[72:75], v[128:131], a[208:211], v[72:75]\n"
      "ds_write_b128 v44, v[56:59]\n"
      "v_mfma_f32_16x16x32_bf16 v[76:79], v[128:131], a[212:215], v[76:79]\n"
      "ds_write_b128 v44, v[60:63] offset:1024\n"
      "v_mfma_f32_16x16x32_bf16 v[80:83], v[128:131], a[216:219], v[80:83]\n"
      "ds_write_b128 v44, v[64:67] offset:2048\n"
      "v_mfma_f32_16x16x32_bf16 v[84:87], v[128:131], a[220:223], v[84:87]\n"
      "ds_write_b128 v44, v[68:71] offset:3072\n"
      "s_add_u32 s77, s84, s18\n"
      "buffer_load_dwordx4 v[128:131], v1, s[44:47], s77 offen sc1\n"
      "s_waitcnt vmcnt(11)\n"
      "v_mfma_f32_16x16x32_bf16 v[72:75], v[132:135], a[224:227], v[72:75]\n"
      "v_mfma_f32_16x16x32_bf16 v[76:79], v[132:135], a[228:231], v[76:79]\n"
      "v_mfma_f32_16x16x32_bf16 v[80:83], v[132:135], a[232:235], v[80:83]\n"
      "v_mfma_f32_16x16x32_bf16 v[84:87], v[132:135], a[236:239], v[84:87]\n"
      "s_add_u32 s77, s84, s19\n"
      "buffer_load_dwordx4 v[132:135], v1, s[44:47], s77 offen sc1\n"
      "s_waitcnt vmcnt(11)\n"
      "v_mfma_f32_16x16x32_bf16 v[72:75], v[136:139], a[240:243], v[72:75]\n"
      "v_mfma_f32_16x16x32_bf16 v[76:79], v[136:139], a[244:247], v[76:79]\n"
      "v_mfma_f32_16x16x32_bf16 v[80:83], v[136:139], a[248:251], v[80:83]\n"
      "v_mfma_f32_16x16x32_bf16 v[84:87], v[136:139], a[252:255], v[84:87]\n"
      "s_add_u32 s77, s84, s88\n"
      "buffer_load_dwordx4 v[136:139], v1, s[44:47], s77 offen sc1\n"
      "s_waitcnt vmcnt(11)\n"
      "v_mfma_f32_16x16x32_bf16 v[72:75], v[140:143], a[0:3], v[72:75]\n"
      "v_mfma_f32_16x16x32_bf16 v[76:79], v[140:143], a[4:7], v[76:79]\n"
      "v_mfma_f32_16x16x32_bf16 v[80:83], v[140:143], a[8:11], v[80:83]\n"
      "v_mfma_f32_16x16x32_bf16 v[84:87], v[140:143], a[12:15], v[84:87]\n"
      "s_add_u32 s77, s84, s87\n"
      "buffer_load_dwordx4 v[140:143], v1, s[44:47], s77 offen sc1\n"
      "s_waitcnt vmcnt(11)\n"
      "v_mfma_f32_16x16x32_bf16 v[72:75], v[144:147], a[16:19], v[72:75]\n"
      "v_mfma_f32_16x16x32_bf16 v[76:79], v[144:147], a[20:23], v[76:79]\n"
      "v_mfma_f32_16x16x32_bf16 v[80:83], v[144:147], a[24:27], v[80:83]\n"
      "v_mfma_f32_16x16x32_bf16 v[84:87], v[144:147], a[28:31], v[84:87]\n"
      "s_add_u32 s77, s84, s13\n"
      "buffer_load_dwordx4 v[144:147], v1, s[44:47], s77 offen sc1\n"
      "s_waitcnt vmcnt(11)\n"
      "v_mfma_f32_16x16x32_bf16 v[72:75], v[148:151], a[32:35], v[72:75]\n"
      "v_mfma_f32_16x16x32_bf16 v[76:79], v[148:151], a[36:39], v[76:79]\n"
      "v_mfma_f32_16x16x32_bf16 v[80:83], v[148:151], a[40:43], v[80:83]\n"
      "v_mfma_f32_16x16x32_bf16 v[84:87], v[148:151], a[44:47], v[84:87]\n"
      "s_add_u32 s77, s84, s95\n"
      "buffer_load_dwordx4 v[148:151], v1, s[44:47], s77 offen sc1\n"
      "s_waitcnt vmcnt(11)\n"
      "v_mfma_f32_16x16x32_bf16 v[72:75], v[152:155], a[48:51], v[72:75]\n"
      "v_mfma_f32_16x16x32_bf16 v[76:79], v[152:155], a[52:55], v[76:79]\n"
      "v_mfma_f32_16x16x32_bf16 v[80:83], v[152:155], a[56:59], v[80:83]\n"
      "v_mfma_f32_16x16x32_bf16 v[84:87], v[152:155], a[60:63], v[84:87]\n"
      "s_add_u32 s77, s84, s90\n"
      "buffer_load_dwordx4 v[152:155], v1, s[44:47], s77 offen sc1\n"
      "s_waitcnt vmcnt(11)\n"
      "v_mfma_f32_16x16x32_bf16 v[72:75], v[156:159], a[64:67], v[72:75]\n"
      "v_mfma_f32_16x16x32_bf16 v[76:79], v[156:159], a[68:71], v[76:79]\n"
      "v_mfma_f32_16x16x32_bf16 v[80:83], v[156:159], a[72:75], v[80:83]\n"
      "v_mfma_f32_16x16x32_bf16 v[84:87], v[156:159], a[76:79], v[84:87]\n"
      "s_add_u32 s77, s84, s23\n"
      "buffer_load_dwordx4 v[156:159], v1, s[44:47], s77 offen sc1\n"
      "s_waitcnt vmcnt(11)\n"
      "v_mfma_f32_16x16x32_bf16 v[72:75], v[160:163], a[80:83], v[72:75]\n"
      "v_mfma_f32_16x16x32_bf16 v[76:79], v[160:163], a[84:87], v[76:79]\n"
      "v_mfma_f32_16x16x32_bf16 v[80:83], v[160:163], a[88:91], v[80:83]\n"
      "v_mfma_f32_16x16x32_bf16 v[84:87], v[160:163], a[92:95], v[84:87]\n"
      "s_add_u32 s77, s84, s22\n"
      "buffer_load_dwordx4 v[160:163], v17, s[44:47], s77 offen sc1\n"
      "s_waitcnt vmcnt(11)\n"
      "v_mfma_f32_16x16x32_bf16 v[72:75], v[164:167], a[96:99], v[72:75]\n"
      "v_mfma_f32_16x16x32_bf16 v[76:79], v[164:167], a[100:103], v[76:79]\n"
      "v_mfma_f32_16x16x32_bf16 v[80:83], v[164:167], a[104:107], v[80:83]\n"
      "v_mfma_f32_16x16x32_bf16 v[84:87], v[164:167], a[108:111], v[84:87]\n"
      "s_add_u32 s77, s83, s16\n"
      "buffer_load_dwordx4 v[164:167], v1, s[44:47], s77 offen sc1\n"
      "s_waitcnt vmcnt(11)\n"
      "v_mfma_f32_16x16x32_bf16 v[56:59], v[168:171], a[192:195], 0\n"
      "v_mfma_f32_16x16x32_bf16 v[60:63], v[168:171], a[196:199], 0\n"
      "v_mfma_f32_16x16x32_bf16 v[64:67], v[168:171], a[200:203], 0\n"
      "v_mfma_f32_16x16x32_bf16 v[68:71], v[168:171], a[204:207], 0\n"
      "s_add_u32 s77, s83, s17\n"
      "buffer_load_dwordx4 v[168:171], v1, s[44:47], s77 offen sc1\n"
      "s_waitcnt vmcnt(11)\n"
      "v_mfma_f32_16x16x32_bf16 v[56:59], v[172:175], a[208:211], v[56:59]\n"
      "ds_write_b128 v45, v[72:75]\n"
      "v_mfma_f32_16x16x32_bf16 v[60:63], v[172:175], a[212:215], v[60:63]\n"
      "ds_write_b128 v45, v[76:79] offset:1024\n"
      "v_mfma_f32_16x16x32_bf16 v[64:67], v[172:175], a[216:219], v[64:67]\n"
      "ds_write_b128 v45, v[80:83] offset:2048\n"
      "v_mfma_f32_16x16x32_bf16 v[68:71], v[172:175], a[220:223], v[68:71]\n"
      "ds_write_b128 v45, v[84:87] offset:3072\n"
      "s_add_u32 s77, s83, s18\n"
      "buffer_load_dwordx4 v[172:175], v1, s[44:47], s77 offen sc1\n"
      "s_waitcnt vmcnt(11)\n"
      "v_mfma_f32_16x16x32_bf16 v[56:59], v[128:131], a[224:227], v[56:59]\n"
      "v_mfma_f32_16x16x32_bf16 v[60:63], v[128:131], a[228:231], v[60:63]\n"
      "v_mfma_f32_16x16x32_bf16 v[64:67], v[128:131], a[232:235], v[64:67]\n"
      "v_mfma_f32_16x16x32_bf16 v[68:71], v[128:131], a[236:239], v[68:71]\n"
      "s_add_u32 s77, s83, s19\n"
      "buffer_load_dwordx4 v[128:131], v1, s[44:47], s77 offen sc1\n"
      "s_waitcnt vmcnt(11)\n"
      "v_mfma_f32_16x16x32_bf16 v[56:59], v[132:135], a[240:243], v[56:59]\n"
      "v_mfma_f32_16x16x32_bf16 v[60:63], v[132:135], a[244:247], v[60:63]\n"
      "v_mfma_f32_16x16x32_bf16 v[64:67], v[132:135], a[248:251], v[64:67]\n"
      "v_mfma_f32_16x16x32_bf16 v[68:71], v[132:135], a[252:255], v[68:71]\n"
      "s_add_u32 s77, s83, s88\n"
      "buffer_load_dwordx4 v[132:135], v1, s[44:47], s77 offen sc1\n"
      "s_waitcnt vmcnt(11)\n"
      "v_mfma_f32_16x16x32_bf16 v[56:59], v[136:139], a[0:3], v[56:59]\n"
      "v_mfma_f32_16x16x32_bf16 v[60:63], v[136:139], a[4:7], v[60:63]\n"
      "v_mfma_f32_16x16x32_bf16 v[64:67], v[136:139], a[8:11], v[64:67]\n"
      "v_mfma_f32_16x16x32_bf16 v[68:71], v[136:139], a[12:15], v[68:71]\n"
      "s_add_u32 s77, s83, s87\n"
      "buffer_load_dwordx4 v[136:139], v1, s[44:47], s77 offen sc1\n"
      "s_waitcnt vmcnt(11)\n"
      "v_mfma_f32_16x16x32_bf16 v[56:59], v[140:143], a[16:19], v[56:59]\n"
      "v_mfma_f32_16x16x32_bf16 v[60:63], v[140:143], a[20:23], v[60:63]\n"
      "v_mfma_f32_16x16x32_bf16 v[64:67], v[140:143], a[24:27], v[64:67]\n"
      "v_mfma_f32_16x16x32_bf16 v[68:71], v[140:143], a[28:31], v[68:71]\n"
      "s_add_u32 s77, s83, s13\n"
      "buffer_load_dwordx4 v[140:143], v1, s[44:47], s77 offen sc1\n"
      "s_waitcnt vmcnt(11)\n"
      "v_mfma_f32_16x16x32_bf16 v[56:59], v[144:147], a[32:35], v[56:59]\n"
      "v_mfma_f32_16x16x32_bf16 v[60:63], v[144:147], a[36:39], v[60:63]\n"
      "v_mfma_f32_16x16x32_bf16 v[64:67], v[144:147], a[40:43], v[64:67]\n"
      "v_mfma_f32_16x16x32_bf16 v[68:71], v[144:147], a[44:47], v[68:71]\n"
      "s_add_u32 s77, s83, s95\n"
      "buffer_load_dwordx4 v[144:147], v1, s[44:47], s77 offen sc1\n"
      "s_waitcnt vmcnt(11)\n"
      "v_mfma_f32_16x16x32_bf16 v[56:59], v[148:151], a[48:51], v[56:59]\n"
      "v_mfma_f32_16x16x32_bf16 v[60:63], v[148:151], a[52:55], v[60:63]\n"
      "v_mfma_f32_16x16x32_bf16 v[64:67], v[148:151], a[56:59], v[64:67]\n"
      "v_mfma_f32_16x16x32_bf16 v[68:71], v[148:151], a[60:63], v[68:71]\n"
      "s_add_u32 s77, s83, s90\n"
      "buffer_load_dwordx4 v[148:151], v1, s[44:47], s77 offen sc1\n"
      "s_waitcnt vmcnt(11)\n"
      "v_mfma_f32_16x16x32_bf16 v[56:59], v[152:155], a[64:67], v[56:59]\n"
      "v_mfma_f32_16x16x32_bf16 v[60:63], v[152:155], a[68:71], v[60:63]\n"
      "v_mfma_f32_16x16x32_bf16 v[64:67], v[152:155], a[72:75], v[64:67]\n"
      "v_mfma_f32_16x16x32_bf16 v[68:71], v[152:155], a[76:79], v[68:71]\n"
      "s_add_u32 s77, s83, s23\n"
      "buffer_load_dwordx4 v[152:155], v1, s[44:47], s77 offen sc1\n"
      "s_waitcnt vmcnt(11)\n"
      "v_mfma_f32_16x16x32_bf16 v[56:59], v[156:159], a[80:83], v[56:59]\n"
      "v_mfma_f32_16x16x32_bf16 v[60:63], v[156:159], a[84:87], v[60:63]\n"
      "v_mfma_f32_16x16x32_bf16 v[64:67], v[156:159], a[88:91], v[64:67]\n"
      "v_mfma_f32_16x16x32_bf16 v[68:71], v[156:159], a[92:95], v[68:71]\n"
      "s_add_u32 s77, s83, s22\n"
      "buffer_load_dwordx4 v[156:159], v17, s[44:47], s77 offen sc1\n"
      "s_waitcnt vmcnt(11)\n"
      "v_mfma_f32_16x16x32_bf16 v[56:59], v[160:163], a[96:99], v[56:59]\n"
      "v_mfma_f32_16x16x32_bf16 v[60:63], v[160:163], a[100:103], v[60:63]\n"
      "v_mfma_f32_16x16x32_bf16 v[64:67], v[160:163], a[104:107], v[64:67]\n"
      "v_mfma_f32_16x16x32_bf16 v[68:71], v[160:163], a[108:111], v[68:71]\n"
      "s_waitcnt vmcnt(10)\n"
      "v_mfma_f32_16x16x32_bf16 v[72:75], v[164:167], a[192:195], 0\n"
      "v_mfma_f32_16x16x32_bf16 v[76:79], v[164:167], a[196:199], 0\n"
      "v_mfma_f32_16x16x32_bf16 v[80:83], v[164:167], a[200:203], 0\n"
      "v_mfma_f32_16x16x32_bf16 v[84:87], v[164:167], a[204:207], 0\n"
      "s_waitcnt vmcnt(9)\n"
      "v_mfma_f32_16x16x32_bf16 v[72:75], v[168:171], a[208:211], v[72:75]\n"
      "ds_write_b128 v46, v[56:59]\n"
      "v_mfma_f32_16x16x32_bf16 v[76:79], v[168:171], a[212:215], v[76:79]\n"
      "ds_write_b128 v46, v[60:63] offset:1024\n"
      "v_mfma_f32_16x16x32_bf16 v[80:83], v[168:171], a[216:219], v[80:83]\n"
      "ds_write_b128 v46, v[64:67] offset:2048\n"
      "v_mfma_f32_16x16x32_bf16 v[84:87], v[168:171], a[220:223], v[84:87]\n"
      "ds_write_b128 v46, v[68:71] offset:3072\n"
      "s_waitcnt vmcnt(8)\n"
      "v_mfma_f32_16x16x32_bf16 v[72:75], v[172:175], a[224:227], v[72:75]\n"
      "v_mfma_f32_16x16x32_bf16 v[76:79], v[172:175], a[228:231], v[76:79]\n"
      "v_mfma_f32_16x16x32_bf16 v[80:83], v[172:175], a[232:235], v[80:83]\n"
      "v_mfma_f32_16x16x32_bf16 v[84:87], v[172:175], a[236:239], v[84:87]\n"
      "s_waitcnt vmcnt(7)\n"
      "v_mfma_f32_16x16x32_bf16 v[72:75], v[128:131], a[240:243], v[72:75]\n"
      "v_mfma_f32_16x16x32_bf16 v[76:79], v[128:131], a[244:247], v[76:79]\n"
      "v_mfma_f32_16x16x32_bf16 v[80:83], v[128:131], a[248:251], v[80:83]\n"
      "v_mfma_f32_16x16x32_bf16 v[84:87], v[128:131], a[252:255], v[84:87]\n"
      "s_waitcnt lgkmcnt(0)\n"
      "s_barrier\n"
      "ds_read_b128 v[224:227], v7\n"
      "ds_read_b128 v[228:231], v7 offset:1024\n"
      "ds_read_b128 v[232:235], v7 offset:2048\n"
      "ds_read_b128 v[236:239], v7 offset:3072\n"
      "ds_read_b128 v[240:243], v7 offset:4096\n"
      "ds_read_b128 v[244:247], v7 offset:5120\n"
      "ds_read_b128 v[248:251], v7 offset:6144\n"
      "ds_read_b128 v[252:255], v7 offset:7168\n"
      "ds_read_b128 v[88:91], v7 offset:8192\n"
      "ds_read_b128 v[92:95], v7 offset:9216\n"
      "ds_read_b128 v[96:99], v7 offset:10240\n"
      "ds_read_b128 v[100:103], v7 offset:11264\n"
      "s_waitcnt vmcnt(6)\n"
      "v_mfma_f32_16x16x32_bf16 v[72:75], v[132:135], a[0:3], v[72:75]\n"
      "v_mfma_f32_16x16x32_bf16 v[76:79], v[132:135], a[4:7], v[76:79]\n"
      "v_mfma_f32_16x16x32_bf16 v[80:83], v[132:135], a[8:11], v[80:83]\n"
      "v_mfma_f32_16x16x32_bf16 v[84:87], v[132:135], a[12:15], v[84:87]\n"
      "s_waitcnt vmcnt(5)\n"
      "v_mfma_f32_16x16x32_bf16 v[72:75], v[136:139], a[16:19], v[72:75]\n"
      "v_mfma_f32_16x16x32_bf16 v[76:79], v[136:139], a[20:23], v[76:79]\n"
      "v_mfma_f32_16x16x32_bf16 v[80:83], v[136:139], a[24:27], v[80:83]\n"
      "v_mfma_f32_16x16x32_bf16 v[84:87], v[136:139], a[28:31], v[84:87]\n"
      "s_waitcnt vmcnt(4)\n"
      "v_mfma_f32_16x16x32_bf16 v[72:75], v[140:143], a[32:35], v[72:75]\n"
      "v_mfma_f32_16x16x32_bf16 v[76:79], v[140:143], a[36:39], v[76:79]\n"
      "v_mfma_f32_16x16x32_bf16 v[80:83], v[140:143], a[40:43], v[80:83]\n"
      "v_mfma_f32_16x16x32_bf16 v[84:87], v[140:143], a[44:47], v[84:87]\n"
      "s_waitcnt vmcnt(3)\n"
      "v_mfma_f32_16x16x32_bf16 v[72:75], v[144:147], a[48:51], v[72:75]\n"
      "v_mfma_f32_16x16x32_bf16 v[76:79], v[144:147], a[52:55], v[76:79]\n"
      "v_mfma_f32_16x16x32_bf16 v[80:83], v[144:147], a[56:59], v[80:83]\n"
      "v_mfma_f32_16x16x32_bf16 v[84:87], v[144:147], a[60:63], v[84:87]\n"
      "s_waitcnt vmcnt(2)\n"
      "v_mfma_f32_16x16x32_bf16 v[72:75], v[148:151], a[64:67], v[72:75]\n"
      "v_mfma_f32_16x16x32_bf16 v[76:79], v[148:151], a[68:71], v[76:79]\n"
      "v_mfma_f32_16x16x32_bf16 v[80:83], v[148:151], a[72:75], v[80:83]\n"
      "v_mfma_f32_16x16x32_bf16 v[84:87], v[148:151], a[76:79], v[84:87]\n"
      "s_waitcnt vmcnt(1)\n"
      "v_mfma_f32_16x16x32_bf16 v[72:75], v[152:155], a[80:83], v[72:75]\n"
      "v_mfma_f32_16x16x32_bf16 v[76:79], v[152:155], a[84:87], v[76:79]\n"
      "v_mfma_f32_16x16x32_bf16 v[80:83], v[152:155], a[88:91], v[80:83]\n"
      "v_mfma_f32_16x16x32_bf16 v[84:87], v[152:155], a[92:95], v[84:87]\n"
      "s_waitcnt vmcnt(0)\n"
      "v_mfma_f32_16x16x32_bf16 v[72:75], v[156:159], a[96:99], v[72:75]\n"
      "v_mfma_f32_16x16x32_bf16 v[76:79], v[156:159], a[100:103], v[76:79]\n"
      "v_mfma_f32_16x16x32_bf16 v[80:83], v[156:159], a[104:107], v[80:83]\n"
      "v_mfma_f32_16x16x32_bf16 v[84:87], v[156:159], a[108:111], v[84:87]\n"
      "s_waitcnt lgkmcnt(11)\n"
      "v_pk_add_f32 v[104:105], v[104:105], v[224:225]\n"
      "v_pk_add_f32 v[106:107], v[106:107], v[226:227]\n"
      "s_waitcnt lgkmcnt(10)\n"
      "v_pk_add_f32 v[108:109], v[108:109], v[228:229]\n"
      "v_pk_add_f32 v[110:111], v[110:111], v[230:231]\n"
      "s_waitcnt lgkmcnt(9)\n"
      "v_pk_add_f32 v[112:113], v[112:113], v[232:233]\n"
      "v_pk_add_f32 v[114:115], v[114:115], v[234:235]\n"
      "s_waitcnt lgkmcnt(8)\n"
      "v_pk_add_f32 v[116:117], v[116:117], v[236:237]\n"
      "v_pk_add_f32 v[118:119], v[118:119], v[238:239]\n"
      "s_waitcnt lgkmcnt(7)\n"
      "v_pk_add_f32 v[104:105], v[104:105], v[240:241]\n"
      "v_pk_add_f32 v[106:107], v[106:107], v[242:243]\n"
      "s_waitcnt lgkmcnt(6)\n"
      "v_pk_add_f32 v[108:109], v[108:109], v[244:245]\n"
      "v_pk_add_f32 v[110:111], v[110:111], v[246:247]\n"
      "s_waitcnt lgkmcnt(5)\n"
      "v_pk_add_f32 v[112:113], v[112:113], v[248:249]\n"
      "v_pk_add_f32 v[114:115], v[114:115], v[250:251]\n"
      "s_waitcnt lgkmcnt(4)\n"
      "v_pk_add_f32 v[116:117], v[116:117], v[252:253]\n"
      "v_pk_add_f32 v[118:119], v[118:119], v[254:255]\n"
      "s_waitcnt lgkmcnt(3)\n"
      "v_pk_add_f32 v[104:105], v[104:105], v[88:89]\n"
      "v_pk_add_f32 v[106:107], v[106:107], v[90:91]\n"
      "s_waitcnt lgkmcnt(2)\n"
      "v_pk_add_f32 v[108:109], v[108:109], v[92:93]\n"
      "v_pk_add_f32 v[110:111], v[110:111], v[94:95]\n"
      "s_waitcnt lgkmcnt(1)\n"
      "v_pk_add_f32 v[112:113], v[112:113], v[96:97]\n"
      "v_pk_add_f32 v[114:115], v[114:115], v[98:99]\n"
      "s_waitcnt lgkmcnt(0)\n"
      "v_pk_add_f32 v[116:117], v[116:117], v[100:101]\n"
      "v_pk_add_f32 v[118:119], v[118:119], v[102:103]\n"
      "v_pk_add_f32 v[104:105], v[104:105], v[72:73]\n"
      "v_pk_add_f32 v[106:107], v[106:107], v[74:75]\n"
      "v_cvt_pk_bf16_f32 v128, v104, v105\n"
      "v_cvt_pk_bf16_f32 v129, v106, v107\n"
      "ds_write_b64 v52, v[128:129]\n"
      "v_pk_add_f32 v[108:109], v[108:109], v[76:77]\n"
      "v_pk_add_f32 v[110:111], v[110:111], v[78:79]\n"
      "v_cvt_pk_bf16_f32 v130, v108, v109\n"
      "v_cvt_pk_bf16_f32 v131, v110, v111\n"
      "ds_write_b64 v52, v[130:131] offset:2048\n"
      "v_pk_add_f32 v[112:113], v[112:113], v[80:81]\n"
      "v_pk_add_f32 v[114:115], v[114:115], v[82:83]\n"
      "v_cvt_pk_bf16_f32 v132, v112, v113\n"
      "v_cvt_pk_bf16_f32 v133, v114, v115\n"
      "ds_write_b64 v52, v[132:133] offset:4096\n"
      "v_pk_add_f32 v[116:117], v[116:117], v[84:85]\n"
      "v_pk_add_f32 v[118:119], v[118:119], v[86:87]\n"
      "v_cvt_pk_bf16_f32 v134, v116, v117\n"
      "v_cvt_pk_bf16_f32 v135, v118, v119\n"
      "ds_write_b64 v52, v[134:135] offset:6144\n"
      "s_waitcnt lgkmcnt(0)\n"
      "s_barrier\n"
      "ds_read_b128 v[136:139], v53\n"
      "ds_read_b128 v[140:143], v53 offset:1024\n"
      "s_waitcnt lgkmcnt(1)\n"
      "buffer_store_dwordx4 v[136:139], v50, s[48:51], s74 offen\n"
      "s_waitcnt lgkmcnt(0)\n"
      "buffer_store_dwordx4 v[140:143], v50, s[48:51], s75 offen\n"
      ".Lend_all_%=:\n"
      "s_endpgm\n"
      ".Lfallback_13_%=:\n"
      "s_waitcnt vmcnt(0)\n"
      "s_nop 15\n"
      "s_add_u32 s75, s97, 0\n"
      "s_sub_i32 s74, 8, s75\n"
      "s_max_i32 s74, s74, 0\n"
      "v_mov_b32 v20, 4286578688\n"
      "s_mov_b32 s73, s74\n"
      "s_add_u32 s75, s74, 0\n"
      "s_lshl_b32 s98, s75, 10\n"
      "v_add_u32 v21, s98, v49\n"
      "s_mul_i32 s98, s75, 1056\n"
      "v_add_u32 v22, s98, v50\n"
      "v_add_u32 v23, s98, v51\n"
      "s_nop 2\n"
      ".Lfl_27_%=:\n"
      "ds_read_b128 v[232:235], v21\n"
      "s_waitcnt lgkmcnt(0)\n"
      "s_cmp_eq_u32 s73, 0\n"
      "s_cbranch_scc1 .Lffar_28_%=\n"
      "s_cmp_eq_u32 s73, 8\n"
      "s_cbranch_scc1 .Lfdiag_29_%=\n"
      "v_mfma_f32_32x32x16_bf16 v[160:175], v[232:235], v[224:227], 0\n"
      "s_nop 11\n"
      "s_branch .Lfj_30_%=\n"
      ".Lffar_28_%=:\n"
      "v_mfma_f32_32x32x16_bf16 v[160:175], v[232:235], v[224:227], v[128:143]\n"
      "s_nop 11\n"
      "s_branch .Lfj_30_%=\n"
      ".Lfdiag_29_%=:\n"
      "v_mfma_f32_32x32x16_bf16 v[160:175], v[232:235], v[224:227], v[144:159]\n"
      "s_nop 11\n"
      ".Lfj_30_%=:\n"
      "v_max3_f32 v20, v20, v160, v161\n"
      "v_max3_f32 v20, v20, v162, v163\n"
      "v_max3_f32 v20, v20, v164, v165\n"
      "v_max3_f32 v20, v20, v166, v167\n"
      "v_max3_f32 v20, v20, v168, v169\n"
      "v_max3_f32 v20, v20, v170, v171\n"
      "v_max3_f32 v20, v20, v172, v173\n"
      "v_max3_f32 v20, v20, v174, v175\n"
      "v_add_u32 v21, 1024, v21\n"
      "v_add_u32 v22, 1056, v22\n"
      "v_add_u32 v23, 1056, v23\n"
      "s_add_u32 s73, s73, 1\n"
      "s_cmp_le_u32 s73, 8\n"
      "s_nop 0\n"
      "s_cbranch_scc1 .Lfl_27_%=\n"
      "v_mov_b32 v72, v20\n"
      "s_nop 1\n"
      "v_permlane32_swap_b32 v20, v72\n"
      "v_max_f32 v20, v20, v72\n"
      "v_mov_b32 v192, 0\n"
      "v_mov_b32 v193, 0\n"
      "v_mov_b32 v194, 0\n"
      "v_mov_b32 v195, 0\n"
      "v_mov_b32 v196, 0\n"
      "v_mov_b32 v197, 0\n"
      "v_mov_b32 v198, 0\n"
      "v_mov_b32 v199, 0\n"
      "v_mov_b32 v200, 0\n"
      "v_mov_b32 v201, 0\n"
      "v_mov_b32 v202, 0\n"
      "v_mov_b32 v203, 0\n"
      "v_mov_b32 v204, 0\n"
      "v_mov_b32 v205, 0\n"
      "v_mov_b32 v206, 0\n"
      "v_mov_b32 v207, 0\n"
      "s_mov_b32 s73, s74\n"
      "s_add_u32 s75, s74, 0\n"
      "s_lshl_b32 s98, s75, 10\n"
      "v_add_u32 v21, s98, v49\n"
      "s_mul_i32 s98, s75, 1056\n"
      "v_add_u32 v22, s98, v50\n"
      "v_add_u32 v23, s98, v51\n"
      "s_nop 2\n"
      ".Lfl_31_%=:\n"
      "ds_read_b128 v[232:235], v21\n"
      "ds_read_b128 v[56:59], v22\n"
      "ds_read_b128 v[60:63], v23\n"
      "s_waitcnt lgkmcnt(0)\n"
      "s_cmp_eq_u32 s73, 0\n"
      "s_cbranch_scc1 .Lffar_32_%=\n"
      "s_cmp_eq_u32 s73, 8\n"
      "s_cbranch_scc1 .Lfdiag_33_%=\n"
      "v_mfma_f32_32x32x16_bf16 v[160:175], v[232:235], v[224:227], 0\n"
      "s_nop 11\n"
      "s_branch .Lfj_34_%=\n"
      ".Lffar_32_%=:\n"
      "v_mfma_f32_32x32x16_bf16 v[160:175], v[232:235], v[224:227], v[128:143]\n"
      "s_nop 11\n"
      "s_branch .Lfj_34_%=\n"
      ".Lfdiag_33_%=:\n"
      "v_mfma_f32_32x32x16_bf16 v[160:175], v[232:235], v[224:227], v[144:159]\n"
      "s_nop 11\n"
      ".Lfj_34_%=:\n"
      "v_sub_f32 v160, v160, v20\n"
      "v_sub_f32 v161, v161, v20\n"
      "v_sub_f32 v162, v162, v20\n"
      "v_sub_f32 v163, v163, v20\n"
      "v_sub_f32 v164, v164, v20\n"
      "v_sub_f32 v165, v165, v20\n"
      "v_sub_f32 v166, v166, v20\n"
      "v_sub_f32 v167, v167, v20\n"
      "v_sub_f32 v168, v168, v20\n"
      "v_sub_f32 v169, v169, v20\n"
      "v_sub_f32 v170, v170, v20\n"
      "v_sub_f32 v171, v171, v20\n"
      "v_sub_f32 v172, v172, v20\n"
      "v_sub_f32 v173, v173, v20\n"
      "v_sub_f32 v174, v174, v20\n"
      "v_sub_f32 v175, v175, v20\n"
      "v_exp_f32 v160, v160\n"
      "v_exp_f32 v161, v161\n"
      "v_exp_f32 v162, v162\n"
      "v_exp_f32 v163, v163\n"
      "v_exp_f32 v164, v164\n"
      "v_exp_f32 v165, v165\n"
      "v_exp_f32 v166, v166\n"
      "v_exp_f32 v167, v167\n"
      "v_exp_f32 v168, v168\n"
      "v_exp_f32 v169, v169\n"
      "v_exp_f32 v170, v170\n"
      "v_exp_f32 v171, v171\n"
      "v_exp_f32 v172, v172\n"
      "v_exp_f32 v173, v173\n"
      "v_exp_f32 v174, v174\n"
      "v_exp_f32 v175, v175\n"
      "v_cvt_pk_bf16_f32 v244, v160, v161\n"
      "v_cvt_pk_bf16_f32 v245, v162, v163\n"
      "v_cvt_pk_bf16_f32 v246, v164, v165\n"
      "v_cvt_pk_bf16_f32 v247, v166, v167\n"
      "v_cvt_pk_bf16_f32 v248, v168, v169\n"
      "v_cvt_pk_bf16_f32 v249, v170, v171\n"
      "v_cvt_pk_bf16_f32 v250, v172, v173\n"
      "v_cvt_pk_bf16_f32 v251, v174, v175\n"
      "v_mfma_f32_32x32x16_bf16 v[192:207], v[56:59], v[244:247], v[192:207]\n"
      "s_nop 0\n"
      "v_mfma_f32_32x32x16_bf16 v[192:207], v[60:63], v[248:251], v[192:207]\n"
      "v_add_u32 v21, 1024, v21\n"
      "v_add_u32 v22, 1056, v22\n"
      "v_add_u32 v23, 1056, v23\n"
      "s_add_u32 s73, s73, 1\n"
      "s_cmp_le_u32 s73, 8\n"
      "s_nop 6\n"
      "s_cbranch_scc1 .Lfl_31_%=\n"
      "s_add_u32 s75, s97, 1\n"
      "s_sub_i32 s74, 8, s75\n"
      "s_max_i32 s74, s74, 0\n"
      "v_mov_b32 v20, 4286578688\n"
      "s_mov_b32 s73, s74\n"
      "s_add_u32 s75, s74, 1\n"
      "s_lshl_b32 s98, s75, 10\n"
      "v_add_u32 v21, s98, v49\n"
      "s_mul_i32 s98, s75, 1056\n"
      "v_add_u32 v22, s98, v50\n"
      "v_add_u32 v23, s98, v51\n"
      "s_nop 2\n"
      ".Lfl_35_%=:\n"
      "ds_read_b128 v[232:235], v21\n"
      "s_waitcnt lgkmcnt(0)\n"
      "s_cmp_eq_u32 s73, 0\n"
      "s_cbranch_scc1 .Lffar_36_%=\n"
      "s_cmp_eq_u32 s73, 8\n"
      "s_cbranch_scc1 .Lfdiag_37_%=\n"
      "v_mfma_f32_32x32x16_bf16 v[160:175], v[232:235], v[228:231], 0\n"
      "s_nop 11\n"
      "s_branch .Lfj_38_%=\n"
      ".Lffar_36_%=:\n"
      "v_mfma_f32_32x32x16_bf16 v[160:175], v[232:235], v[228:231], v[128:143]\n"
      "s_nop 11\n"
      "s_branch .Lfj_38_%=\n"
      ".Lfdiag_37_%=:\n"
      "v_mfma_f32_32x32x16_bf16 v[160:175], v[232:235], v[228:231], v[144:159]\n"
      "s_nop 11\n"
      ".Lfj_38_%=:\n"
      "v_max3_f32 v20, v20, v160, v161\n"
      "v_max3_f32 v20, v20, v162, v163\n"
      "v_max3_f32 v20, v20, v164, v165\n"
      "v_max3_f32 v20, v20, v166, v167\n"
      "v_max3_f32 v20, v20, v168, v169\n"
      "v_max3_f32 v20, v20, v170, v171\n"
      "v_max3_f32 v20, v20, v172, v173\n"
      "v_max3_f32 v20, v20, v174, v175\n"
      "v_add_u32 v21, 1024, v21\n"
      "v_add_u32 v22, 1056, v22\n"
      "v_add_u32 v23, 1056, v23\n"
      "s_add_u32 s73, s73, 1\n"
      "s_cmp_le_u32 s73, 8\n"
      "s_nop 0\n"
      "s_cbranch_scc1 .Lfl_35_%=\n"
      "v_mov_b32 v72, v20\n"
      "s_nop 1\n"
      "v_permlane32_swap_b32 v20, v72\n"
      "v_max_f32 v20, v20, v72\n"
      "v_mov_b32 v208, 0\n"
      "v_mov_b32 v209, 0\n"
      "v_mov_b32 v210, 0\n"
      "v_mov_b32 v211, 0\n"
      "v_mov_b32 v212, 0\n"
      "v_mov_b32 v213, 0\n"
      "v_mov_b32 v214, 0\n"
      "v_mov_b32 v215, 0\n"
      "v_mov_b32 v216, 0\n"
      "v_mov_b32 v217, 0\n"
      "v_mov_b32 v218, 0\n"
      "v_mov_b32 v219, 0\n"
      "v_mov_b32 v220, 0\n"
      "v_mov_b32 v221, 0\n"
      "v_mov_b32 v222, 0\n"
      "v_mov_b32 v223, 0\n"
      "s_mov_b32 s73, s74\n"
      "s_add_u32 s75, s74, 1\n"
      "s_lshl_b32 s98, s75, 10\n"
      "v_add_u32 v21, s98, v49\n"
      "s_mul_i32 s98, s75, 1056\n"
      "v_add_u32 v22, s98, v50\n"
      "v_add_u32 v23, s98, v51\n"
      "s_nop 2\n"
      ".Lfl_39_%=:\n"
      "ds_read_b128 v[232:235], v21\n"
      "ds_read_b128 v[56:59], v22\n"
      "ds_read_b128 v[60:63], v23\n"
      "s_waitcnt lgkmcnt(0)\n"
      "s_cmp_eq_u32 s73, 0\n"
      "s_cbranch_scc1 .Lffar_40_%=\n"
      "s_cmp_eq_u32 s73, 8\n"
      "s_cbranch_scc1 .Lfdiag_41_%=\n"
      "v_mfma_f32_32x32x16_bf16 v[160:175], v[232:235], v[228:231], 0\n"
      "s_nop 11\n"
      "s_branch .Lfj_42_%=\n"
      ".Lffar_40_%=:\n"
      "v_mfma_f32_32x32x16_bf16 v[160:175], v[232:235], v[228:231], v[128:143]\n"
      "s_nop 11\n"
      "s_branch .Lfj_42_%=\n"
      ".Lfdiag_41_%=:\n"
      "v_mfma_f32_32x32x16_bf16 v[160:175], v[232:235], v[228:231], v[144:159]\n"
      "s_nop 11\n"
      ".Lfj_42_%=:\n"
      "v_sub_f32 v160, v160, v20\n"
      "v_sub_f32 v161, v161, v20\n"
      "v_sub_f32 v162, v162, v20\n"
      "v_sub_f32 v163, v163, v20\n"
      "v_sub_f32 v164, v164, v20\n"
      "v_sub_f32 v165, v165, v20\n"
      "v_sub_f32 v166, v166, v20\n"
      "v_sub_f32 v167, v167, v20\n"
      "v_sub_f32 v168, v168, v20\n"
      "v_sub_f32 v169, v169, v20\n"
      "v_sub_f32 v170, v170, v20\n"
      "v_sub_f32 v171, v171, v20\n"
      "v_sub_f32 v172, v172, v20\n"
      "v_sub_f32 v173, v173, v20\n"
      "v_sub_f32 v174, v174, v20\n"
      "v_sub_f32 v175, v175, v20\n"
      "v_exp_f32 v160, v160\n"
      "v_exp_f32 v161, v161\n"
      "v_exp_f32 v162, v162\n"
      "v_exp_f32 v163, v163\n"
      "v_exp_f32 v164, v164\n"
      "v_exp_f32 v165, v165\n"
      "v_exp_f32 v166, v166\n"
      "v_exp_f32 v167, v167\n"
      "v_exp_f32 v168, v168\n"
      "v_exp_f32 v169, v169\n"
      "v_exp_f32 v170, v170\n"
      "v_exp_f32 v171, v171\n"
      "v_exp_f32 v172, v172\n"
      "v_exp_f32 v173, v173\n"
      "v_exp_f32 v174, v174\n"
      "v_exp_f32 v175, v175\n"
      "v_cvt_pk_bf16_f32 v244, v160, v161\n"
      "v_cvt_pk_bf16_f32 v245, v162, v163\n"
      "v_cvt_pk_bf16_f32 v246, v164, v165\n"
      "v_cvt_pk_bf16_f32 v247, v166, v167\n"
      "v_cvt_pk_bf16_f32 v248, v168, v169\n"
      "v_cvt_pk_bf16_f32 v249, v170, v171\n"
      "v_cvt_pk_bf16_f32 v250, v172, v173\n"
      "v_cvt_pk_bf16_f32 v251, v174, v175\n"
      "v_mfma_f32_32x32x16_bf16 v[208:223], v[56:59], v[244:247], v[208:223]\n"
      "s_nop 0\n"
      "v_mfma_f32_32x32x16_bf16 v[208:223], v[60:63], v[248:251], v[208:223]\n"
      "v_add_u32 v21, 1024, v21\n"
      "v_add_u32 v22, 1056, v22\n"
      "v_add_u32 v23, 1056, v23\n"
      "s_add_u32 s73, s73, 1\n"
      "s_cmp_le_u32 s73, 8\n"
      "s_nop 6\n"
      "s_cbranch_scc1 .Lfl_39_%=\n"
      "v_rcp_f32 v20, v200\n"
      "v_rcp_f32 v22, v216\n"
      "s_nop 2\n"
      "s_branch .Lattn_epi_14_%=\n"
      ".Lcode_end_%=:\n"
      : [tid] "+v"(tid)
      : [karg] "s"(__builtin_amdgcn_kernarg_segment_ptr()), [wg] "s"(blockIdx.x), "s"((unsigned)(size_t)(__attribute__((address_space(3))) char*)lds)
      : "memory", "vcc", "scc", CLOB_V, CLOB_A, CLOB_S);
}

extern "C" void kernel_launch(void* const* d_in, const int* in_sizes, int n_in, void* d_out, int out_size, void* d_ws, size_t ws_size,
                              hipStream_t stream) {
  fused_block_kernel<<<256, 256, 0, stream>>>(d_in[0], d_in[1], d_in[2], d_in[4], d_in[5], d_in[6], d_in[7], d_out, d_ws);
}
```
